```python
import torch

def _softmax(x, axis=-1):
    return torch.softmax(x, dim=axis)
def _gelu(x):
    return torch.nn.functional.gelu(x, approximate="tanh")
def _silu(x):
    return torch.nn.functional.silu(x)
def _sigmoid(x):
    return torch.sigmoid(x)
def _layernorm(x, axis=-1, eps=1e-5):
    m = x.mean(dim=axis, keepdim=True)
    v = x.var(dim=axis, keepdim=True, unbiased=False)
    return (x - m) / torch.sqrt(v + eps)
def _scatter_add(tgt, idx, src, axis=0):
    return tgt.clone().index_add_(axis, idx, src)
def _segment_sum(src, seg, n):
    out = torch.zeros((n,) + src.shape[1:], dtype=src.dtype)
    return out.index_add_(0, seg, src)
def _pad(x, pw):
    flat = tuple(v for pair in reversed(pw) for v in pair)
    return torch.nn.functional.pad(x, flat)

kernel_name = 'recurrent_scan__gqa_block_cached__glu_gated_glu__decode'

def setup_inputs():

    _w = (lambda a: (lambda t: t.to(torch.bfloat16) if t.is_floating_point() else t)(torch.as_tensor(a)))
    def _A_setup():
        B, Sq, D = 8, 1, 1024
        Hq, Hkv, Sk, Dh = 32, 4, 4096, 128
        assert Hq % Hkv == 0, (Hq, Hkv)
        return {
            "x": _w((lambda *s: torch.randn(*s, dtype=torch.bfloat16) * 0.05)(B, Sq, D)),
            "k_cache": _w((lambda *s: torch.randn(*s, dtype=torch.bfloat16) * 1.6)(B, Hkv, Sk, Dh)),
            "v_cache": _w((lambda *s: torch.randn(*s, dtype=torch.bfloat16) * 0.2)(B, Hkv, Sk, Dh)),
            "wq": _w((lambda *s: torch.randn(*s, dtype=torch.bfloat16) * 1.6)(D, Hq * Dh) * (D ** -0.5)),
            "wo": _w((lambda *s: torch.randn(*s, dtype=torch.bfloat16) * 0.2)(Hq * Dh, D) * ((Hq * Dh) ** -0.5)),
        }
    _A_in = _A_setup()

    def _B_setup():
        B, S, D, F = 8, 1, 1024, 3584
        return {
            "x": _w((lambda *s: torch.randn(*s, dtype=torch.bfloat16) * 0.05)(B, S, D)),
            "w_gate": _w((lambda *s: torch.randn(*s, dtype=torch.bfloat16) * 0.2)(D, F)),
            "w_up": _w((lambda *s: torch.randn(*s, dtype=torch.bfloat16) * 0.1)(D, F)),
            "w_down": _w((lambda *s: torch.randn(*s, dtype=torch.bfloat16) * 0.1)(F, D)),
        }
    _B_in = _B_setup()

    return {
        "A_x": _A_in["x"],
        "A_k_cache": _A_in["k_cache"],
        "A_v_cache": _A_in["v_cache"],
        "A_wq": _A_in["wq"],
        "A_wo": _A_in["wo"],
        "B_x": _B_in["x"],
        "B_w_gate": _B_in["w_gate"],
        "B_w_up": _B_in["w_up"],
        "B_w_down": _B_in["w_down"],
    }

def reference(A_x, A_k_cache, A_v_cache, A_wq, A_wo, B_x, B_w_gate, B_w_up, B_w_down):
    def _A_ref(x, k_cache, v_cache, wq, wo):
        B, Sq, D = x.shape
        Hq, Dh = 32, 128
        Hkv = k_cache.shape[1]
        rep = Hq // Hkv
        q = (lambda x, *d: x.permute(*d))((x @ wq).reshape(B, Sq, Hq, Dh), 0, 2, 1, 3)
        k = (lambda x, n, axis: torch.repeat_interleave(x, n, dim=axis))(k_cache, rep, axis=1)
        v = (lambda x, n, axis: torch.repeat_interleave(x, n, dim=axis))(v_cache, rep, axis=1)
        scores = torch.einsum("bhqd,bhkd->bhqk", q, k) * (Dh ** -0.5)
        p = _softmax(scores, axis=-1)
        o = torch.einsum("bhqk,bhkd->bhqd", p, v)
        return (lambda x, *d: x.permute(*d))(o, 0, 2, 1, 3).reshape(B, Sq, Hq * Dh) @ wo

    def _B_ref(x, w_gate, w_up, w_down):
        g = torch.einsum("bsd,df->bsf", x, w_gate)
        u = torch.einsum("bsd,df->bsf", x, w_up)
        h = _sigmoid(g) * u
        return torch.einsum("bsf,fd->bsd", h, w_down)

    x = A_x
    xn = _layernorm(x, axis=-1)
    s = _A_ref(xn, A_k_cache, A_v_cache, A_wq, A_wo)
    g = _silu(_B_ref(xn, B_w_gate, B_w_up, B_w_down))
    return x + s * g
```

```cpp
#include <hip/hip_runtime.h>
#include <hip/hip_bf16.h>
#include <stdint.h>

typedef unsigned int u32;
typedef unsigned short u16;
typedef u32 u32x4 __attribute__((ext_vector_type(4)));
typedef u32 u32x2 __attribute__((ext_vector_type(2)));
typedef float f32x4 __attribute__((ext_vector_type(4)));
typedef __bf16 bf16x8 __attribute__((ext_vector_type(8)));

constexpr int NB = 8, DM = 1024, HQ = 32, HKV = 4, SK = 4096, DH = 128, FF = 3584;
constexpr int NWG = 256, NT = 512;
constexpr int XN_STRIDE = DM + 8;

__device__ __forceinline__ f32x4 mfma16(u32x4 a, u32x4 b, f32x4 c) {
  return __builtin_amdgcn_mfma_f32_16x16x32_bf16(__builtin_bit_cast(bf16x8, a), __builtin_bit_cast(bf16x8, b), c, 0, 0, 0);
}
__device__ __forceinline__ float bf_lo(u32 x) { return __builtin_bit_cast(float, x << 16); }
__device__ __forceinline__ float bf_hi(u32 x) { return __builtin_bit_cast(float, x & 0xffff0000u); }
__device__ __forceinline__ u32 pack_bf(float lo, float hi) {
  typedef __bf16 bf16x2 __attribute__((ext_vector_type(2)));
  typedef float f32x2 __attribute__((ext_vector_type(2)));
  f32x2 v = {lo, hi};
  return __builtin_bit_cast(u32, __builtin_convertvector(v, bf16x2));
}
__device__ __forceinline__ u32 perm_lo(u32 a, u32 b) { return __builtin_amdgcn_perm(b, a, 0x05040100u); }
__device__ __forceinline__ u32 perm_hi(u32 a, u32 b) { return __builtin_amdgcn_perm(b, a, 0x07060302u); }
__device__ __forceinline__ float wave_sum(float v) {
#pragma unroll
  for (int o = 32; o >= 1; o >>= 1) v += __shfl_xor(v, o);
  return v;
}

struct Params {
  const u16* x; const u16* kc; const u16* vc; const u16* wq; const u16* wo;
  const u16* wg; const u16* wu; const u16* wd;
  u16* out;
  u16* q;
  u16* h;
  float* op;
  float* ml;
  float* sp;
  float* yp;
};

__device__ __forceinline__ void layernorm_lds(const u16* x, u16* xn, int wave, int lane) {
  const u32x4 a = *(const u32x4*)(x + wave * DM + lane * 8);
  const u32x4 b = *(const u32x4*)(x + wave * DM + 512 + lane * 8);
  float v[16];
#pragma unroll
  for (int i = 0; i < 4; ++i) { v[2 * i] = bf_lo(a[i]); v[2 * i + 1] = bf_hi(a[i]); v[8 + 2 * i] = bf_lo(b[i]); v[9 + 2 * i] = bf_hi(b[i]); }
  float s = 0.f;
#pragma unroll
  for (int i = 0; i < 16; ++i) s += v[i];
  const float mean = wave_sum(s) * (1.f / DM);
  float q = 0.f;
#pragma unroll
  for (int i = 0; i < 16; ++i) { v[i] -= mean; q += v[i] * v[i]; }
  const float var = wave_sum(q) * (1.f / DM);
  const float rs = 1.f / sqrtf(var + 1e-5f);
  u32x4 oa, ob;
#pragma unroll
  for (int i = 0; i < 4; ++i) { oa[i] = pack_bf(v[2 * i] * rs, v[2 * i + 1] * rs); ob[i] = pack_bf(v[8 + 2 * i] * rs, v[9 + 2 * i] * rs); }
  *(u32x4*)(xn + wave * XN_STRIDE + lane * 8) = oa;
  *(u32x4*)(xn + wave * XN_STRIDE + 512 + lane * 8) = ob;
}

template <int LDW>
__device__ __forceinline__ void narrow_load(const u16* w, int col, int row0, int lane, u32 (&r)[16]) {
  const int g = lane >> 4, c = lane & 15, set = c >> 3;
  const u16* p = w + (size_t)(row0 + 32 * set + 8 * g) * LDW + col;
#pragma unroll
  for (int st = 0; st < 2; ++st)
#pragma unroll
    for (int j = 0; j < 8; ++j) r[st * 8 + j] = __builtin_nontemporal_load((const u32*)(p + (size_t)(st * 64 + j) * LDW));
}
__device__ __forceinline__ void narrow_mma(const u32 (&r)[16], const u16* xn, int row0, int lane, f32x4& e, f32x4& o) {
  const int g = lane >> 4, m = lane & 15, set = m >> 3, b = m & 7;
#pragma unroll
  for (int st = 0; st < 2; ++st) {
    const u32x4 a = *(const u32x4*)(xn + b * XN_STRIDE + row0 + st * 64 + 32 * set + 8 * g);
    u32x4 f0, f1;
#pragma unroll
    for (int jj = 0; jj < 4; ++jj) { f0[jj] = perm_lo(r[st * 8 + 2 * jj], r[st * 8 + 2 * jj + 1]); f1[jj] = perm_hi(r[st * 8 + 2 * jj], r[st * 8 + 2 * jj + 1]); }
    e = mfma16(a, f0, e); o = mfma16(a, f1, o);
  }
}

__global__ void __launch_bounds__(NT, 2) k1_proj(Params p) {
  __shared__ __attribute__((aligned(16))) u16 xn[NB * XN_STRIDE];
  __shared__ __attribute__((aligned(16))) float red[3][8][2][4][64];
  const int tid = threadIdx.x, lane = tid & 63, wave = __builtin_amdgcn_readfirstlane(tid >> 6);
  const int cw = (blockIdx.x & 7) * 32 + (blockIdx.x >> 3);
  const int c = lane & 15, chunk = c & 7;
  u32 rq[16], rg[16], ru[16];
  narrow_load<HQ * DH>(p.wq, cw * 16 + 2 * chunk, wave * 128, lane, rq);
  const int ch2 = chunk < 7 ? chunk : 6;
  narrow_load<FF>(p.wg, cw * 14 + 2 * ch2, wave * 128, lane, rg);
  narrow_load<FF>(p.wu, cw * 14 + 2 * ch2, wave * 128, lane, ru);
  layernorm_lds(p.x, xn, wave, lane);
  __syncthreads();
  f32x4 acc[3][2];
#pragma unroll
  for (int i = 0; i < 3; ++i) { acc[i][0] = f32x4{0, 0, 0, 0}; acc[i][1] = f32x4{0, 0, 0, 0}; }
  narrow_mma(rq, xn, wave * 128, lane, acc[0][0], acc[0][1]);
  narrow_mma(rg, xn, wave * 128, lane, acc[1][0], acc[1][1]);
  narrow_mma(ru, xn, wave * 128, lane, acc[2][0], acc[2][1]);
#pragma unroll
  for (int i = 0; i < 3; ++i)
#pragma unroll
    for (int par = 0; par < 2; ++par)
#pragma unroll
      for (int r = 0; r < 4; ++r) red[i][wave][par][r][lane] = acc[i][par][r];
  __syncthreads();
  if (tid < 256) {
    const int which = tid >> 7, b = (tid >> 4) & 7, col = tid & 15, ch = col >> 1, par = col & 1;
    const int l0 = (b >> 2) * 16 + ch, l1 = (2 + (b >> 2)) * 16 + 8 + ch, r = b & 3;
    if (which == 0) {
      float s = 0.f;
#pragma unroll
      for (int w = 0; w < 8; ++w) s += red[0][w][par][r][l0] + red[0][w][par][r][l1];
      p.q[b * (HQ * DH) + cw * 16 + col] = (u16)(pack_bf(s, 0.f) & 0xffff);
    } else if (col < 14) {
      float g = 0.f, u = 0.f;
#pragma unroll
      for (int w = 0; w < 8; ++w) { g += red[1][w][par][r][l0] + red[1][w][par][r][l1]; u += red[2][w][par][r][l0] + red[2][w][par][r][l1]; }
      const float hv = u / (1.f + __expf(-g));
      p.h[b * FF + cw * 14 + col] = (u16)(pack_bf(hv, 0.f) & 0xffff);
    }
  }
}

__global__ void __launch_bounds__(NT, 2) k2_attn(Params p) {
  __shared__ __attribute__((aligned(16))) float smax[8][8];
  __shared__ __attribute__((aligned(16))) float ssum[8][8];
  __shared__ __attribute__((aligned(16))) float ored[8][8][132];
  const int tid = threadIdx.x, lane = tid & 63, wave = __builtin_amdgcn_readfirstlane(tid >> 6);
  const int g = lane >> 4, c = lane & 15;
  const int split = blockIdx.x & 7, bk = blockIdx.x >> 3, b = bk >> 2, kvh = bk & 3;
  const size_t kvbase = ((size_t)bk * SK + split * 512 + wave * 64) * DH;
  const u16* kp = p.kc + kvbase + (size_t)(4 * (c & 3) + (c >> 2)) * DH + g * 8;
  const u16* vp = p.vc + kvbase + lane * 8;
  u32x4 kr[16], vr[16];
#pragma unroll
  for (int t = 0; t < 4; ++t)
#pragma unroll
    for (int s = 0; s < 4; ++s) kr[t * 4 + s] = __builtin_nontemporal_load((const u32x4*)(kp + t * 16 * DH + s * 32));
#pragma unroll
  for (int v = 0; v < 16; ++v) vr[v] = __builtin_nontemporal_load((const u32x4*)(vp + v * 512));
  u32x4 qf[4];
#pragma unroll
  for (int s = 0; s < 4; ++s) qf[s] = *(const u32x4*)(p.q + b * (HQ * DH) + (kvh * 8 + (c & 7)) * DH + s * 32 + g * 8);
  f32x4 sc[4];
#pragma unroll
  for (int t = 0; t < 4; ++t) {
    sc[t] = f32x4{0, 0, 0, 0};
#pragma unroll
    for (int s = 0; s < 4; ++s) sc[t] = mfma16(kr[t * 4 + s], qf[s], sc[t]);
  }
  float mx = -INFINITY;
#pragma unroll
  for (int t = 0; t < 4; ++t)
#pragma unroll
    for (int r = 0; r < 4; ++r) mx = fmaxf(mx, sc[t][r]);
  mx = fmaxf(mx, __shfl_xor(mx, 16)); mx = fmaxf(mx, __shfl_xor(mx, 32));
  if (lane < 8) smax[wave][lane] = mx;
  __syncthreads();
  float M = smax[0][c & 7];
#pragma unroll
  for (int w = 1; w < 8; ++w) M = fmaxf(M, smax[w][c & 7]);
  constexpr float C2 = 0.088388347648318440f * 1.4426950408889634f;
  float l = 0.f;
  u32x4 pf[2];
#pragma unroll
  for (int t = 0; t < 4; ++t) {
    float pv[4];
#pragma unroll
    for (int r = 0; r < 4; ++r) pv[r] = __builtin_amdgcn_exp2f((sc[t][r] - M) * C2);
    const u32 p0 = pack_bf(pv[0], pv[1]), p1 = pack_bf(pv[2], pv[3]);
    l += (bf_lo(p0) + bf_hi(p0)) + (bf_lo(p1) + bf_hi(p1));
    pf[t >> 1][(t & 1) * 2] = p0; pf[t >> 1][(t & 1) * 2 + 1] = p1;
  }
  l += __shfl_xor(l, 16); l += __shfl_xor(l, 32);
  if (lane < 8) ssum[wave][lane] = l;
  f32x4 acc[8];
#pragma unroll
  for (int i = 0; i < 8; ++i) acc[i] = f32x4{0, 0, 0, 0};
#pragma unroll
  for (int u = 0; u < 2; ++u)
#pragma unroll
    for (int i = 0; i < 8; ++i) {
      u32x4 f;
#pragma unroll
      for (int jj = 0; jj < 4; ++jj) {
        const u32 a = vr[u * 8 + 2 * jj][i >> 1], bb = vr[u * 8 + 2 * jj + 1][i >> 1];
        f[jj] = (i & 1) ? perm_hi(a, bb) : perm_lo(a, bb);
      }
      acc[i] = mfma16(pf[u], f, acc[i]);
    }
  if (g < 2) {
#pragma unroll
    for (int r = 0; r < 4; ++r) {
      *(f32x4*)&ored[wave][4 * g + r][8 * c] = f32x4{acc[0][r], acc[1][r], acc[2][r], acc[3][r]};
      *(f32x4*)&ored[wave][4 * g + r][8 * c + 4] = f32x4{acc[4][r], acc[5][r], acc[6][r], acc[7][r]};
    }
  }
  __syncthreads();
  {
    const int head = wave, d = lane * 2;
    float o0 = 0.f, o1 = 0.f;
#pragma unroll
    for (int w = 0; w < 8; ++w) { o0 += ored[w][head][d]; o1 += ored[w][head][d + 1]; }
    const size_t slot = ((size_t)(b * HQ + kvh * 8 + head) * 8 + split);
    *(float2*)(p.op + slot * DH + d) = make_float2(o0, o1);
    if (lane == 0) {
      float L = 0.f, MM = smax[0][head];
#pragma unroll
      for (int w = 0; w < 8; ++w) { L += ssum[w][head]; MM = fmaxf(MM, smax[w][head]); }
      *(float2*)(p.ml + slot * 2) = make_float2(MM * C2, L);
    }
  }
}

constexpr int A_STRIDE = 128 + 8;
__global__ void __launch_bounds__(NT, 2) k3_out(Params p) {
  __shared__ __attribute__((aligned(16))) u16 oa[8][A_STRIDE];
  __shared__ __attribute__((aligned(16))) u16 ha[8][A_STRIDE];
  __shared__ __attribute__((aligned(16))) float red[8][8][128 + 4];
  const int tid = threadIdx.x, lane = tid & 63, wave = __builtin_amdgcn_readfirstlane(tid >> 6);
  const int g = lane >> 4, c = lane & 15;
  const int xcd = blockIdx.x & 7, i5 = blockIdx.x >> 3, ks = xcd * 4 + (i5 >> 3), cg = i5 & 7;
  const bool iso = wave < 4;
  const int wv = wave & 3;
  const u16* wp = iso ? p.wo + (size_t)(ks * 128 + 32 * wv + 8 * g) * DM : p.wd + (size_t)(ks * 112 + 32 * wv + 8 * g) * DM;
  wp += cg * 128 + c * 8;
  u32x4 wr[8];
  const bool valid = iso || wv < 3 || g < 2;
#pragma unroll
  for (int j = 0; j < 8; ++j) wr[j] = u32x4{0, 0, 0, 0};
  if (valid) {
#pragma unroll
    for (int j = 0; j < 8; ++j) wr[j] = __builtin_nontemporal_load((const u32x4*)(wp + (size_t)j * DM));
  }
  {
    const int b = wave, d = lane * 2;
    const size_t slot = (size_t)(b * HQ + ks) * 8;
    float m[8], l[8], M = -INFINITY;
#pragma unroll
    for (int s = 0; s < 8; ++s) { const float2 t = *(const float2*)(p.ml + (slot + s) * 2); m[s] = t.x; l[s] = t.y; M = fmaxf(M, t.x); }
    float L = 0.f, o0 = 0.f, o1 = 0.f;
#pragma unroll
    for (int s = 0; s < 8; ++s) {
      const float w = __builtin_amdgcn_exp2f(m[s] - M);
      const float2 t = *(const float2*)(p.op + (slot + s) * DH + d);
      L += w * l[s]; o0 += w * t.x; o1 += w * t.y;
    }
    const float inv = 1.f / L;
    *(u32*)&oa[b][d] = pack_bf(o0 * inv, o1 * inv);
    u32 hv = 0;
    if (d < 112) hv = *(const u32*)(p.h + b * FF + ks * 112 + d);
    *(u32*)&ha[b][d] = hv;
  }
  __syncthreads();
  const u16* ap = (iso ? &oa[0][0] : &ha[0][0]) + (c & 7) * A_STRIDE + 32 * wv + 8 * g;
  const u32x4 af = *(const u32x4*)ap;
  f32x4 acc[8];
#pragma unroll
  for (int i = 0; i < 8; ++i) {
    u32x4 f;
#pragma unroll
    for (int jj = 0; jj < 4; ++jj) {
      const u32 a = wr[2 * jj][i >> 1], bb = wr[2 * jj + 1][i >> 1];
      f[jj] = (i & 1) ? perm_hi(a, bb) : perm_lo(a, bb);
    }
    acc[i] = mfma16(af, f, f32x4{0, 0, 0, 0});
  }
  if (g < 2) {
#pragma unroll
    for (int r = 0; r < 4; ++r) {
      *(f32x4*)&red[wave][4 * g + r][8 * c] = f32x4{acc[0][r], acc[1][r], acc[2][r], acc[3][r]};
      *(f32x4*)&red[wave][4 * g + r][8 * c + 4] = f32x4{acc[4][r], acc[5][r], acc[6][r], acc[7][r]};
    }
  }
  __syncthreads();
  {
    const int b = wave, d = lane * 2;
    float s0 = 0, s1 = 0, y0 = 0, y1 = 0;
#pragma unroll
    for (int w = 0; w < 4; ++w) { s0 += red[w][b][d]; s1 += red[w][b][d + 1]; y0 += red[4 + w][b][d]; y1 += red[4 + w][b][d + 1]; }
    *(float2*)(p.sp + ((size_t)ks * 8 + b) * DM + cg * 128 + d) = make_float2(s0, s1);
    *(float2*)(p.yp + ((size_t)ks * 8 + b) * DM + cg * 128 + d) = make_float2(y0, y1);
  }
}

__global__ void __launch_bounds__(256) k4_final(Params p) {
  const int i = blockIdx.x * 256 + threadIdx.x;
  float s = 0.f, y = 0.f;
#pragma unroll
  for (int ks = 0; ks < 32; ++ks) { s += p.sp[(size_t)ks * 8192 + i]; y += p.yp[(size_t)ks * 8192 + i]; }
  const float xv = bf_lo((u32)p.x[i]);
  const float sil = y / (1.f + __expf(-y));
  p.out[i] = (u16)(pack_bf(xv + s * sil, 0.f) & 0xffff);
}

extern "C" void kernel_launch(void* const* d_in, const int* in_sizes, int n_in, void* d_out, int out_size, void* d_ws, size_t ws_size,
                              hipStream_t stream) {
  Params p;
  p.x = (const u16*)d_in[0]; p.kc = (const u16*)d_in[1]; p.vc = (const u16*)d_in[2]; p.wq = (const u16*)d_in[3]; p.wo = (const u16*)d_in[4];
  p.wg = (const u16*)d_in[6]; p.wu = (const u16*)d_in[7]; p.wd = (const u16*)d_in[8];
  p.out = (u16*)d_out;
  char* ws = (char*)d_ws;
  p.q = (u16*)ws; p.h = (u16*)(ws + 65536); p.op = (float*)(ws + (1 << 20)); p.ml = (float*)(ws + (3 << 20));
  p.sp = (float*)(ws + (4 << 20)); p.yp = (float*)(ws + (6 << 20));
  k1_proj<<<NWG, NT, 0, stream>>>(p);
  k2_attn<<<NWG, NT, 0, stream>>>(p);
  k3_out<<<NWG, NT, 0, stream>>>(p);
  k4_final<<<32, 256, 0, stream>>>(p);
}
```

```cpp
#include <hip/hip_runtime.h>
#include <hip/hip_bf16.h>
#include <stdint.h>

typedef unsigned int u32;
typedef unsigned short u16;
typedef u32 u32x4 __attribute__((ext_vector_type(4)));
typedef u32 u32x2 __attribute__((ext_vector_type(2)));
typedef float f32x4 __attribute__((ext_vector_type(4)));
typedef __bf16 bf16x8 __attribute__((ext_vector_type(8)));
typedef short s16x4 __attribute__((ext_vector_type(4)));
typedef __amdgpu_buffer_rsrc_t rsrc_t;

constexpr int DM = 1024, HQD = 4096, SK = 4096, DH = 128, FF = 3584;
constexpr int NWG = 256, NT = 512;

constexpr int WS_Q = 4096;
constexpr int WS_GU = WS_Q + 8 * 32 * 4 * 512;
constexpr int WS_O = WS_GU + 224 * 4096;
constexpr int WS_ST = WS_O + 32 * 8 * 8 * 512;
constexpr int WS_F = WS_ST + 32 * 8 * 8 * 16;
constexpr int WS_END = WS_F + 256 * 8192;
constexpr int AUX_NT = 2, AUX_SC1 = 16;
constexpr u32 SPIN_CAP = 20000;

constexpr int XN_STR = 264;
constexpr int A_STR = 136;
constexpr int R_IS = 288, R_GS = 8 * R_IS, R_WS = 2 * R_GS;
constexpr int L_XN = 0;
constexpr int L_QS = L_XN + 8 * XN_STR * 2;
constexpr int L_OA = L_QS + 8 * A_STR * 2;
constexpr int L_HA = L_OA + 8 * A_STR * 2;
constexpr int L_ST = L_HA + 8 * A_STR * 2;
constexpr int L_RQ = L_ST + 8 * 16 * 4;
constexpr int L_RG = L_RQ + 8 * 8 * 64 * 4;
constexpr int L_RO = L_RG + 8 * 8 * 64 * 4;
constexpr int L_RF = L_RO + 8 * R_WS;
constexpr int L_END = L_RF + 8 * R_WS + 16;

#define PIN() do { asm volatile("" ::: "memory"); __builtin_amdgcn_sched_barrier(0); } while (0)
#define USE4(x) asm volatile("" ::"v"(x))
#define BAR() do { asm volatile("s_waitcnt lgkmcnt(0)\n\ts_barrier" ::: "memory"); __builtin_amdgcn_sched_barrier(0); } while (0)

#define TS(i)
#define TS_DECL
#define TS_SET(i, v)
#define TS_FLUSH()

__device__ __forceinline__ f32x4 mfma32(u32x4 a, u32x4 b, f32x4 c) {
  return __builtin_amdgcn_mfma_f32_16x16x32_bf16(__builtin_bit_cast(bf16x8, a), __builtin_bit_cast(bf16x8, b), c, 0, 0, 0);
}
__device__ __forceinline__ f32x4 mfma16(u32x2 a, u32x2 b, f32x4 c) {
  return __builtin_amdgcn_mfma_f32_16x16x16bf16_1k(__builtin_bit_cast(s16x4, a), __builtin_bit_cast(s16x4, b), c, 0, 0, 0);
}
__device__ __forceinline__ float asf(u32 x) { return __builtin_bit_cast(float, x); }
__device__ __forceinline__ u32 asu(float x) { return __builtin_bit_cast(u32, x); }
__device__ __forceinline__ float bf_lo(u32 x) { return asf(x << 16); }
__device__ __forceinline__ float bf_hi(u32 x) { return asf(x & 0xffff0000u); }
__device__ __forceinline__ u32 pack_bf(float lo, float hi) {
  typedef __bf16 bf16x2 __attribute__((ext_vector_type(2)));
  typedef float f32x2 __attribute__((ext_vector_type(2)));
  f32x2 v = {lo, hi};
  return __builtin_bit_cast(u32, __builtin_convertvector(v, bf16x2));
}
__device__ __forceinline__ float rcp(float x) { return __builtin_amdgcn_rcpf(x); }
constexpr float L2E = 1.4426950408889634f;
constexpr int OOB = 0x7ffffff0;
__device__ __forceinline__ float sigm(float x) { return rcp(1.f + __builtin_amdgcn_exp2f(-L2E * x)); }
__device__ __forceinline__ float rbf(float x) { return bf_lo(pack_bf(x, x)); }
__device__ __forceinline__ u32 perm_lo(u32 a, u32 b) { return __builtin_amdgcn_perm(b, a, 0x05040100u); }
__device__ __forceinline__ u32 perm_hi(u32 a, u32 b) { return __builtin_amdgcn_perm(b, a, 0x07060302u); }
template <int CTRL> __device__ __forceinline__ float dpp(float x) {
  return asf(__builtin_amdgcn_mov_dpp(asu(x), CTRL, 0xf, 0xf, true));
}
template <int BANK> __device__ __forceinline__ float ror8_merge(float old, float src) {
  return asf(__builtin_amdgcn_update_dpp(asu(old), asu(src), 0x128, 0xf, BANK, false));
}
constexpr int DPP_X1 = 0xB1, DPP_X2 = 0x4E, DPP_HMIR = 0x141, DPP_MIR = 0x140, DPP_ROR8 = 0x128, DPP_ROR4 = 0x124;
__device__ __forceinline__ void swap32(float& a, float& b) {
  auto r = __builtin_amdgcn_permlane32_swap(asu(a), asu(b), false, false);
  a = asf(r[0]); b = asf(r[1]);
}
__device__ __forceinline__ void swap16(float& a, float& b) {
  auto r = __builtin_amdgcn_permlane16_swap(asu(a), asu(b), false, false);
  a = asf(r[0]); b = asf(r[1]);
}
__device__ __forceinline__ float add32(float x) { float a = x, b = x; swap32(a, b); return a + b; }
__device__ __forceinline__ float add16(float x) { float a = x, b = x; swap16(a, b); return a + b; }
__device__ __forceinline__ float max32(float x) { float a = x, b = x; swap32(a, b); return fmaxf(a, b); }
__device__ __forceinline__ float max16(float x) { float a = x, b = x; swap16(a, b); return fmaxf(a, b); }
__device__ __forceinline__ float wave_sum(float x) {
  x += dpp<DPP_X1>(x); x += dpp<DPP_X2>(x); x += dpp<DPP_HMIR>(x); x += dpp<DPP_MIR>(x);
  return add32(add16(x));
}
__device__ __forceinline__ u32x4 ldnt(rsrc_t r, int voff, int soff) { return __builtin_amdgcn_raw_buffer_load_b128(r, voff, soff, AUX_NT); }
__device__ __forceinline__ u32x4 ldsc(rsrc_t r, int voff, int soff) { return __builtin_amdgcn_raw_buffer_load_b128(r, voff, soff, AUX_SC1); }
__device__ __forceinline__ rsrc_t mkr(const void* p, int bytes) { return __builtin_amdgcn_make_buffer_rsrc((void*)p, 0, bytes, 0x00020000); }

struct Tags { u32 t[4]; };
__device__ __forceinline__ void publish(rsrc_t r, int voff, f32x4 v, const Tags& tg) {
  u32x4 o;
#pragma unroll
  for (int i = 0; i < 4; ++i) o[i] = ((asu(v[i]) + 0x800u) & 0xfffff000u) | tg.t[i];
  __builtin_amdgcn_raw_buffer_store_b128(o, r, voff, 0, AUX_SC1);
}
template <int N> __device__ __forceinline__ bool untag(u32x4 (&m)[N], const Tags& tg) {
  u32 bad = 0;
#pragma unroll
  for (int n = 0; n < N; ++n)
#pragma unroll
    for (int i = 0; i < 4; ++i) { m[n][i] ^= tg.t[i]; bad |= m[n][i]; }
  return __builtin_amdgcn_ballot_w64((bad & 0xfffu) != 0) == 0;
}

struct Params {
  const u16* x; const u16* kc; const u16* vc; const u16* wq; const u16* wo;
  const u16* wg; const u16* wu; const u16* wd;
  u16* out; char* ws;
};

__device__ __forceinline__ void store64(f32x4 (&d)[8], float* red, int g, int c) {
  f32x4 s[4];
#pragma unroll
  for (int i = 0; i < 4; ++i)
#pragma unroll
    for (int r = 0; r < 4; ++r) {
      float a = d[i][r], b = d[i + 4][r];
      swap32(a, b);
      s[r][i] = a + dpp<DPP_ROR8>(b);
    }
  if (c < 8) {
#pragma unroll
    for (int r = 0; r < 4; ++r) *(f32x4*)(red + (4 * (g & 1) + r) * 64 + 8 * c + 4 * (g >> 1)) = s[r];
  }
}
__device__ __forceinline__ void store_acc(f32x4 (&d)[8], char* red, int g, int c) {
  if (g < 2) {
#pragma unroll
    for (int i = 0; i < 8; ++i) *(f32x4*)(red + g * R_GS + i * R_IS + c * 16) = d[i];
  }
}
__device__ __forceinline__ void tr_pair(const u32x4* w, u32x4 (&f)[8], int jj) {
#pragma unroll
  for (int i = 0; i < 8; ++i) {
    const u32 lo = w[2 * jj][i >> 1], hi = w[2 * jj + 1][i >> 1];
    f[i][jj] = (i & 1) ? perm_hi(lo, hi) : perm_lo(lo, hi);
  }
}
__device__ __forceinline__ void merge_stats(float m, float l, float& M, float& L) {
  M = fmaxf(m, dpp<DPP_ROR8>(m));
  M = max32(max16(M));
  L = l * __builtin_amdgcn_exp2f((m - M) * L2E);
  L += dpp<DPP_ROR8>(L);
  L = add32(add16(L));
}

__global__ void __launch_bounds__(NT, 2) fused_kernel(Params p) {
  __shared__ __attribute__((aligned(16))) char lds[L_END];
  const int tid = threadIdx.x, lane = tid & 63, wave = __builtin_amdgcn_readfirstlane(tid >> 6);
  const int g = lane >> 4, c = lane & 15, hi = lane >> 5;
  const int wg = blockIdx.x, grp = (wg & 7) * 4 + (wg >> 6), sub = (wg >> 3) & 7;
  const int ks4 = wg & 3, cg4 = wg >> 2;
  const rsrc_t rws = mkr(p.ws, WS_END);
  TS_DECL;
  TS(0);
  TS_SET(22, (u32)__builtin_amdgcn_s_memtime());

  const u32x4 xa = *(const u32x4*)(p.x + wave * DM + lane * 8);
  const u32x4 xb = *(const u32x4*)(p.x + wave * DM + 512 + lane * 8);
  const u32 ep = __builtin_amdgcn_raw_buffer_load_b32(rws, 0, 0, AUX_SC1);
  const int oidx = (4 * ((wg >> 4) & 1) + g) * DM + (wg >> 5) * 128 + (wg & 15) * 8 + wave;
  const u32 xfin = p.x[oidx];
  PIN();
  asm volatile("s_barrier" ::: "memory");
  PIN();
  u32x4 wqr[4];
  {
    const rsrc_t r = mkr(p.wq + (size_t)(ks4 * 256 + wave * 32) * HQD + cg4 * 64, 32 * HQD * 2);
    const int vo = (16 * (c >> 3) + 4 * g) * (HQD * 2) + (c & 7) * 16;
#pragma unroll
    for (int j = 0; j < 4; ++j) wqr[j] = ldnt(r, vo, j * HQD * 2);
  }
  PIN();
  const size_t kvoff = ((size_t)grp * SK + sub * 512 + wave * 64) * DH;
  const rsrc_t rk = mkr(p.kc + kvoff, 64 * DH * 2);
  const rsrc_t rv = mkr(p.vc + kvoff, 64 * DH * 2);
  const int kvo = (4 * (c & 3) + ((c >> 2) & 1)) * 256 + (4 * (c >> 3) + g) * 16;
  u32x4 kr[16];
  u32x4 vr[16];
#define KGRP(q) do { _Pragma("unroll") for (int n = 4 * (q); n < 4 * (q) + 4; ++n) kr[n] = ldnt(rk, kvo, (16 * (n >> 2) + 2 * ((n >> 1) & 1)) * 256 + (n & 1) * 128); PIN(); } while (0)
#define VGRP(q) do { _Pragma("unroll") for (int n = 4 * (q); n < 4 * (q) + 4; ++n) vr[n] = ldnt(rv, lane * 16, n * 1024); PIN(); } while (0)
#define WAITFOR(x) do { USE4(x); PIN(); } while (0)

  {
    float v[16];
#pragma unroll
    for (int i = 0; i < 4; ++i) { v[2 * i] = bf_lo(xa[i]); v[2 * i + 1] = bf_hi(xa[i]); v[8 + 2 * i] = bf_lo(xb[i]); v[9 + 2 * i] = bf_hi(xb[i]); }
    float s = 0.f;
#pragma unroll
    for (int i = 0; i < 16; ++i) s += v[i];
    const float mean = wave_sum(s) * (1.f / DM);
    float q = 0.f;
#pragma unroll
    for (int i = 0; i < 16; ++i) { const float d = v[i] - mean; q += d * d; }
    const float var = rbf(wave_sum(q) * (1.f / DM));
    const float mb = rbf(mean);
    const float rs = rcp(rbf(__builtin_amdgcn_sqrtf(rbf(var + 1e-5f))));
    const u32x4 xs = (ks4 & 2) ? xb : xa;
    u32x4 o;
#pragma unroll
    for (int i = 0; i < 4; ++i) {
      const u32 d = pack_bf(bf_lo(xs[i]) - mb, bf_hi(xs[i]) - mb);
      o[i] = pack_bf(bf_lo(d) * rs, bf_hi(d) * rs);
    }
    if (hi == (ks4 & 1)) *(u32x4*)(lds + L_XN + (wave * XN_STR + (lane & 31) * 8) * 2) = o;
  }
  TS(1);
  BAR();
  KGRP(0);
  KGRP(1);
  Tags tg;
  {
    const u32 e = __builtin_amdgcn_readfirstlane(ep) + 1u;
#pragma unroll
    for (int i = 0; i < 4; ++i) tg.t[i] = (e + 0x4d5u * i) & 0xfffu;
  }
  {
    const u32x2 a = *(const u32x2*)(lds + L_XN + ((c & 7) * XN_STR + wave * 32 + 16 * (c >> 3) + 4 * g) * 2);
    f32x4 d[8];
#pragma unroll
    for (int i = 0; i < 8; ++i) {
      u32x2 f;
#pragma unroll
      for (int jj = 0; jj < 2; ++jj) {
        const u32 lo = wqr[2 * jj][i >> 1], hh = wqr[2 * jj + 1][i >> 1];
        f[jj] = (i & 1) ? perm_hi(lo, hh) : perm_lo(lo, hh);
      }
      d[i] = mfma16(a, f, f32x4{0, 0, 0, 0});
    }
    store64(d, (float*)(lds + L_RQ) + wave * 512, g, c);
  }
  TS(2);
  BAR();
  {
    const float* rq = (const float*)(lds + L_RQ) + g * 1024 + wave * 64 + c * 4;
    f32x4 s = *(const f32x4*)rq + *(const f32x4*)(rq + 512);
#pragma unroll
    for (int i = 0; i < 4; ++i) s[i] = add32(add16(s[i]));
    publish(rws, lane < 16 ? WS_Q + ((wave * 32 + (cg4 >> 1)) * 4 + ks4) * 512 + (cg4 & 1) * 256 + c * 16 : OOB, s, tg);
  }
  TS(3);
  PIN();
  KGRP(2);
  KGRP(3);
  u32x4 gr[8];
  u32x4 qm[2];
  const int qoff = WS_Q + ((grp >> 2) * 32 + (grp & 3) * 8 + wave) * 2048 + lane * 16;
  const bool has_gu = wg < 224;
  {
    const rsrc_t r = mkr((wave < 4 ? p.wg : p.wu) + (size_t)(ks4 * 256 + (wave & 3) * 64) * FF + (has_gu ? cg4 * 64 : 0), has_gu ? 64 * FF * 2 : 0);
    const int vo = (32 * (c >> 3) + 8 * g) * (FF * 2) + (c & 7) * 16;
#pragma unroll
    for (int j = 0; j < 4; ++j) gr[j] = ldnt(r, vo, j * FF * 2);
    PIN();
    WAITFOR(kr[9]);
    TS(4);
    qm[0] = ldsc(rws, qoff, 0);
    qm[1] = ldsc(rws, qoff, 1024);
    PIN();
#pragma unroll
    for (int j = 4; j < 8; ++j) gr[j] = ldnt(r, vo, j * FF * 2);
  }
  PIN();
  if (__builtin_expect(!untag(qm, tg), 0)) {
    u32 spin = 0;
    do {
      asm volatile("; retry q" ::: "memory");
      qm[0] = ldsc(rws, qoff, 0);
      qm[1] = ldsc(rws, qoff, 1024);
    } while (!untag(qm, tg) && ++spin < SPIN_CAP);
  }
  TS(5);
  {
    float q0 = asf(qm[0][0]) + asf(qm[1][0]), q1 = asf(qm[0][1]) + asf(qm[1][1]);
    float q2 = asf(qm[0][2]) + asf(qm[1][2]), q3 = asf(qm[0][3]) + asf(qm[1][3]);
    swap32(q0, q2); swap32(q1, q3);
    *(u32*)(lds + L_QS + (wave * A_STR + (lane & 31) * 4 + 2 * hi) * 2) = pack_bf(q0 + q2, q1 + q3);
  }
  BAR();
  TS(6);
  VGRP(0);
  float ev[8], mw;
  {
    const u32x4 q0 = *(const u32x4*)(lds + L_QS + ((c & 7) * A_STR + 32 * (c >> 3) + 8 * g) * 2);
    const u32x4 q1 = *(const u32x4*)(lds + L_QS + ((c & 7) * A_STR + 64 + 32 * (c >> 3) + 8 * g) * 2);
    float sc[8];
#pragma unroll
    for (int e = 0; e < 2; ++e) {
      f32x4 t[4];
#pragma unroll
      for (int ua = 0; ua < 4; ++ua) t[ua] = mfma32(kr[(ua >> 1) * 8 + e * 4 + (ua & 1) * 2], q0, f32x4{0, 0, 0, 0});
#pragma unroll
      for (int ua = 0; ua < 4; ++ua) t[ua] = mfma32(kr[(ua >> 1) * 8 + e * 4 + (ua & 1) * 2 + 1], q1, t[ua]);
#pragma unroll
      for (int r = 0; r < 4; ++r) {
        float a = t[0][r], b = t[1][r], a2 = t[2][r], b2 = t[3][r];
        swap32(a, b); swap32(a2, b2);
        sc[e * 4 + r] = ror8_merge<0x3>(b2, b) + ror8_merge<0xc>(a, a2);
      }
    }
    float mx = -INFINITY;
#pragma unroll
    for (int i = 0; i < 4; ++i) {
      const u32 s1 = pack_bf(sc[2 * i], sc[2 * i + 1]);
      const u32 s2 = pack_bf(bf_lo(s1) * 0.08838834764831845f, bf_hi(s1) * 0.08838834764831845f);
      sc[2 * i] = bf_lo(s2); sc[2 * i + 1] = bf_hi(s2);
      mx = fmaxf(mx, fmaxf(sc[2 * i], sc[2 * i + 1]));
    }
    mx = fmaxf(mx, dpp<DPP_ROR8>(mx));
    mw = max32(max16(mx));
    float l = 0.f;
#pragma unroll
    for (int i = 0; i < 8; ++i) { ev[i] = __builtin_amdgcn_exp2f((sc[i] - mw) * L2E); l += ev[i]; }
    l += dpp<DPP_ROR8>(l);
    l = add32(add16(l));
    if (lane < 8) *(float2*)(lds + L_ST + (wave * 8 + lane) * 8) = make_float2(mw, l);
  }
  TS(7);
  BAR();
  {
    const float2 ml = *(const float2*)(lds + L_ST + lane * 8);
    float M, L;
    merge_stats(ml.x, ml.y, M, L);
    const float Lh = asf((asu(L) + 0x800u) & 0xfffff000u);
    publish(rws, (wave == 0 && lane < 8) ? WS_ST + (grp * 8 + sub) * 128 + lane * 16 : OOB, f32x4{M, Lh, L - Lh, 0.f}, tg);
  }
  PIN();
  VGRP(1);
  {
    const u32x4 a = *(const u32x4*)(lds + L_XN + ((c & 7) * XN_STR + (wave & 3) * 64 + 32 * (c >> 3) + 8 * g) * 2);
    u32x4 f[8];
#pragma unroll
    for (int jj = 0; jj < 4; ++jj) tr_pair(gr, f, jj);
    f32x4 d[8];
#pragma unroll
    for (int i = 0; i < 8; ++i) d[i] = mfma32(a, f[i], f32x4{0, 0, 0, 0});
    store64(d, (float*)(lds + L_RG) + wave * 512, g, c);
    TS(8);
    BAR();
    const float* rg = (const float*)(lds + L_RG) + ((wave & 1) * 4 + hi * 2) * 512 + ((lane >> 2) & 7) * 64 + (wave >> 1) * 16 + (lane & 3) * 4;
    f32x4 s = *(const f32x4*)rg + *(const f32x4*)(rg + 512);
#pragma unroll
    for (int i = 0; i < 4; ++i) s[i] = add32(s[i]);
    publish(rws, (has_gu && lane < 32) ? WS_GU + (cg4 * 4 + (wave >> 1)) * 4096 + ks4 * 1024 + (wave & 1) * 512 + lane * 16 : OOB, s, tg);
  }
  TS(9);
  PIN();
  u32x4 sm[1];
  const int stoff = WS_ST + grp * 1024 + lane * 16;
  sm[0] = ldsc(rws, stoff, 0);
  PIN();
  VGRP(2);
  VGRP(3);
  u32x4 gm[4];
  const int goff = WS_GU + (grp * 7 + (wave < 7 ? wave : 6)) * 4096 + lane * 16;
#pragma unroll
  for (int k = 0; k < 4; ++k) gm[k] = ldsc(rws, goff, k * 1024);
  PIN();
  u32x4 wr[8];
  const int wv = wave & 3;
  const rsrc_t rw = wave < 4 ? mkr(p.wo + (size_t)((grp >> 2) * 512 + (grp & 3) * 32 + 8 * wv) * DM + sub * 128, 400 * DM * 2)
                             : mkr(p.wd + (size_t)(grp * 112 + 32 * wv) * DM + sub * 128, (wave == 7 ? 16 : 32) * DM * 2 - sub * 256);
  const int wvo = (wave < 4 ? 2 : 8) * g * (DM * 2) + c * 16;
  const int wsa = (wave < 4 ? 128 : 1) * (DM * 2), wsb = (wave < 4 ? 1 : 4) * (DM * 2);
#pragma unroll
  for (int j = 0; j < 4; ++j) wr[j] = ldnt(rw, wvo, j * wsa);
  PIN();
  u32x4 vt[8];
#pragma unroll
  for (int jj = 0; jj < 4; ++jj) tr_pair(vr, vt, jj);
  if (__builtin_expect(!untag(sm, tg), 0)) {
    u32 spin = 0;
    do {
      asm volatile("; retry s" ::: "memory");
      sm[0] = ldsc(rws, stoff, 0);
    } while (!untag(sm, tg) && ++spin < SPIN_CAP);
  }
  TS(10);
  u32x4 pf, pf1;
  {
    float M, L;
    merge_stats(asf(sm[0][0]), asf(sm[0][1]) + asf(sm[0][2]), M, L);
    const float f = __builtin_amdgcn_exp2f((mw - M) * L2E) * rcp(L);
#pragma unroll
    for (int i = 0; i < 4; ++i) { pf[i] = pack_bf(ev[2 * i] * f, ev[2 * i + 1] * f); pf1[i] = asu(dpp<DPP_ROR8>(asf(pf[i]))); }
  }
  f32x4 acc[8];
#pragma unroll
  for (int i = 0; i < 8; ++i) acc[i] = mfma32(pf, vt[i], f32x4{0, 0, 0, 0});
  PIN();
#pragma unroll
  for (int j = 4; j < 8; ++j) wr[j] = ldnt(rw, wvo, (j & 3) * wsa + wsb);
  PIN();
#pragma unroll
  for (int jj = 0; jj < 4; ++jj) { tr_pair(vr + 8, vt, jj); PIN(); }
#pragma unroll
  for (int i = 0; i < 8; ++i) acc[i] = mfma32(pf1, vt[i], acc[i]);
  store_acc(acc, lds + L_RO + wave * R_WS, g, c);
  TS(11);
  BAR();
  if (wave < 4) {
    const int d = tid & 127, g2 = tid >> 7;
    const char* ro = lds + L_RO + g2 * R_GS + (d & 7) * R_IS + (d >> 3) * 16;
    f32x4 o = *(const f32x4*)ro;
#pragma unroll
    for (int k = 1; k < 8; ++k) o += *(const f32x4*)(ro + k * R_WS);
    publish(rws, WS_O + (((((grp & 3) * 2 + g2) * 4 + (d >> 5)) * 8 + (grp >> 2)) * 8 + sub) * 512 + (d & 31) * 16, o, tg);
  } else {
    publish(rws, OOB, f32x4{0, 0, 0, 0}, tg);
  }
  TS(12);
  PIN();
  if (__builtin_expect(!untag(gm, tg), 0)) {
    u32 spin = 0;
    do {
      asm volatile("; retry g" ::: "memory");
#pragma unroll
      for (int k = 0; k < 4; ++k) gm[k] = ldsc(rws, goff, k * 1024);
    } while (!untag(gm, tg) && ++spin < SPIN_CAP);
  }
  {
    float v[4];
#pragma unroll
    for (int i = 0; i < 4; ++i) v[i] = (asf(gm[0][i]) + asf(gm[1][i])) + (asf(gm[2][i]) + asf(gm[3][i]));
    swap32(v[0], v[2]); swap32(v[1], v[3]);
    const u32 gg = pack_bf(v[0], v[1]), uu = pack_bf(v[2], v[3]);
    const u32 sg = pack_bf(sigm(bf_lo(gg)), sigm(bf_hi(gg)));
    u32 hv = pack_bf(bf_lo(sg) * bf_lo(uu), bf_hi(sg) * bf_hi(uu));
    if (wave == 7) hv = 0;
    *(u32*)(lds + L_HA + (((lane >> 2) & 7) * A_STR + wave * 16 + (lane & 3) * 4 + 2 * hi) * 2) = hv;
  }
  TS(13);
  PIN();
  u32x4 wt[8];
  tr_pair(wr, wt, 0); tr_pair(wr, wt, 1);
  PIN();
  TS(14);
  {
    u32x4 om[4];
    const int ooff = WS_O + (grp * 8 + wave) * 4096 + lane * 16;
#pragma unroll
    for (int k = 0; k < 4; ++k) om[k] = ldsc(rws, ooff, k * 1024);
    PIN();
    tr_pair(wr, wt, 2); tr_pair(wr, wt, 3);
    PIN();
    u32 spin = 0;
    while (!untag(om, tg) && ++spin < SPIN_CAP) {
      asm volatile("; poll o" ::: "memory");
#pragma unroll
      for (int k = 0; k < 4; ++k) om[k] = ldsc(rws, ooff, k * 1024);
    }
    TS(15);
    TS_SET(21, spin + 1);
    float v[4];
#pragma unroll
    for (int i = 0; i < 4; ++i) v[i] = (asf(om[0][i]) + asf(om[1][i])) + (asf(om[2][i]) + asf(om[3][i]));
    swap32(v[0], v[2]); swap32(v[1], v[3]);
    *(u32*)(lds + L_OA + (wave * A_STR + (lane & 31) * 4 + 2 * hi) * 2) = pack_bf(v[0] + v[2], v[1] + v[3]);
  }
  BAR();
  TS(16);
  {
    const u32x4 a = *(const u32x4*)(lds + (wave < 4 ? L_OA : L_HA) + ((c & 7) * A_STR + 32 * wv + 8 * g) * 2);
    f32x4 d[8];
#pragma unroll
    for (int i = 0; i < 8; ++i) d[i] = mfma32(a, wt[i], f32x4{0, 0, 0, 0});
    store_acc(d, lds + L_RF + wave * R_WS + (wave >> 2) * 16, g, c);
  }
  TS(17);
  BAR();
  {
    const int sy = tid & 1, i = (tid >> 1) & 7, oct = (tid >> 4) & 15, g2 = tid >> 8;
    const char* rf = lds + L_RF + sy * (4 * R_WS + 16) + g2 * R_GS + i * R_IS + oct * 16;
    f32x4 s = *(const f32x4*)rf;
#pragma unroll
    for (int k = 1; k < 4; ++k) s += *(const f32x4*)(rf + k * R_WS);
    publish(rws, WS_F + (sub * 32 + (tid >> 4)) * 8192 + grp * 256 + c * 16, s, tg);
  }
  TS(18);
  {
    u32x4 fm[1];
    const int foff = WS_F + wg * 8192 + (lane >> 1) * 256 + wave * 32 + (lane & 1) * 16;
    u32 spin = 0;
    do {
      asm volatile("; poll f" ::: "memory");
      fm[0] = ldsc(rws, foff, 0);
    } while (!untag(fm, tg) && ++spin < SPIN_CAP);
    TS(19);
    float v0 = asf(fm[0][0]), v1 = asf(fm[0][1]), v2 = asf(fm[0][2]), v3 = asf(fm[0][3]);
    swap32(v0, v2); swap32(v1, v3);
    v0 += v2; v1 += v3;
    swap16(v0, v1);
    float z = v0 + v1;
    z += dpp<DPP_ROR8>(z); z += dpp<DPP_ROR4>(z); z += dpp<DPP_X2>(z);
    const float s = rbf(z);
    const float y = dpp<DPP_X1>(s);
    const float sl = rbf(y * sigm(y));
    const float r = bf_lo(xfin) + rbf(s * sl);
    if (c == 0) p.out[oidx] = (u16)pack_bf(r, r);
    if (wg == 0 && tid == 0) __builtin_amdgcn_raw_buffer_store_b32(__builtin_amdgcn_readfirstlane(ep) + 1u, rws, 0, 0, AUX_SC1);
  }
  TS(20);
  TS_SET(23, (u32)__builtin_amdgcn_s_memtime());
  TS_FLUSH();
}

extern "C" void kernel_launch(void* const* d_in, const int* in_sizes, int n_in, void* d_out, int out_size, void* d_ws, size_t ws_size,
                              hipStream_t stream) {
  Params p;
  p.x = (const u16*)d_in[0]; p.kc = (const u16*)d_in[1]; p.vc = (const u16*)d_in[2]; p.wq = (const u16*)d_in[3]; p.wo = (const u16*)d_in[4];
  p.wg = (const u16*)d_in[6]; p.wu = (const u16*)d_in[7]; p.wd = (const u16*)d_in[8];
  p.out = (u16*)d_out; p.ws = (char*)d_ws;
  fused_kernel<<<NWG, NT, 0, stream>>>(p);
}
```

```cpp
#include <hip/hip_runtime.h>
#include <hip/hip_bf16.h>
#include <stdint.h>

typedef unsigned int u32;
typedef unsigned short u16;
typedef u32 u32x4 __attribute__((ext_vector_type(4)));
typedef u32 u32x2 __attribute__((ext_vector_type(2)));
typedef float f32x4 __attribute__((ext_vector_type(4)));
typedef __bf16 bf16x8 __attribute__((ext_vector_type(8)));
typedef short s16x4 __attribute__((ext_vector_type(4)));
typedef __amdgpu_buffer_rsrc_t rsrc_t;

constexpr int DM = 1024, HQD = 4096, SK = 4096, DH = 128, FF = 3584;
constexpr int NWG = 256, NT = 1024;
#define TS_WAVES 16

constexpr int WS_Q = 4096;
constexpr int WS_GU = WS_Q + 8 * 32 * 4 * 1024;
constexpr int WS_O = WS_GU + 224 * 8192;
constexpr int WS_ST = WS_O + 32 * 65536;
constexpr int WS_FS = WS_ST + 32 * 2048;
constexpr int WS_FY = WS_FS + 256 * 4096;
constexpr int WS_END = WS_FY + 256 * 4096;
constexpr int AUX_NT = 2, AUX_SC1 = 16;
constexpr u32 SPIN_CAP = 20000;

constexpr int XN_STR = 264;
constexpr int A_STR = 136;
constexpr int RG_STR = 68;
constexpr int R_IS = 288, R_GS = 8 * R_IS, R_WS = 2 * R_GS;
constexpr int L_CNT = 0;
constexpr int L_XN = 128;
constexpr int L_QS = L_XN + 8 * XN_STR * 2;
constexpr int L_ST = L_QS + 8 * A_STR * 2;
constexpr int L_F = L_ST + 512;
constexpr int L_P = L_F + 256;
constexpr int L_RQ = L_P + 8192;
constexpr int L_RG = L_RQ + 8 * 8 * 64 * 4;
constexpr int L_RO = L_RG + 8 * 8 * RG_STR * 4;
constexpr int L_RS = L_RO + 8 * R_WS;
constexpr int L_RY = L_RS + 4 * R_WS;
constexpr int L_END = L_RY + 4 * R_WS;
enum { C_XN = 0, C_RQ, C_QS, C_ST, C_RG, C_VI, C_DI, C_F, C_RO, C_M4, C_Y, C_S, C_S5, C_P = 16 };

#define PIN() do { asm volatile("" ::: "memory"); __builtin_amdgcn_sched_barrier(0); } while (0)
#define USE4(x) asm volatile("" ::"v"(x))
#define BAR() do { asm volatile("s_waitcnt lgkmcnt(0)\n\ts_barrier" ::: "memory"); __builtin_amdgcn_sched_barrier(0); } while (0)

#define TS(i)
#define TS_DECL
#define TS_SET(i, v) do {} while (0)
#define TS_FLUSH()

__device__ __forceinline__ f32x4 mfma32(u32x4 a, u32x4 b, f32x4 c) {
  return __builtin_amdgcn_mfma_f32_16x16x32_bf16(__builtin_bit_cast(bf16x8, a), __builtin_bit_cast(bf16x8, b), c, 0, 0, 0);
}
__device__ __forceinline__ f32x4 mfma16(u32x2 a, u32x2 b, f32x4 c) {
  return __builtin_amdgcn_mfma_f32_16x16x16bf16_1k(__builtin_bit_cast(s16x4, a), __builtin_bit_cast(s16x4, b), c, 0, 0, 0);
}
__device__ __forceinline__ float asf(u32 x) { return __builtin_bit_cast(float, x); }
__device__ __forceinline__ u32 asu(float x) { return __builtin_bit_cast(u32, x); }
__device__ __forceinline__ float bf_lo(u32 x) { return asf(x << 16); }
__device__ __forceinline__ float bf_hi(u32 x) { return asf(x & 0xffff0000u); }
__device__ __forceinline__ u32 pack_bf(float lo, float hi) {
  typedef __bf16 bf16x2 __attribute__((ext_vector_type(2)));
  typedef float f32x2 __attribute__((ext_vector_type(2)));
  f32x2 v = {lo, hi};
  return __builtin_bit_cast(u32, __builtin_convertvector(v, bf16x2));
}
__device__ __forceinline__ float rcp(float x) { return __builtin_amdgcn_rcpf(x); }
constexpr float L2E = 1.4426950408889634f;
constexpr int OOB = 0x7ffffff0;
__device__ __forceinline__ float sigm(float x) { return rcp(1.f + __builtin_amdgcn_exp2f(-L2E * x)); }
__device__ __forceinline__ float rbf(float x) { return bf_lo(pack_bf(x, x)); }
__device__ __forceinline__ u32 perm_lo(u32 a, u32 b) { return __builtin_amdgcn_perm(b, a, 0x05040100u); }
__device__ __forceinline__ u32 perm_hi(u32 a, u32 b) { return __builtin_amdgcn_perm(b, a, 0x07060302u); }
template <int CTRL> __device__ __forceinline__ float dpp(float x) {
  return asf(__builtin_amdgcn_mov_dpp(asu(x), CTRL, 0xf, 0xf, true));
}
template <int BANK> __device__ __forceinline__ float ror8_merge(float old, float src) {
  return asf(__builtin_amdgcn_update_dpp(asu(old), asu(src), 0x128, 0xf, BANK, false));
}
constexpr int DPP_X1 = 0xB1, DPP_X2 = 0x4E, DPP_HMIR = 0x141, DPP_MIR = 0x140, DPP_ROR8 = 0x128, DPP_ROR4 = 0x124;
__device__ __forceinline__ void swap32(float& a, float& b) {
  auto r = __builtin_amdgcn_permlane32_swap(asu(a), asu(b), false, false);
  a = asf(r[0]); b = asf(r[1]);
}
__device__ __forceinline__ void swap16(float& a, float& b) {
  auto r = __builtin_amdgcn_permlane16_swap(asu(a), asu(b), false, false);
  a = asf(r[0]); b = asf(r[1]);
}
__device__ __forceinline__ float add32(float x) { float a = x, b = x; swap32(a, b); return a + b; }
__device__ __forceinline__ float add16(float x) { float a = x, b = x; swap16(a, b); return a + b; }
__device__ __forceinline__ float max32(float x) { float a = x, b = x; swap32(a, b); return fmaxf(a, b); }
__device__ __forceinline__ float max16(float x) { float a = x, b = x; swap16(a, b); return fmaxf(a, b); }
__device__ __forceinline__ float wave_sum(float x) {
  x += dpp<DPP_X1>(x); x += dpp<DPP_X2>(x); x += dpp<DPP_HMIR>(x); x += dpp<DPP_MIR>(x);
  return add32(add16(x));
}
__device__ __forceinline__ u32x4 ldnt(rsrc_t r, int voff, int soff) { return __builtin_amdgcn_raw_buffer_load_b128(r, voff, soff, AUX_NT); }
__device__ __forceinline__ u32x4 ldsc(rsrc_t r, int voff, int soff) { return __builtin_amdgcn_raw_buffer_load_b128(r, voff, soff, AUX_SC1); }
__device__ __forceinline__ rsrc_t mkr(const void* p, int bytes) { return __builtin_amdgcn_make_buffer_rsrc((void*)p, 0, bytes, 0x00020000); }

struct Tags { u32 t[4]; };
__device__ __forceinline__ int win(int o) { return o + (o >= 384 ? 128 : 0); }
__device__ __forceinline__ void publish(rsrc_t r, int voff, f32x4 v, const Tags& tg) {
  u32x4 o;
#pragma unroll
  for (int i = 0; i < 4; ++i) o[i] = ((asu(v[i]) + 0x800u) & 0xfffff000u) | tg.t[i];
  __builtin_amdgcn_raw_buffer_store_b128(o, r, voff, 0, AUX_SC1);
}
__device__ __forceinline__ void strip(u32x4& m, const Tags& tg) {
#pragma unroll
  for (int i = 0; i < 4; ++i) m[i] ^= tg.t[i];
}
__device__ __forceinline__ u32 or4(const u32x4& m) { return (m[0] | m[1]) | (m[2] | m[3]); }

struct Params {
  const u16* x; const u16* kc; const u16* vc; const u16* wq; const u16* wo;
  const u16* wg; const u16* wu; const u16* wd;
  u16* out; char* ws;
};

template <int STR> __device__ __forceinline__ void store64(f32x4 (&d)[8], float* red, int g, int c) {
  f32x4 s[4];
#pragma unroll
  for (int i = 0; i < 4; ++i)
#pragma unroll
    for (int r = 0; r < 4; ++r) {
      float a = d[i][r], b = d[i + 4][r];
      swap32(a, b);
      s[r][i] = a + dpp<DPP_ROR8>(b);
    }
  if (c < 8) {
#pragma unroll
    for (int r = 0; r < 4; ++r) *(f32x4*)(red + (4 * (g & 1) + r) * STR + 8 * c + 4 * (g >> 1)) = s[r];
  }
}
__device__ __forceinline__ void store_acc(f32x4 (&d)[8], char* red, int g, int c) {
  if (g < 2) {
#pragma unroll
    for (int i = 0; i < 8; ++i) *(f32x4*)(red + g * R_GS + i * R_IS + c * 16) = d[i];
  }
}
__device__ __forceinline__ void tr_pair(const u32x4* w, u32x4 (&f)[8], int jj) {
#pragma unroll
  for (int i = 0; i < 8; ++i) {
    const u32 lo = w[2 * jj][i >> 1], hi = w[2 * jj + 1][i >> 1];
    f[i][jj] = (i & 1) ? perm_hi(lo, hi) : perm_lo(lo, hi);
  }
}
__device__ __forceinline__ void merge_stats(float m, float l, float& M, float& L) {
  M = fmaxf(m, dpp<DPP_ROR8>(m));
  M = max32(max16(M));
  L = l * __builtin_amdgcn_exp2f((m - M) * L2E);
  L += dpp<DPP_ROR8>(L);
  L = add32(add16(L));
}

#define ARRIVE(i) do { PIN(); asm volatile("s_mov_b64 exec, 1\n\tds_add_u32 %0, %1\n\ts_mov_b64 exec, -1" :: "v"(lcnt + 4 * (i)), "v"(1u) : "memory"); PIN(); } while (0)
#define PINV(a, n) do { _Pragma("unroll") for (int i_ = 0; i_ < (n); ++i_) asm volatile("" : "+v"((a)[i_])); } while (0)
#define LWAIT(i, n) do { PIN(); u32 it_ = 0; \
    while (__builtin_amdgcn_readfirstlane(__hip_atomic_load((u32*)(lds + L_CNT) + (i), __ATOMIC_RELAXED, __HIP_MEMORY_SCOPE_WORKGROUP)) < (u32)(n) && ++it_ < (1u << 22)) __builtin_amdgcn_s_sleep(1); \
    PIN(); } while (0)
#define PRIO(n) do { __builtin_amdgcn_s_setprio(n); PIN(); } while (0)
#define MKTAGS() Tags tg; { const u32 e_ = __builtin_amdgcn_readfirstlane(ep) + 1u; _Pragma("unroll") for (int i_ = 0; i_ < 4; ++i_) tg.t[i_] = (e_ + 0x4d5u * i_) & 0xfffu; }
#define RETRY(m, n, addr) do { u32 bad_ = 0; \
    _Pragma("unroll") for (int k = 0; k < (n); ++k) { strip(m[k], tg); bad_ |= or4(m[k]); } \
    if (__builtin_expect(__builtin_amdgcn_ballot_w64((bad_ & 0xfffu) != 0) != 0, 0)) { u32 spin_ = 0; do { asm volatile("; retry" ::: "memory"); \
    bool st_[n]; \
    _Pragma("unroll") for (int k = 0; k < (n); ++k) st_[k] = (or4(m[k]) & 0xfffu) != 0; \
    _Pragma("unroll") for (int k = 0; k < (n); ++k) if (st_[k]) m[k] = ldsc(rws, (addr), 0); \
    PIN(); bad_ = 0; \
    _Pragma("unroll") for (int k = 0; k < (n); ++k) { if (st_[k]) strip(m[k], tg); bad_ |= or4(m[k]); } \
    } while (__builtin_amdgcn_ballot_w64((bad_ & 0xfffu) != 0) != 0 && ++spin_ < SPIN_CAP); TS_SET(21, tsv[21] + spin_ + 1); } } while (0)

__global__ void __launch_bounds__(NT, 4) fused_kernel(Params p) {
  __shared__ __attribute__((aligned(16))) char lds[L_END];
  const int tid = threadIdx.x, lane = tid & 63, wave = __builtin_amdgcn_readfirstlane(tid >> 6);
  const int g = lane >> 4, c = lane & 15, hi = lane >> 5;
  const int wg = blockIdx.x, grp = (wg & 7) * 4 + (wg >> 6), sub = (wg >> 3) & 7;
  const int ks4 = wg & 3, jt = wg >> 2;
  const int cgq = jt >= 56 ? 8 * (jt - 56) + 3 : 8 * (jt / 7) + (jt % 7) + ((jt % 7) >= 3);
  const int mg = jt < 48 ? jt : 48;
  const int cgn = 8 * (mg / 7) + (mg % 7) + ((mg % 7) >= 3);
  const bool has_g = jt < 55 || jt == 62, has_u = jt < 48 || (jt >= 55 && jt != 62);
  const int cgg = (jt >= 48 && jt < 55) ? 8 * (jt - 48) + 3 : cgn, cgu = (jt >= 55 && jt < 62) ? 8 * (jt - 55) + 3 : cgn;
  const int gw = (wg >> 5) * 4 + ks4, sO = (wg & 4) + (((wg >> 3) + 1) & 3), sD = (wg & 4) + (((wg >> 3) + 3) & 3);
  const int w = wave & 7, wv = wave & 3;
  const rsrc_t rws = mkr(p.ws, WS_END);
  const u32 lcnt = (u32)(uintptr_t)(__attribute__((address_space(3))) char*)lds + L_CNT;
  TS_DECL;
  TS(0);
  const u32 ep = __builtin_amdgcn_raw_buffer_load_b32(rws, 0, 0, AUX_SC1);
  const size_t kvoff = ((size_t)grp * SK + sub * 512 + w * 64) * DH;
  if (wave < 8) {
    PRIO(3);
    const u32x4 xa = *(const u32x4*)(p.x + w * DM + lane * 8);
    const u32x4 xb = *(const u32x4*)(p.x + w * DM + 512 + lane * 8);
    const int oidx = (4 * ((wg >> 4) & 1) + g) * DM + (wg >> 5) * 128 + (wg & 15) * 8 + (c & 7);
    const u32 xfin = p.x[oidx];
    PIN();
    u32x4 wqr[4];
    {
      const rsrc_t r = mkr(p.wq + (size_t)(ks4 * 256 + w * 32) * HQD + cgq * 64, 32 * HQD * 2);
      const int vo = (16 * (c >> 3) + 4 * g) * (HQD * 2) + (c & 7) * 16;
#pragma unroll
      for (int j = 0; j < 4; ++j) wqr[j] = ldnt(r, vo, j * HQD * 2);
    }
    if (tid < 32) ((u32*)(lds + L_CNT))[tid] = 0;
    BAR();
    {
      float v[16];
#pragma unroll
      for (int i = 0; i < 4; ++i) { v[2 * i] = bf_lo(xa[i]); v[2 * i + 1] = bf_hi(xa[i]); v[8 + 2 * i] = bf_lo(xb[i]); v[9 + 2 * i] = bf_hi(xb[i]); }
      float s = 0.f;
#pragma unroll
      for (int i = 0; i < 16; ++i) s += v[i];
      const float mean = wave_sum(s) * (1.f / DM);
      float q = 0.f;
#pragma unroll
      for (int i = 0; i < 16; ++i) { const float d = v[i] - mean; q += d * d; }
      const float var = rbf(wave_sum(q) * (1.f / DM));
      const float mb = rbf(mean);
      const float rs = rcp(rbf(__builtin_amdgcn_sqrtf(rbf(var + 1e-5f))));
      const u32x4 xs = (ks4 & 2) ? xb : xa;
      u32x4 o;
#pragma unroll
      for (int i = 0; i < 4; ++i) {
        const u32 d = pack_bf(bf_lo(xs[i]) - mb, bf_hi(xs[i]) - mb);
        o[i] = pack_bf(bf_lo(d) * rs, bf_hi(d) * rs);
      }
      if (hi == (ks4 & 1)) *(u32x4*)(lds + L_XN + (w * XN_STR + (lane & 31) * 8) * 2) = o;
    }
    ARRIVE(C_XN);
    TS(1);
    MKTAGS();
    u32x2 fq[8];
#pragma unroll
    for (int i = 0; i < 8; ++i)
#pragma unroll
      for (int jj = 0; jj < 2; ++jj) {
        const u32 lo = wqr[2 * jj][i >> 1], hh = wqr[2 * jj + 1][i >> 1];
        fq[i][jj] = (i & 1) ? perm_hi(lo, hh) : perm_lo(lo, hh);
      }
    PINV(fq, 8);
    LWAIT(C_XN, 8);
    {
      const u32x2 a = *(const u32x2*)(lds + L_XN + ((c & 7) * XN_STR + w * 32 + 16 * (c >> 3) + 4 * g) * 2);
      f32x4 d[8];
#pragma unroll
      for (int i = 0; i < 8; ++i) d[i] = mfma16(a, fq[i], f32x4{0, 0, 0, 0});
      store64<64>(d, (float*)(lds + L_RQ) + w * 512, g, c);
    }
    ARRIVE(C_RQ);
    TS(2);
    LWAIT(C_RQ, 8);
    {
      const float* rq = (const float*)(lds + L_RQ) + g * 1024 + w * 64 + c * 4;
      f32x4 s = *(const f32x4*)rq + *(const f32x4*)(rq + 512);
#pragma unroll
      for (int i = 0; i < 4; ++i) s[i] = add32(add16(s[i]));
      publish(rws, lane < 16 ? WS_Q + ((w * 32 + (cgq >> 1)) * 2 + (ks4 >> 1)) * 2048 + win((ks4 & 1) * 512 + (cgq & 1) * 256 + c * 16) : OOB, s, tg);
    }
    TS(3);
    PRIO(1);
    const rsrc_t rv = mkr(p.vc + kvoff, 64 * DH * 2);
    u32x4 gr[8], vr[16];
    {
      const bool has_gu = w < 4 ? has_g : has_u;
      const rsrc_t r = mkr((w < 4 ? p.wg : p.wu) + (size_t)(ks4 * 256 + wv * 64) * FF + (has_gu ? (w < 4 ? cgg : cgu) * 64 : 0), has_gu ? 64 * FF * 2 : 0);
      const int vo = (32 * (c >> 3) + 8 * g) * (FF * 2) + (c & 7) * 16;
#pragma unroll
      for (int j = 0; j < 8; ++j) gr[j] = ldnt(r, vo, j * FF * 2);
    }
    PRIO(0);
#pragma unroll
    for (int n = 0; n < 8; ++n) vr[n] = ldnt(rv, lane * 16, n * 1024);
    PIN();
    TS(4);
    PRIO(3);
    {
      const u32x4 a = *(const u32x4*)(lds + L_XN + ((c & 7) * XN_STR + wv * 64 + 32 * (c >> 3) + 8 * g) * 2);
      u32x4 f[8];
#pragma unroll
      for (int jj = 0; jj < 4; ++jj) tr_pair(gr, f, jj);
      f32x4 d[8];
#pragma unroll
      for (int i = 0; i < 8; ++i) d[i] = mfma32(a, f[i], f32x4{0, 0, 0, 0});
      store64<RG_STR>(d, (float*)(lds + L_RG) + w * 8 * RG_STR, g, c);
    }
    ARRIVE(C_RG);
    TS(5);
    PRIO(0);
#pragma unroll
    for (int n = 8; n < 16; ++n) vr[n] = ldnt(rv, lane * 16, n * 1024);
    ARRIVE(C_VI);
    TS(6);
    PRIO(3);
    u32x4 vt[8];
#pragma unroll
    for (int jj = 0; jj < 4; ++jj) tr_pair(vr, vt, jj);
    PINV(vt, 8);
    LWAIT(C_P + w, 1);
    TS(7);
    f32x4 acc[8];
    {
      const u32x4 pf = *(const u32x4*)(lds + L_P + w * 1024 + lane * 16);
#pragma unroll
      for (int i = 0; i < 8; ++i) acc[i] = mfma32(pf, vt[i], f32x4{0, 0, 0, 0});
    }
    PIN();
    TS(8);
    const u32x4 pf1 = *(const u32x4*)(lds + L_P + w * 1024 + (lane ^ 8) * 16);
#pragma unroll
    for (int jj = 0; jj < 4; ++jj) { tr_pair(vr + 8, vt, jj); PIN(); }
#pragma unroll
    for (int i = 0; i < 8; ++i) acc[i] = mfma32(pf1, vt[i], acc[i]);
    store_acc(acc, lds + L_RO + w * R_WS, g, c);
    ARRIVE(C_RO);
    TS(9);
    LWAIT(C_F, 1);
    LWAIT(C_RO, 8);
    TS(10);
    {
      const int g2 = w >> 2, i = lane & 7, cl = (lane >> 3) & 3;
      const char* ro = lds + L_RO + hi * 4 * R_WS + g2 * R_GS + i * R_IS + (wv * 4 + cl) * 16;
      const char* fp = lds + L_F + hi * 128 + g2 * 16;
      f32x4 o = *(const f32x4*)ro * *(const f32x4*)fp;
#pragma unroll
      for (int k = 1; k < 4; ++k) o += *(const f32x4*)(ro + k * R_WS) * *(const f32x4*)(fp + k * 32);
#pragma unroll
      for (int r = 0; r < 4; ++r) o[r] = add32(o[r]);
      publish(rws, hi ? OOB : WS_O + ((((grp & 3) * 2 + g2) * 4 + wv) * 4 + cl) * 16384 + sub * 2048 + win((grp >> 2) * 128) + i * 16, o, tg);
    }
    ARRIVE(C_M4);
    TS(11);
    if (w == 0) {
      LWAIT(C_S5, 4);
      TS(12);
      u32x4 ym[4], sm[4];
      const int foff = wg * 4096 + lane * 16;
#pragma unroll
      for (int k = 0; k < 4; ++k) ym[k] = ldsc(rws, WS_FY + foff, k * 1024);
#pragma unroll
      for (int k = 0; k < 4; ++k) sm[k] = ldsc(rws, WS_FS + foff, k * 1024);
      RETRY(ym, 4, WS_FY + foff + k * 1024);
      float z[2];
      {
        float v[4];
#pragma unroll
        for (int i = 0; i < 4; ++i) v[i] = (asf(ym[0][i]) + asf(ym[1][i])) + (asf(ym[2][i]) + asf(ym[3][i]));
        swap32(v[0], v[2]); swap32(v[1], v[3]);
        v[0] += v[2]; v[1] += v[3];
        swap16(v[0], v[1]);
        float t = v[0] + v[1];
        t = rbf(t + dpp<DPP_ROR8>(t));
        z[0] = rbf(t * sigm(t));
      }
      TS(13);
      RETRY(sm, 4, WS_FS + foff + k * 1024);
      TS(14);
      {
        float v[4];
#pragma unroll
        for (int i = 0; i < 4; ++i) v[i] = (asf(sm[0][i]) + asf(sm[1][i])) + (asf(sm[2][i]) + asf(sm[3][i]));
        swap32(v[0], v[2]); swap32(v[1], v[3]);
        v[0] += v[2]; v[1] += v[3];
        swap16(v[0], v[1]);
        float t = v[0] + v[1];
        z[1] = rbf(t + dpp<DPP_ROR8>(t));
      }
      const float r = bf_lo(xfin) + rbf(z[1] * z[0]);
      if (c < 8) p.out[oidx] = (u16)pack_bf(r, r);
      if (wg == 0 && lane == 0) __builtin_amdgcn_raw_buffer_store_b32(__builtin_amdgcn_readfirstlane(ep) + 1u, rws, 0, 0, AUX_SC1);
      TS(15);
    }
  } else {
    asm volatile("s_barrier" ::: "memory");
    PRIO(2);
    const rsrc_t rk = mkr(p.kc + kvoff, 64 * DH * 2);
    const int kvo = (4 * (c & 3) + ((c >> 2) & 1)) * 256 + (4 * (c >> 3) + g) * 16;
    u32x4 kr[16];
#pragma unroll
    for (int n = 0; n < 16; ++n) kr[n] = ldnt(rk, kvo, (16 * (n >> 2) + 2 * ((n >> 1) & 1)) * 256 + (n & 1) * 128);
    PIN();
    TS(1);
    PRIO(3);
    if (w < 4) { USE4(kr[15]); PIN(); }
    u32x4 qm[2];
    const int qoff = WS_Q + ((grp >> 2) * 32 + (grp & 3) * 8 + w) * 4096 + win(lane * 16);
    qm[0] = ldsc(rws, qoff, 0);
    qm[1] = ldsc(rws, qoff, 2048);
    PIN();
    MKTAGS();
    RETRY(qm, 2, qoff + k * 2048);
    TS(2);
    {
      float q0 = asf(qm[0][0]) + asf(qm[1][0]), q1 = asf(qm[0][1]) + asf(qm[1][1]);
      float q2 = asf(qm[0][2]) + asf(qm[1][2]), q3 = asf(qm[0][3]) + asf(qm[1][3]);
      swap32(q0, q2); swap32(q1, q3);
      *(u32*)(lds + L_QS + (w * A_STR + (lane & 31) * 4 + 2 * hi) * 2) = pack_bf(q0 + q2, q1 + q3);
    }
    ARRIVE(C_QS);
    LWAIT(C_QS, 8);
    TS(3);
    {
      const u32x4 q0 = *(const u32x4*)(lds + L_QS + ((c & 7) * A_STR + 32 * (c >> 3) + 8 * g) * 2);
      const u32x4 q1 = *(const u32x4*)(lds + L_QS + ((c & 7) * A_STR + 64 + 32 * (c >> 3) + 8 * g) * 2);
      float sc[8];
#pragma unroll
      for (int e = 0; e < 2; ++e) {
        f32x4 t[4];
#pragma unroll
        for (int ua = 0; ua < 4; ++ua) t[ua] = mfma32(kr[(ua >> 1) * 8 + e * 4 + (ua & 1) * 2], q0, f32x4{0, 0, 0, 0});
#pragma unroll
        for (int ua = 0; ua < 4; ++ua) t[ua] = mfma32(kr[(ua >> 1) * 8 + e * 4 + (ua & 1) * 2 + 1], q1, t[ua]);
#pragma unroll
        for (int r = 0; r < 4; ++r) {
          float a = t[0][r], b = t[1][r], a2 = t[2][r], b2 = t[3][r];
          swap32(a, b); swap32(a2, b2);
          sc[e * 4 + r] = ror8_merge<0x3>(b2, b) + ror8_merge<0xc>(a, a2);
        }
      }
      float mx = -INFINITY;
#pragma unroll
      for (int i = 0; i < 4; ++i) {
        const u32 s1 = pack_bf(sc[2 * i], sc[2 * i + 1]);
        const u32 s2 = pack_bf(bf_lo(s1) * 0.08838834764831845f, bf_hi(s1) * 0.08838834764831845f);
        sc[2 * i] = bf_lo(s2); sc[2 * i + 1] = bf_hi(s2);
        mx = fmaxf(mx, fmaxf(sc[2 * i], sc[2 * i + 1]));
      }
      mx = fmaxf(mx, dpp<DPP_ROR8>(mx));
      const float mw = max32(max16(mx));
      float l = 0.f;
      u32x4 pf;
#pragma unroll
      for (int i = 0; i < 4; ++i) {
        const float e0 = __builtin_amdgcn_exp2f((sc[2 * i] - mw) * L2E), e1 = __builtin_amdgcn_exp2f((sc[2 * i + 1] - mw) * L2E);
        l += e0 + e1;
        pf[i] = pack_bf(e0, e1);
      }
      *(u32x4*)(lds + L_P + w * 1024 + lane * 16) = pf;
      ARRIVE(C_P + w);
      l += dpp<DPP_ROR8>(l);
      l = add32(add16(l));
      if (lane < 8) *(float2*)(lds + L_ST + (w * 8 + lane) * 8) = make_float2(mw, l);
    }
    ARRIVE(C_ST);
    TS(4);
    u32x4 wr[8], wt[8];
    if (w >= 4) {
      LWAIT(C_RG, 8);
      TS(5);
      {
        const float* rg = (const float*)(lds + L_RG) + ((lane >> 3) & 1) * 4 * 8 * RG_STR + (lane & 7) * RG_STR + wv * 16 + ((lane >> 4) & 1) * 8 + hi * 4;
        f32x4 s = *(const f32x4*)rg;
#pragma unroll
        for (int k = 1; k < 4; ++k) s += *(const f32x4*)(rg + k * 8 * RG_STR);
        publish(rws, ((lane & 8) ? has_u : has_g) ? WS_GU + (((lane & 8) ? cgu : cgg) * 4 + wv) * 8192 + ks4 * 2048 + win(lane * 16) : OOB, s, tg);
      }
      TS(6);
      LWAIT(C_VI, 8);
      TS(7);
      u32x4 gm[8];
      const int gbase = WS_GU + (gw * 7 + 2 * wv + (wv == 3 ? 0 : hi)) * 8192;
      const int goff0 = gbase + win((g & 1) * 256 + c * 16), goff1 = gbase + 640 + (g & 1) * 256 + c * 16;
#pragma unroll
      for (int k = 0; k < 8; ++k) gm[k] = ldsc(rws, (k & 1) ? goff1 : goff0, (k >> 1) * 2048);
      PRIO(0);
      const rsrc_t rw = mkr(p.wd + (size_t)(gw * 112 + 32 * wv) * DM + sD * 128, (wv == 3 ? 16 : 32) * DM * 2 - sD * 256);
#pragma unroll
      for (int j = 0; j < 8; ++j) wr[j] = ldnt(rw, 8 * g * (DM * 2) + c * 16, j * DM * 2);
      ARRIVE(C_DI);
      PRIO(3);
      TS(8);
      RETRY(gm, 8, ((k & 1) ? goff1 : goff0) + (k >> 1) * 2048);
      TS(9);
      u32x4 a;
#pragma unroll
      for (int i = 0; i < 4; ++i) {
        const int qp = i >> 1, e = (i & 1) * 2;
        const float v0 = (asf(gm[qp][e]) + asf(gm[2 + qp][e])) + (asf(gm[4 + qp][e]) + asf(gm[6 + qp][e]));
        const float v1 = (asf(gm[qp][e + 1]) + asf(gm[2 + qp][e + 1])) + (asf(gm[4 + qp][e + 1]) + asf(gm[6 + qp][e + 1]));
        const u32 gg = pack_bf(v0, v1);
        const u32 uu = asu(dpp<DPP_ROR8>(asf(gg)));
        const u32 sg = pack_bf(sigm(bf_lo(gg)), sigm(bf_hi(gg)));
        a[i] = pack_bf(bf_lo(sg) * bf_lo(uu), bf_hi(sg) * bf_hi(uu));
        if (wv == 3 && hi) a[i] = 0;
      }
      PINV(a, 4);
      TS(10);
#pragma unroll
      for (int jj = 0; jj < 4; ++jj) { tr_pair(wr, wt, jj); PIN(); }
      f32x4 d[8];
#pragma unroll
      for (int i = 0; i < 8; ++i) d[i] = mfma32(a, wt[i], f32x4{0, 0, 0, 0});
      store_acc(d, lds + L_RY + wv * R_WS, g, c);
      ARRIVE(C_Y);
      TS(11);
      LWAIT(C_Y, 4);
      {
        const int t = wv * 64 + lane;
        const char* rf = lds + L_RY + (t >> 7) * R_GS + (t & 7) * R_IS + ((t >> 3) & 15) * 16;
        f32x4 s = *(const f32x4*)rf;
#pragma unroll
        for (int k = 1; k < 4; ++k) s += *(const f32x4*)(rf + k * R_WS);
        publish(rws, WS_FY + (sD * 32 + (t >> 3)) * 4096 + gw * 128 + (t & 7) * 16, s, tg);
      }
      TS(12);
    } else {
      if (w == 0) {
        LWAIT(C_ST, 8);
        const float2 ml = *(const float2*)(lds + L_ST + lane * 8);
        float M, L;
        merge_stats(ml.x, ml.y, M, L);
        const float Lh = asf((asu(L) + 0x800u) & 0xfffff000u);
        publish(rws, lane < 8 ? WS_ST + grp * 2048 + win(sub * 128) + lane * 16 : OOB, f32x4{M, Lh, L - Lh, 0.f}, tg);
        TS(5);
        LWAIT(C_RG, 8);
        u32x4 sm[1];
        const int stoff = WS_ST + grp * 2048 + win(lane * 16);
        sm[0] = ldsc(rws, stoff, 0);
        RETRY(sm, 1, stoff);
        merge_stats(asf(sm[0][0]), asf(sm[0][1]) + asf(sm[0][2]), M, L);
        *(float*)(lds + L_F + lane * 4) = __builtin_amdgcn_exp2f((ml.x - M) * L2E) * rcp(L);
        ARRIVE(C_F);
        TS(6);
      }
      LWAIT(C_DI, 4);
      TS(7);
      PRIO(0);
      const rsrc_t rw = mkr(p.wo + (size_t)((gw >> 2) * 512 + (gw & 3) * 32 + 8 * wv) * DM + sO * 128, 400 * DM * 2);
#pragma unroll
      for (int j = 0; j < 8; ++j) wr[j] = ldnt(rw, 2 * g * (DM * 2) + c * 16, ((j & 3) * 128 + (j >> 2)) * DM * 2);
      PIN();
      PRIO(3);
      TS(8);
      LWAIT(C_M4, 8);
      TS(9);
      u32x4 om[8];
      const int ooff = WS_O + (gw * 4 + wv) * 16384 + win((c & 7) * 128) + (2 * g + (c >> 3)) * 16;
#pragma unroll
      for (int k = 0; k < 8; ++k) om[k] = ldsc(rws, ooff, k * 2048);
      PIN();
#pragma unroll
      for (int jj = 0; jj < 4; ++jj) { tr_pair(wr, wt, jj); PIN(); }
      PINV(wt, 8);
      TS(10);
      RETRY(om, 8, ooff + k * 2048);
      TS(11);
      u32x4 a;
      {
        float v[4];
#pragma unroll
        for (int i = 0; i < 4; ++i)
          v[i] = ((asf(om[0][i]) + asf(om[1][i])) + (asf(om[2][i]) + asf(om[3][i]))) + ((asf(om[4][i]) + asf(om[5][i])) + (asf(om[6][i]) + asf(om[7][i])));
        a[0] = pack_bf(v[0], v[1]); a[1] = pack_bf(v[2], v[3]);
        a[2] = asu(dpp<DPP_ROR8>(asf(a[0]))); a[3] = asu(dpp<DPP_ROR8>(asf(a[1])));
      }
      f32x4 d[8];
#pragma unroll
      for (int i = 0; i < 8; ++i) d[i] = mfma32(a, wt[i], f32x4{0, 0, 0, 0});
      store_acc(d, lds + L_RS + wv * R_WS, g, c);
      ARRIVE(C_S);
      TS(12);
      LWAIT(C_S, 4);
      {
        const int t = wv * 64 + lane;
        const char* rf = lds + L_RS + (t >> 7) * R_GS + (t & 7) * R_IS + ((t >> 3) & 15) * 16;
        f32x4 s = *(const f32x4*)rf;
#pragma unroll
        for (int k = 1; k < 4; ++k) s += *(const f32x4*)(rf + k * R_WS);
        publish(rws, WS_FS + (sO * 32 + (t >> 3)) * 4096 + gw * 128 + (t & 7) * 16, s, tg);
      }
      ARRIVE(C_S5);
      TS(13);
    }
  }
  TS_FLUSH();
}

extern "C" void kernel_launch(void* const* d_in, const int* in_sizes, int n_in, void* d_out, int out_size, void* d_ws, size_t ws_size,
                              hipStream_t stream) {
  Params p;
  p.x = (const u16*)d_in[0]; p.kc = (const u16*)d_in[1]; p.vc = (const u16*)d_in[2]; p.wq = (const u16*)d_in[3]; p.wo = (const u16*)d_in[4];
  p.wg = (const u16*)d_in[6]; p.wu = (const u16*)d_in[7]; p.wd = (const u16*)d_in[8];
  p.out = (u16*)d_out; p.ws = (char*)d_ws;
  fused_kernel<<<NWG, NT, 0, stream>>>(p);
}
```

```cpp
#include <hip/hip_runtime.h>
#include <hip/hip_bf16.h>
#include <stdint.h>

typedef unsigned int u32;
typedef unsigned short u16;
typedef u32 u32x4 __attribute__((ext_vector_type(4)));
typedef u32 u32x2 __attribute__((ext_vector_type(2)));
typedef float f32x4 __attribute__((ext_vector_type(4)));
typedef __bf16 bf16x8 __attribute__((ext_vector_type(8)));
typedef short s16x4 __attribute__((ext_vector_type(4)));
typedef __amdgpu_buffer_rsrc_t rsrc_t;

constexpr int DM = 1024, HQD = 4096, SK = 4096, DH = 128, FF = 3584;
constexpr int NWG = 256, NT = 1024;
#define TS_WAVES 16

constexpr int WS_Q = 4096;
constexpr int WS_GU = WS_Q + 8 * 32 * 4 * 1024;
constexpr int WS_O = WS_GU + 224 * 8192;
constexpr int WS_ST = WS_O + 32 * 65536;
constexpr int WS_FS = WS_ST + 32 * 2048;
constexpr int WS_FY = WS_FS + 256 * 4096;
constexpr int WS_END = WS_FY + 256 * 4096;
constexpr int AUX_NT = 2, AUX_SC1 = 16;
constexpr u32 SPIN_CAP = 20000;

constexpr int XN_STR = 264;
constexpr int A_STR = 136;
constexpr int RG_STR = 68;
constexpr int R_IS = 288, R_GS = 8 * R_IS, R_WS = 2 * R_GS;
constexpr int L_CNT = 0;
constexpr int L_XN = 128;
constexpr int L_QS = L_XN + 8 * XN_STR * 2;
constexpr int L_ST = L_QS + 8 * A_STR * 2;
constexpr int L_F = L_ST + 512;
constexpr int L_P = L_F + 256;
constexpr int L_RQ = L_P + 8192;
constexpr int L_RG = L_RQ + 8 * 8 * 64 * 4;
constexpr int L_RO = L_RG + 8 * 8 * RG_STR * 4;
constexpr int L_RS = L_RO + 8 * R_WS;
constexpr int L_RY = L_RS + 4 * R_WS;
constexpr int L_END = L_RY + 4 * R_WS;
enum { C_XN = 0, C_RQ, C_QS, C_ST, C_RG, C_VI, C_DI, C_F, C_RO, C_M4, C_Y, C_S, C_S5, C_P = 16 };

#define PIN() do { asm volatile("" ::: "memory"); __builtin_amdgcn_sched_barrier(0); } while (0)
#define USE4(x) asm volatile("" ::"v"(x))
#define BAR() do { asm volatile("s_waitcnt lgkmcnt(0)\n\ts_barrier" ::: "memory"); __builtin_amdgcn_sched_barrier(0); } while (0)

#define TS(i)
#define TS_DECL
#define TS_SET(i, v) do {} while (0)
#define TS_FLUSH()

__device__ __forceinline__ f32x4 mfma32(u32x4 a, u32x4 b, f32x4 c) {
  return __builtin_amdgcn_mfma_f32_16x16x32_bf16(__builtin_bit_cast(bf16x8, a), __builtin_bit_cast(bf16x8, b), c, 0, 0, 0);
}
__device__ __forceinline__ f32x4 mfma16(u32x2 a, u32x2 b, f32x4 c) {
  return __builtin_amdgcn_mfma_f32_16x16x16bf16_1k(__builtin_bit_cast(s16x4, a), __builtin_bit_cast(s16x4, b), c, 0, 0, 0);
}
__device__ __forceinline__ float asf(u32 x) { return __builtin_bit_cast(float, x); }
__device__ __forceinline__ u32 asu(float x) { return __builtin_bit_cast(u32, x); }
__device__ __forceinline__ float bf_lo(u32 x) { return asf(x << 16); }
__device__ __forceinline__ float bf_hi(u32 x) { return asf(x & 0xffff0000u); }
__device__ __forceinline__ u32 pack_bf(float lo, float hi) {
  typedef __bf16 bf16x2 __attribute__((ext_vector_type(2)));
  typedef float f32x2 __attribute__((ext_vector_type(2)));
  f32x2 v = {lo, hi};
  return __builtin_bit_cast(u32, __builtin_convertvector(v, bf16x2));
}
__device__ __forceinline__ float rcp(float x) { return __builtin_amdgcn_rcpf(x); }
constexpr float L2E = 1.4426950408889634f;
constexpr int OOB = 0x7ffffff0;
__device__ __forceinline__ float sigm(float x) { return rcp(1.f + __builtin_amdgcn_exp2f(-L2E * x)); }
__device__ __forceinline__ float rbf(float x) { return bf_lo(pack_bf(x, x)); }
__device__ __forceinline__ u32 perm_lo(u32 a, u32 b) { return __builtin_amdgcn_perm(b, a, 0x05040100u); }
__device__ __forceinline__ u32 perm_hi(u32 a, u32 b) { return __builtin_amdgcn_perm(b, a, 0x07060302u); }
template <int CTRL> __device__ __forceinline__ float dpp(float x) {
  return asf(__builtin_amdgcn_mov_dpp(asu(x), CTRL, 0xf, 0xf, true));
}
template <int BANK> __device__ __forceinline__ float ror8_merge(float old, float src) {
  return asf(__builtin_amdgcn_update_dpp(asu(old), asu(src), 0x128, 0xf, BANK, false));
}
constexpr int DPP_X1 = 0xB1, DPP_X2 = 0x4E, DPP_HMIR = 0x141, DPP_MIR = 0x140, DPP_ROR8 = 0x128, DPP_ROR4 = 0x124;
__device__ __forceinline__ void swap32(float& a, float& b) {
  auto r = __builtin_amdgcn_permlane32_swap(asu(a), asu(b), false, false);
  a = asf(r[0]); b = asf(r[1]);
}
__device__ __forceinline__ void swap16(float& a, float& b) {
  auto r = __builtin_amdgcn_permlane16_swap(asu(a), asu(b), false, false);
  a = asf(r[0]); b = asf(r[1]);
}
__device__ __forceinline__ float add32(float x) { float a = x, b = x; swap32(a, b); return a + b; }
__device__ __forceinline__ float add16(float x) { float a = x, b = x; swap16(a, b); return a + b; }
__device__ __forceinline__ float max32(float x) { float a = x, b = x; swap32(a, b); return fmaxf(a, b); }
__device__ __forceinline__ float max16(float x) { float a = x, b = x; swap16(a, b); return fmaxf(a, b); }
__device__ __forceinline__ float wave_sum(float x) {
  x += dpp<DPP_X1>(x); x += dpp<DPP_X2>(x); x += dpp<DPP_HMIR>(x); x += dpp<DPP_MIR>(x);
  return add32(add16(x));
}
__device__ __forceinline__ u32x4 ldnt(rsrc_t r, int voff, int soff) { return __builtin_amdgcn_raw_buffer_load_b128(r, voff, soff, AUX_NT); }
__device__ __forceinline__ u32x4 ldsc(rsrc_t r, int voff, int soff) { return __builtin_amdgcn_raw_buffer_load_b128(r, voff, soff, AUX_SC1); }
__device__ __forceinline__ rsrc_t mkr(const void* p, int bytes) { return __builtin_amdgcn_make_buffer_rsrc((void*)p, 0, bytes, 0x00020000); }

struct Tags { u32 t[4]; };
__device__ __forceinline__ int win(int o) { return o + (o >= 384 ? 128 : 0); }
__device__ __forceinline__ void publish(rsrc_t r, int voff, f32x4 v, const Tags& tg) {
  u32x4 o;
#pragma unroll
  for (int i = 0; i < 4; ++i) o[i] = ((asu(v[i]) + 0x800u) & 0xfffff000u) | tg.t[i];
  __builtin_amdgcn_raw_buffer_store_b128(o, r, voff, 0, AUX_SC1);
}
__device__ __forceinline__ void strip(u32x4& m, const Tags& tg) {
#pragma unroll
  for (int i = 0; i < 4; ++i) m[i] ^= tg.t[i];
}
__device__ __forceinline__ u32 or4(const u32x4& m) { return (m[0] | m[1]) | (m[2] | m[3]); }

constexpr int P_STEP16 = 64, P_LEAD = 24;
struct Params {
  const u16* x; const u16* kc; const u16* vc; const u16* wq; const u16* wo;
  const u16* wg; const u16* wu; const u16* wd;
  u16* out; char* ws;
};
#define PACE(pos) do { PIN(); const int due_ = (((pos) - P_LEAD) * P_STEP16) >> 4; while ((int)((u32)__builtin_amdgcn_s_memrealtime() - tz) < due_) __builtin_amdgcn_s_sleep(1); PIN(); } while (0)

template <int STR> __device__ __forceinline__ void store64(f32x4 (&d)[8], float* red, int g, int c) {
  f32x4 s[4];
#pragma unroll
  for (int i = 0; i < 4; ++i)
#pragma unroll
    for (int r = 0; r < 4; ++r) {
      float a = d[i][r], b = d[i + 4][r];
      swap32(a, b);
      s[r][i] = a + dpp<DPP_ROR8>(b);
    }
  if (c < 8) {
#pragma unroll
    for (int r = 0; r < 4; ++r) *(f32x4*)(red + (4 * (g & 1) + r) * STR + 8 * c + 4 * (g >> 1)) = s[r];
  }
}
__device__ __forceinline__ void store_acc(f32x4 (&d)[8], char* red, int g, int c) {
  if (g < 2) {
#pragma unroll
    for (int i = 0; i < 8; ++i) *(f32x4*)(red + g * R_GS + i * R_IS + c * 16) = d[i];
  }
}
__device__ __forceinline__ void tr_pair(const u32x4* w, u32x4 (&f)[8], int jj) {
#pragma unroll
  for (int i = 0; i < 8; ++i) {
    const u32 lo = w[2 * jj][i >> 1], hi = w[2 * jj + 1][i >> 1];
    f[i][jj] = (i & 1) ? perm_hi(lo, hi) : perm_lo(lo, hi);
  }
}
__device__ __forceinline__ void merge_stats(float m, float l, float& M, float& L) {
  M = fmaxf(m, dpp<DPP_ROR8>(m));
  M = max32(max16(M));
  L = l * __builtin_amdgcn_exp2f((m - M) * L2E);
  L += dpp<DPP_ROR8>(L);
  L = add32(add16(L));
}

#define ARRIVE(i) do { PIN(); asm volatile("s_mov_b64 exec, 1\n\tds_add_u32 %0, %1\n\ts_mov_b64 exec, -1" :: "v"(lcnt + 4 * (i)), "v"(1u) : "memory"); PIN(); } while (0)
#define PINV(a, n) do { _Pragma("unroll") for (int i_ = 0; i_ < (n); ++i_) asm volatile("" : "+v"((a)[i_])); } while (0)
#define LWAIT(i, n) do { PIN(); u32 it_ = 0; \
    while (__builtin_amdgcn_readfirstlane(__hip_atomic_load((u32*)(lds + L_CNT) + (i), __ATOMIC_RELAXED, __HIP_MEMORY_SCOPE_WORKGROUP)) < (u32)(n) && ++it_ < (1u << 22)) __builtin_amdgcn_s_sleep(1); \
    PIN(); } while (0)
#define PRIO(n) do { __builtin_amdgcn_s_setprio(n); PIN(); } while (0)
#define MKTAGS() Tags tg; { const u32 e_ = __builtin_amdgcn_readfirstlane(ep) + 1u; _Pragma("unroll") for (int i_ = 0; i_ < 4; ++i_) tg.t[i_] = (e_ + 0x4d5u * i_) & 0xfffu; }
#define RETRY(m, n, addr) do { u32 bad_ = 0; \
    _Pragma("unroll") for (int k = 0; k < (n); ++k) { strip(m[k], tg); bad_ |= or4(m[k]); } \
    if (__builtin_expect(__builtin_amdgcn_ballot_w64((bad_ & 0xfffu) != 0) != 0, 0)) { u32 spin_ = 0; do { asm volatile("; retry" ::: "memory"); \
    bool st_[n]; \
    _Pragma("unroll") for (int k = 0; k < (n); ++k) st_[k] = (or4(m[k]) & 0xfffu) != 0; \
    _Pragma("unroll") for (int k = 0; k < (n); ++k) if (st_[k]) m[k] = ldsc(rws, (addr), 0); \
    PIN(); bad_ = 0; \
    _Pragma("unroll") for (int k = 0; k < (n); ++k) { if (st_[k]) strip(m[k], tg); bad_ |= or4(m[k]); } \
    } while (__builtin_amdgcn_ballot_w64((bad_ & 0xfffu) != 0) != 0 && ++spin_ < SPIN_CAP); TS_SET(21, tsv[21] + spin_ + 1); } } while (0)

__global__ void __launch_bounds__(NT, 4) fused_kernel(Params p) {
  __shared__ __attribute__((aligned(16))) char lds[L_END];
  const int tid = threadIdx.x, lane = tid & 63, wave = __builtin_amdgcn_readfirstlane(tid >> 6);
  const int g = lane >> 4, c = lane & 15, hi = lane >> 5;
  const int wg = blockIdx.x, grp = (wg & 7) * 4 + (wg >> 6), sub = (wg >> 3) & 7;
  const int ks4 = wg & 3, jt = wg >> 2;
  const int cgq = jt >= 56 ? 8 * (jt - 56) + 3 : 8 * (jt / 7) + (jt % 7) + ((jt % 7) >= 3);
  const int mg = jt < 48 ? jt : 48;
  const int cgn = 8 * (mg / 7) + (mg % 7) + ((mg % 7) >= 3);
  const bool has_g = jt < 55 || jt == 62, has_u = jt < 48 || (jt >= 55 && jt != 62);
  const int cgg = (jt >= 48 && jt < 55) ? 8 * (jt - 48) + 3 : cgn, cgu = (jt >= 55 && jt < 62) ? 8 * (jt - 55) + 3 : cgn;
  const int gw = (wg >> 5) * 4 + ks4, sO = (wg & 4) + (((wg >> 3) + 1) & 3), sD = (wg & 4) + (((wg >> 3) + 3) & 3);
  const int w = wave & 7, wv = wave & 3;
  const rsrc_t rws = mkr(p.ws, WS_END);
  const u32 lcnt = (u32)(uintptr_t)(__attribute__((address_space(3))) char*)lds + L_CNT;
  TS_DECL;
  TS(0);
  const u32 tz = (u32)__builtin_amdgcn_s_memrealtime();
  const u32 ep = __builtin_amdgcn_raw_buffer_load_b32(rws, 0, 0, AUX_SC1);
  const size_t kvoff = ((size_t)grp * SK + sub * 512 + w * 64) * DH;
  if (wave < 8) {
    PRIO(3);
    const u32x4 xa = *(const u32x4*)(p.x + w * DM + lane * 8);
    const u32x4 xb = *(const u32x4*)(p.x + w * DM + 512 + lane * 8);
    const int oidx = (4 * ((wg >> 4) & 1) + g) * DM + (wg >> 5) * 128 + (wg & 15) * 8 + (c & 7);
    const u32 xfin = p.x[oidx];
    PIN();
    u32x4 wqr[4];
    {
      const rsrc_t r = mkr(p.wq + (size_t)(ks4 * 256 + w * 32) * HQD + cgq * 64, 32 * HQD * 2);
      const int vo = (16 * (c >> 3) + 4 * g) * (HQD * 2) + (c & 7) * 16;
#pragma unroll
      for (int j = 0; j < 4; ++j) wqr[j] = ldnt(r, vo, j * HQD * 2);
    }
    if (tid < 32) ((u32*)(lds + L_CNT))[tid] = 0;
    BAR();
    {
      float v[16];
#pragma unroll
      for (int i = 0; i < 4; ++i) { v[2 * i] = bf_lo(xa[i]); v[2 * i + 1] = bf_hi(xa[i]); v[8 + 2 * i] = bf_lo(xb[i]); v[9 + 2 * i] = bf_hi(xb[i]); }
      float s = 0.f;
#pragma unroll
      for (int i = 0; i < 16; ++i) s += v[i];
      const float mean = wave_sum(s) * (1.f / DM);
      float q = 0.f;
#pragma unroll
      for (int i = 0; i < 16; ++i) { const float d = v[i] - mean; q += d * d; }
      const float var = rbf(wave_sum(q) * (1.f / DM));
      const float mb = rbf(mean);
      const float rs = rcp(rbf(__builtin_amdgcn_sqrtf(rbf(var + 1e-5f))));
      const u32x4 xs = (ks4 & 2) ? xb : xa;
      u32x4 o;
#pragma unroll
      for (int i = 0; i < 4; ++i) {
        const u32 d = pack_bf(bf_lo(xs[i]) - mb, bf_hi(xs[i]) - mb);
        o[i] = pack_bf(bf_lo(d) * rs, bf_hi(d) * rs);
      }
      if (hi == (ks4 & 1)) *(u32x4*)(lds + L_XN + (w * XN_STR + (lane & 31) * 8) * 2) = o;
    }
    ARRIVE(C_XN);
    TS(1);
    MKTAGS();
    u32x2 fq[8];
#pragma unroll
    for (int i = 0; i < 8; ++i)
#pragma unroll
      for (int jj = 0; jj < 2; ++jj) {
        const u32 lo = wqr[2 * jj][i >> 1], hh = wqr[2 * jj + 1][i >> 1];
        fq[i][jj] = (i & 1) ? perm_hi(lo, hh) : perm_lo(lo, hh);
      }
    PINV(fq, 8);
    LWAIT(C_XN, 8);
    {
      const u32x2 a = *(const u32x2*)(lds + L_XN + ((c & 7) * XN_STR + w * 32 + 16 * (c >> 3) + 4 * g) * 2);
      f32x4 d[8];
#pragma unroll
      for (int i = 0; i < 8; ++i) d[i] = mfma16(a, fq[i], f32x4{0, 0, 0, 0});
      store64<64>(d, (float*)(lds + L_RQ) + w * 512, g, c);
    }
    ARRIVE(C_RQ);
    TS(2);
    LWAIT(C_RQ, 8);
    {
      const float* rq = (const float*)(lds + L_RQ) + g * 1024 + w * 64 + c * 4;
      f32x4 s = *(const f32x4*)rq + *(const f32x4*)(rq + 512);
#pragma unroll
      for (int i = 0; i < 4; ++i) s[i] = add32(add16(s[i]));
      publish(rws, lane < 16 ? WS_Q + ((w * 32 + (cgq >> 1)) * 2 + (ks4 >> 1)) * 2048 + win((ks4 & 1) * 512 + (cgq & 1) * 256 + c * 16) : OOB, s, tg);
    }
    TS(3);
    PRIO(1);
    const rsrc_t rv = mkr(p.vc + kvoff, 64 * DH * 2);
    u32x4 gr[8], vr[16];
    {
      const bool has_gu = w < 4 ? has_g : has_u;
      const rsrc_t r = mkr((w < 4 ? p.wg : p.wu) + (size_t)(ks4 * 256 + wv * 64) * FF + (has_gu ? (w < 4 ? cgg : cgu) * 64 : 0), has_gu ? 64 * FF * 2 : 0);
      const int vo = (32 * (c >> 3) + 8 * g) * (FF * 2) + (c & 7) * 16;
#pragma unroll
      for (int j = 0; j < 8; ++j) { PACE(168 + j * 7 + w); gr[j] = ldnt(r, vo, j * FF * 2); }
    }
    PRIO(0);
#pragma unroll
    for (int n = 0; n < 8; ++n) { PACE(224 + n * 8 + w); vr[n] = ldnt(rv, lane * 16, n * 1024); }
    PIN();
    TS(4);
    PRIO(3);
    {
      const u32x4 a = *(const u32x4*)(lds + L_XN + ((c & 7) * XN_STR + wv * 64 + 32 * (c >> 3) + 8 * g) * 2);
      u32x4 f[8];
#pragma unroll
      for (int jj = 0; jj < 4; ++jj) tr_pair(gr, f, jj);
      f32x4 d[8];
#pragma unroll
      for (int i = 0; i < 8; ++i) d[i] = mfma32(a, f[i], f32x4{0, 0, 0, 0});
      store64<RG_STR>(d, (float*)(lds + L_RG) + w * 8 * RG_STR, g, c);
    }
    ARRIVE(C_RG);
    TS(5);
    PRIO(0);
#pragma unroll
    for (int n = 8; n < 16; ++n) { PACE(224 + n * 8 + w); vr[n] = ldnt(rv, lane * 16, n * 1024); }
    ARRIVE(C_VI);
    TS(6);
    PRIO(3);
    u32x4 vt[8];
#pragma unroll
    for (int jj = 0; jj < 4; ++jj) tr_pair(vr, vt, jj);
    PINV(vt, 8);
    LWAIT(C_P + w, 1);
    TS(7);
    f32x4 acc[8];
    {
      const u32x4 pf = *(const u32x4*)(lds + L_P + w * 1024 + lane * 16);
#pragma unroll
      for (int i = 0; i < 8; ++i) acc[i] = mfma32(pf, vt[i], f32x4{0, 0, 0, 0});
    }
    PIN();
    TS(8);
    const u32x4 pf1 = *(const u32x4*)(lds + L_P + w * 1024 + (lane ^ 8) * 16);
#pragma unroll
    for (int jj = 0; jj < 4; ++jj) { tr_pair(vr + 8, vt, jj); PIN(); }
#pragma unroll
    for (int i = 0; i < 8; ++i) acc[i] = mfma32(pf1, vt[i], acc[i]);
    store_acc(acc, lds + L_RO + w * R_WS, g, c);
    ARRIVE(C_RO);
    TS(9);
    LWAIT(C_F, 1);
    LWAIT(C_RO, 8);
    TS(10);
    {
      const int g2 = w >> 2, i = lane & 7, cl = (lane >> 3) & 3;
      const char* ro = lds + L_RO + hi * 4 * R_WS + g2 * R_GS + i * R_IS + (wv * 4 + cl) * 16;
      const char* fp = lds + L_F + hi * 128 + g2 * 16;
      f32x4 o = *(const f32x4*)ro * *(const f32x4*)fp;
#pragma unroll
      for (int k = 1; k < 4; ++k) o += *(const f32x4*)(ro + k * R_WS) * *(const f32x4*)(fp + k * 32);
#pragma unroll
      for (int r = 0; r < 4; ++r) o[r] = add32(o[r]);
      publish(rws, hi ? OOB : WS_O + ((((grp & 3) * 2 + g2) * 4 + wv) * 4 + cl) * 16384 + sub * 2048 + win((grp >> 2) * 128) + i * 16, o, tg);
    }
    ARRIVE(C_M4);
    TS(11);
    if (w == 0) {
      LWAIT(C_S5, 4);
      TS(12);
      u32x4 ym[4], sm[4];
      const int foff = wg * 4096 + lane * 16;
#pragma unroll
      for (int k = 0; k < 4; ++k) ym[k] = ldsc(rws, WS_FY + foff, k * 1024);
#pragma unroll
      for (int k = 0; k < 4; ++k) sm[k] = ldsc(rws, WS_FS + foff, k * 1024);
      RETRY(ym, 4, WS_FY + foff + k * 1024);
      float z[2];
      {
        float v[4];
#pragma unroll
        for (int i = 0; i < 4; ++i) v[i] = (asf(ym[0][i]) + asf(ym[1][i])) + (asf(ym[2][i]) + asf(ym[3][i]));
        swap32(v[0], v[2]); swap32(v[1], v[3]);
        v[0] += v[2]; v[1] += v[3];
        swap16(v[0], v[1]);
        float t = v[0] + v[1];
        t = rbf(t + dpp<DPP_ROR8>(t));
        z[0] = rbf(t * sigm(t));
      }
      TS(13);
      RETRY(sm, 4, WS_FS + foff + k * 1024);
      TS(14);
      {
        float v[4];
#pragma unroll
        for (int i = 0; i < 4; ++i) v[i] = (asf(sm[0][i]) + asf(sm[1][i])) + (asf(sm[2][i]) + asf(sm[3][i]));
        swap32(v[0], v[2]); swap32(v[1], v[3]);
        v[0] += v[2]; v[1] += v[3];
        swap16(v[0], v[1]);
        float t = v[0] + v[1];
        z[1] = rbf(t + dpp<DPP_ROR8>(t));
      }
      const float r = bf_lo(xfin) + rbf(z[1] * z[0]);
      if (c < 8) p.out[oidx] = (u16)pack_bf(r, r);
      if (wg == 0 && lane == 0) __builtin_amdgcn_raw_buffer_store_b32(__builtin_amdgcn_readfirstlane(ep) + 1u, rws, 0, 0, AUX_SC1);
      TS(15);
    }
  } else {
    asm volatile("s_barrier" ::: "memory");
    PRIO(2);
    const rsrc_t rk = mkr(p.kc + kvoff, 64 * DH * 2);
    const int kvo = (4 * (c & 3) + ((c >> 2) & 1)) * 256 + (4 * (c >> 3) + g) * 16;
    u32x4 kr[16];
#pragma unroll
    for (int n = 0; n < 16; ++n) { if (!(n & 1)) PACE(32 + n * 8 + w * 2); kr[n] = ldnt(rk, kvo, (16 * (n >> 2) + 2 * ((n >> 1) & 1)) * 256 + (n & 1) * 128); }
    PIN();
    TS(1);
    PRIO(3);
    if (w < 4) { USE4(kr[15]); PIN(); }
    u32x4 qm[2];
    const int qoff = WS_Q + ((grp >> 2) * 32 + (grp & 3) * 8 + w) * 4096 + win(lane * 16);
    qm[0] = ldsc(rws, qoff, 0);
    qm[1] = ldsc(rws, qoff, 2048);
    PIN();
    MKTAGS();
    RETRY(qm, 2, qoff + k * 2048);
    TS(2);
    {
      float q0 = asf(qm[0][0]) + asf(qm[1][0]), q1 = asf(qm[0][1]) + asf(qm[1][1]);
      float q2 = asf(qm[0][2]) + asf(qm[1][2]), q3 = asf(qm[0][3]) + asf(qm[1][3]);
      swap32(q0, q2); swap32(q1, q3);
      *(u32*)(lds + L_QS + (w * A_STR + (lane & 31) * 4 + 2 * hi) * 2) = pack_bf(q0 + q2, q1 + q3);
    }
    ARRIVE(C_QS);
    LWAIT(C_QS, 8);
    TS(3);
    {
      const u32x4 q0 = *(const u32x4*)(lds + L_QS + ((c & 7) * A_STR + 32 * (c >> 3) + 8 * g) * 2);
      const u32x4 q1 = *(const u32x4*)(lds + L_QS + ((c & 7) * A_STR + 64 + 32 * (c >> 3) + 8 * g) * 2);
      float sc[8];
#pragma unroll
      for (int e = 0; e < 2; ++e) {
        f32x4 t[4];
#pragma unroll
        for (int ua = 0; ua < 4; ++ua) t[ua] = mfma32(kr[(ua >> 1) * 8 + e * 4 + (ua & 1) * 2], q0, f32x4{0, 0, 0, 0});
#pragma unroll
        for (int ua = 0; ua < 4; ++ua) t[ua] = mfma32(kr[(ua >> 1) * 8 + e * 4 + (ua & 1) * 2 + 1], q1, t[ua]);
#pragma unroll
        for (int r = 0; r < 4; ++r) {
          float a = t[0][r], b = t[1][r], a2 = t[2][r], b2 = t[3][r];
          swap32(a, b); swap32(a2, b2);
          sc[e * 4 + r] = ror8_merge<0x3>(b2, b) + ror8_merge<0xc>(a, a2);
        }
      }
      float mx = -INFINITY;
#pragma unroll
      for (int i = 0; i < 4; ++i) {
        const u32 s1 = pack_bf(sc[2 * i], sc[2 * i + 1]);
        const u32 s2 = pack_bf(bf_lo(s1) * 0.08838834764831845f, bf_hi(s1) * 0.08838834764831845f);
        sc[2 * i] = bf_lo(s2); sc[2 * i + 1] = bf_hi(s2);
        mx = fmaxf(mx, fmaxf(sc[2 * i], sc[2 * i + 1]));
      }
      mx = fmaxf(mx, dpp<DPP_ROR8>(mx));
      const float mw = max32(max16(mx));
      float l = 0.f;
      u32x4 pf;
#pragma unroll
      for (int i = 0; i < 4; ++i) {
        const float e0 = __builtin_amdgcn_exp2f((sc[2 * i] - mw) * L2E), e1 = __builtin_amdgcn_exp2f((sc[2 * i + 1] - mw) * L2E);
        l += e0 + e1;
        pf[i] = pack_bf(e0, e1);
      }
      *(u32x4*)(lds + L_P + w * 1024 + lane * 16) = pf;
      ARRIVE(C_P + w);
      l += dpp<DPP_ROR8>(l);
      l = add32(add16(l));
      if (lane < 8) *(float2*)(lds + L_ST + (w * 8 + lane) * 8) = make_float2(mw, l);
    }
    ARRIVE(C_ST);
    TS(4);
    u32x4 wr[8], wt[8];
    if (w >= 4) {
      LWAIT(C_RG, 8);
      TS(5);
      {
        const float* rg = (const float*)(lds + L_RG) + ((lane >> 3) & 1) * 4 * 8 * RG_STR + (lane & 7) * RG_STR + wv * 16 + ((lane >> 4) & 1) * 8 + hi * 4;
        f32x4 s = *(const f32x4*)rg;
#pragma unroll
        for (int k = 1; k < 4; ++k) s += *(const f32x4*)(rg + k * 8 * RG_STR);
        publish(rws, ((lane & 8) ? has_u : has_g) ? WS_GU + (((lane & 8) ? cgu : cgg) * 4 + wv) * 8192 + ks4 * 2048 + win(lane * 16) : OOB, s, tg);
      }
      TS(6);
      LWAIT(C_VI, 8);
      TS(7);
      u32x4 gm[8];
      const int gbase = WS_GU + (gw * 7 + 2 * wv + (wv == 3 ? 0 : hi)) * 8192;
      const int goff0 = gbase + win((g & 1) * 256 + c * 16), goff1 = gbase + 640 + (g & 1) * 256 + c * 16;
#pragma unroll
      for (int k = 0; k < 8; ++k) gm[k] = ldsc(rws, (k & 1) ? goff1 : goff0, (k >> 1) * 2048);
      PRIO(0);
      const rsrc_t rw = mkr(p.wd + (size_t)(gw * 112 + 32 * wv) * DM + sD * 128, (wv == 3 ? 16 : 32) * DM * 2 - sD * 256);
#pragma unroll
      for (int j = 0; j < 8; ++j) { PACE(364 + j * 7 / 2 + wv); wr[j] = ldnt(rw, 8 * g * (DM * 2) + c * 16, j * DM * 2); }
      ARRIVE(C_DI);
      PRIO(3);
      TS(8);
      RETRY(gm, 8, ((k & 1) ? goff1 : goff0) + (k >> 1) * 2048);
      TS(9);
      u32x4 a;
#pragma unroll
      for (int i = 0; i < 4; ++i) {
        const int qp = i >> 1, e = (i & 1) * 2;
        const float v0 = (asf(gm[qp][e]) + asf(gm[2 + qp][e])) + (asf(gm[4 + qp][e]) + asf(gm[6 + qp][e]));
        const float v1 = (asf(gm[qp][e + 1]) + asf(gm[2 + qp][e + 1])) + (asf(gm[4 + qp][e + 1]) + asf(gm[6 + qp][e + 1]));
        const u32 gg = pack_bf(v0, v1);
        const u32 uu = asu(dpp<DPP_ROR8>(asf(gg)));
        const u32 sg = pack_bf(sigm(bf_lo(gg)), sigm(bf_hi(gg)));
        a[i] = pack_bf(bf_lo(sg) * bf_lo(uu), bf_hi(sg) * bf_hi(uu));
        if (wv == 3 && hi) a[i] = 0;
      }
      PINV(a, 4);
      TS(10);
#pragma unroll
      for (int jj = 0; jj < 4; ++jj) { tr_pair(wr, wt, jj); PIN(); }
      f32x4 d[8];
#pragma unroll
      for (int i = 0; i < 8; ++i) d[i] = mfma32(a, wt[i], f32x4{0, 0, 0, 0});
      store_acc(d, lds + L_RY + wv * R_WS, g, c);
      ARRIVE(C_Y);
      TS(11);
      LWAIT(C_Y, 4);
      {
        const int t = wv * 64 + lane;
        const char* rf = lds + L_RY + (t >> 7) * R_GS + (t & 7) * R_IS + ((t >> 3) & 15) * 16;
        f32x4 s = *(const f32x4*)rf;
#pragma unroll
        for (int k = 1; k < 4; ++k) s += *(const f32x4*)(rf + k * R_WS);
        publish(rws, WS_FY + (sD * 32 + (t >> 3)) * 4096 + gw * 128 + (t & 7) * 16, s, tg);
      }
      TS(12);
    } else {
      if (w == 0) {
        LWAIT(C_ST, 8);
        const float2 ml = *(const float2*)(lds + L_ST + lane * 8);
        float M, L;
        merge_stats(ml.x, ml.y, M, L);
        const float Lh = asf((asu(L) + 0x800u) & 0xfffff000u);
        publish(rws, lane < 8 ? WS_ST + grp * 2048 + win(sub * 128) + lane * 16 : OOB, f32x4{M, Lh, L - Lh, 0.f}, tg);
        TS(5);
        LWAIT(C_RG, 8);
        u32x4 sm[1];
        const int stoff = WS_ST + grp * 2048 + win(lane * 16);
        sm[0] = ldsc(rws, stoff, 0);
        RETRY(sm, 1, stoff);
        merge_stats(asf(sm[0][0]), asf(sm[0][1]) + asf(sm[0][2]), M, L);
        *(float*)(lds + L_F + lane * 4) = __builtin_amdgcn_exp2f((ml.x - M) * L2E) * rcp(L);
        ARRIVE(C_F);
        TS(6);
      }
      LWAIT(C_DI, 4);
      TS(7);
      PRIO(0);
      const rsrc_t rw = mkr(p.wo + (size_t)((gw >> 2) * 512 + (gw & 3) * 32 + 8 * wv) * DM + sO * 128, 400 * DM * 2);
#pragma unroll
      for (int j = 0; j < 8; ++j) { PACE(392 + j * 4 + wv); wr[j] = ldnt(rw, 2 * g * (DM * 2) + c * 16, ((j & 3) * 128 + (j >> 2)) * DM * 2); }
      PIN();
      PRIO(3);
      TS(8);
      LWAIT(C_M4, 8);
      TS(9);
      u32x4 om[8];
      const int ooff = WS_O + (gw * 4 + wv) * 16384 + win((c & 7) * 128) + (2 * g + (c >> 3)) * 16;
#pragma unroll
      for (int k = 0; k < 8; ++k) om[k] = ldsc(rws, ooff, k * 2048);
      PIN();
#pragma unroll
      for (int jj = 0; jj < 4; ++jj) { tr_pair(wr, wt, jj); PIN(); }
      PINV(wt, 8);
      TS(10);
      RETRY(om, 8, ooff + k * 2048);
      TS(11);
      u32x4 a;
      {
        float v[4];
#pragma unroll
        for (int i = 0; i < 4; ++i)
          v[i] = ((asf(om[0][i]) + asf(om[1][i])) + (asf(om[2][i]) + asf(om[3][i]))) + ((asf(om[4][i]) + asf(om[5][i])) + (asf(om[6][i]) + asf(om[7][i])));
        a[0] = pack_bf(v[0], v[1]); a[1] = pack_bf(v[2], v[3]);
        a[2] = asu(dpp<DPP_ROR8>(asf(a[0]))); a[3] = asu(dpp<DPP_ROR8>(asf(a[1])));
      }
      f32x4 d[8];
#pragma unroll
      for (int i = 0; i < 8; ++i) d[i] = mfma32(a, wt[i], f32x4{0, 0, 0, 0});
      store_acc(d, lds + L_RS + wv * R_WS, g, c);
      ARRIVE(C_S);
      TS(12);
      LWAIT(C_S, 4);
      {
        const int t = wv * 64 + lane;
        const char* rf = lds + L_RS + (t >> 7) * R_GS + (t & 7) * R_IS + ((t >> 3) & 15) * 16;
        f32x4 s = *(const f32x4*)rf;
#pragma unroll
        for (int k = 1; k < 4; ++k) s += *(const f32x4*)(rf + k * R_WS);
        publish(rws, WS_FS + (sO * 32 + (t >> 3)) * 4096 + gw * 128 + (t & 7) * 16, s, tg);
      }
      ARRIVE(C_S5);
      TS(13);
    }
  }
  TS_FLUSH();
}

extern "C" void kernel_launch(void* const* d_in, const int* in_sizes, int n_in, void* d_out, int out_size, void* d_ws, size_t ws_size,
                              hipStream_t stream) {
  Params p;
  p.x = (const u16*)d_in[0]; p.kc = (const u16*)d_in[1]; p.vc = (const u16*)d_in[2]; p.wq = (const u16*)d_in[3]; p.wo = (const u16*)d_in[4];
  p.wg = (const u16*)d_in[6]; p.wu = (const u16*)d_in[7]; p.wd = (const u16*)d_in[8];
  p.out = (u16*)d_out; p.ws = (char*)d_ws;
  fused_kernel<<<NWG, NT, 0, stream>>>(p);
}
```

```cpp
#define ENC_W 0
#define ENC_I 0
#include <hip/hip_runtime.h>
#include <hip/hip_bf16.h>
#include <stdint.h>

typedef unsigned int u32;
typedef unsigned short u16;
typedef u32 u32x4 __attribute__((ext_vector_type(4)));
typedef u32 u32x2 __attribute__((ext_vector_type(2)));
typedef float f32x4 __attribute__((ext_vector_type(4)));
typedef __bf16 bf16x8 __attribute__((ext_vector_type(8)));
typedef short s16x4 __attribute__((ext_vector_type(4)));
typedef __amdgpu_buffer_rsrc_t rsrc_t;

constexpr int DM = 1024, HQD = 4096, SK = 4096, DH = 128, FF = 3584;
constexpr int NWG = 256, NT = 1024;
#define TS_WAVES 16

constexpr int WS_Q = 4096;
constexpr int WS_GU = WS_Q + 8 * 32 * 4 * 1024;
constexpr int WS_O = WS_GU + 224 * 8192;
constexpr int WS_ST = WS_O + 32 * 65536;
constexpr int WS_FS = WS_ST + 32 * 2048;
constexpr int WS_FY = WS_FS + 256 * 4096;
constexpr int WS_END = WS_FY + 256 * 4096;
constexpr int AUX_NT = 2, AUX_SC1 = 16;
constexpr u32 SPIN_CAP = 20000;

constexpr int XN_STR = 264;
constexpr int A_STR = 136;
constexpr int RG_STR = 68;
constexpr int R_IS = 288, R_GS = 8 * R_IS, R_WS = 2 * R_GS;
constexpr int L_CNT = 0;
constexpr int L_XN = 128;
constexpr int L_QS = L_XN + 8 * XN_STR * 2;
constexpr int L_ST = L_QS + 8 * A_STR * 2;
constexpr int L_F = L_ST + 512;
constexpr int L_P = L_F + 256;
constexpr int L_RQ = L_P + 8192;
constexpr int L_RG = L_RQ + 8 * 8 * 64 * 4;
constexpr int L_RO = L_RG + 8 * 8 * RG_STR * 4;
constexpr int L_RS = L_RO + 8 * R_WS;
constexpr int L_RY = L_RS + 4 * R_WS;
constexpr int L_END = L_RY + 4 * R_WS;
enum { C_XN = 0, C_RQ, C_QS, C_ST, C_RG, C_VI, C_DI, C_F, C_RO, C_M4, C_Y, C_S, C_S5, C_P = 16 };

#define PIN() do { asm volatile("" ::: "memory"); __builtin_amdgcn_sched_barrier(0); } while (0)
#define USE4(x) asm volatile("" ::"v"(x))
#define BAR() do { asm volatile("s_waitcnt lgkmcnt(0)\n\ts_barrier" ::: "memory"); __builtin_amdgcn_sched_barrier(0); } while (0)

#define TS(i) do { if (wave == ENC_W && (i) == ENC_I) { PIN(); *(u32*)(lds + L_CNT + 124) = (u32)__builtin_amdgcn_s_memrealtime() - tz; PIN(); } } while (0)
#define TS_DECL
#define TS_SET(i, v) do {} while (0)
#define TS_FLUSH()

__device__ __forceinline__ f32x4 mfma32(u32x4 a, u32x4 b, f32x4 c) {
  return __builtin_amdgcn_mfma_f32_16x16x32_bf16(__builtin_bit_cast(bf16x8, a), __builtin_bit_cast(bf16x8, b), c, 0, 0, 0);
}
__device__ __forceinline__ f32x4 mfma16(u32x2 a, u32x2 b, f32x4 c) {
  return __builtin_amdgcn_mfma_f32_16x16x16bf16_1k(__builtin_bit_cast(s16x4, a), __builtin_bit_cast(s16x4, b), c, 0, 0, 0);
}
__device__ __forceinline__ float asf(u32 x) { return __builtin_bit_cast(float, x); }
__device__ __forceinline__ u32 asu(float x) { return __builtin_bit_cast(u32, x); }
__device__ __forceinline__ float bf_lo(u32 x) { return asf(x << 16); }
__device__ __forceinline__ float bf_hi(u32 x) { return asf(x & 0xffff0000u); }
__device__ __forceinline__ u32 pack_bf(float lo, float hi) {
  typedef __bf16 bf16x2 __attribute__((ext_vector_type(2)));
  typedef float f32x2 __attribute__((ext_vector_type(2)));
  f32x2 v = {lo, hi};
  return __builtin_bit_cast(u32, __builtin_convertvector(v, bf16x2));
}
__device__ __forceinline__ float rcp(float x) { return __builtin_amdgcn_rcpf(x); }
constexpr float L2E = 1.4426950408889634f;
constexpr int OOB = 0x7ffffff0;
__device__ __forceinline__ float sigm(float x) { return rcp(1.f + __builtin_amdgcn_exp2f(-L2E * x)); }
__device__ __forceinline__ float rbf(float x) { return bf_lo(pack_bf(x, x)); }
__device__ __forceinline__ u32 perm_lo(u32 a, u32 b) { return __builtin_amdgcn_perm(b, a, 0x05040100u); }
__device__ __forceinline__ u32 perm_hi(u32 a, u32 b) { return __builtin_amdgcn_perm(b, a, 0x07060302u); }
template <int CTRL> __device__ __forceinline__ float dpp(float x) {
  return asf(__builtin_amdgcn_mov_dpp(asu(x), CTRL, 0xf, 0xf, true));
}
template <int BANK> __device__ __forceinline__ float ror8_merge(float old, float src) {
  return asf(__builtin_amdgcn_update_dpp(asu(old), asu(src), 0x128, 0xf, BANK, false));
}
constexpr int DPP_X1 = 0xB1, DPP_X2 = 0x4E, DPP_HMIR = 0x141, DPP_MIR = 0x140, DPP_ROR8 = 0x128, DPP_ROR4 = 0x124;
__device__ __forceinline__ void swap32(float& a, float& b) {
  auto r = __builtin_amdgcn_permlane32_swap(asu(a), asu(b), false, false);
  a = asf(r[0]); b = asf(r[1]);
}
__device__ __forceinline__ void swap16(float& a, float& b) {
  auto r = __builtin_amdgcn_permlane16_swap(asu(a), asu(b), false, false);
  a = asf(r[0]); b = asf(r[1]);
}
__device__ __forceinline__ float add32(float x) { float a = x, b = x; swap32(a, b); return a + b; }
__device__ __forceinline__ float add16(float x) { float a = x, b = x; swap16(a, b); return a + b; }
__device__ __forceinline__ float max32(float x) { float a = x, b = x; swap32(a, b); return fmaxf(a, b); }
__device__ __forceinline__ float max16(float x) { float a = x, b = x; swap16(a, b); return fmaxf(a, b); }
__device__ __forceinline__ float wave_sum(float x) {
  x += dpp<DPP_X1>(x); x += dpp<DPP_X2>(x); x += dpp<DPP_HMIR>(x); x += dpp<DPP_MIR>(x);
  return add32(add16(x));
}
__device__ __forceinline__ u32x4 ldnt(rsrc_t r, int voff, int soff) { return __builtin_amdgcn_raw_buffer_load_b128(r, voff, soff, AUX_NT); }
__device__ __forceinline__ u32x4 ldsc(rsrc_t r, int voff, int soff) { return __builtin_amdgcn_raw_buffer_load_b128(r, voff, soff, AUX_SC1); }
__device__ __forceinline__ rsrc_t mkr(const void* p, int bytes) { return __builtin_amdgcn_make_buffer_rsrc((void*)p, 0, bytes, 0x00020000); }

struct Tags { u32 t[4]; };
__device__ __forceinline__ int win(int o) { return o + (o >= 384 ? 128 : 0); }
__device__ __forceinline__ void publish(rsrc_t r, int voff, f32x4 v, const Tags& tg) {
  u32x4 o;
#pragma unroll
  for (int i = 0; i < 4; ++i) o[i] = ((asu(v[i]) + 0x800u) & 0xfffff000u) | tg.t[i];
  __builtin_amdgcn_raw_buffer_store_b128(o, r, voff, 0, AUX_SC1);
}
__device__ __forceinline__ void strip(u32x4& m, const Tags& tg) {
#pragma unroll
  for (int i = 0; i < 4; ++i) m[i] ^= tg.t[i];
}
__device__ __forceinline__ u32 or4(const u32x4& m) { return (m[0] | m[1]) | (m[2] | m[3]); }

constexpr int P_STEP16 = 64, P_LEAD = 24;
struct Params {
  const u16* x; const u16* kc; const u16* vc; const u16* wq; const u16* wo;
  const u16* wg; const u16* wu; const u16* wd;
  u16* out; char* ws;
};
#define PACE(pos) do { PIN(); const int due_ = (((pos) - P_LEAD) * P_STEP16) >> 4; while ((int)((u32)__builtin_amdgcn_s_memrealtime() - tz) < due_) __builtin_amdgcn_s_sleep(1); PIN(); } while (0)

template <int STR> __device__ __forceinline__ void store64(f32x4 (&d)[8], float* red, int g, int c) {
  f32x4 s[4];
#pragma unroll
  for (int i = 0; i < 4; ++i)
#pragma unroll
    for (int r = 0; r < 4; ++r) {
      float a = d[i][r], b = d[i + 4][r];
      swap32(a, b);
      s[r][i] = a + dpp<DPP_ROR8>(b);
    }
  if (c < 8) {
#pragma unroll
    for (int r = 0; r < 4; ++r) *(f32x4*)(red + (4 * (g & 1) + r) * STR + 8 * c + 4 * (g >> 1)) = s[r];
  }
}
__device__ __forceinline__ void store_acc(f32x4 (&d)[8], char* red, int g, int c) {
  if (g < 2) {
#pragma unroll
    for (int i = 0; i < 8; ++i) *(f32x4*)(red + g * R_GS + i * R_IS + c * 16) = d[i];
  }
}
__device__ __forceinline__ void tr_pair(const u32x4* w, u32x4 (&f)[8], int jj) {
#pragma unroll
  for (int i = 0; i < 8; ++i) {
    const u32 lo = w[2 * jj][i >> 1], hi = w[2 * jj + 1][i >> 1];
    f[i][jj] = (i & 1) ? perm_hi(lo, hi) : perm_lo(lo, hi);
  }
}
__device__ __forceinline__ void merge_stats(float m, float l, float& M, float& L) {
  M = fmaxf(m, dpp<DPP_ROR8>(m));
  M = max32(max16(M));
  L = l * __builtin_amdgcn_exp2f((m - M) * L2E);
  L += dpp<DPP_ROR8>(L);
  L = add32(add16(L));
}

#define ARRIVE(i) do { PIN(); asm volatile("s_mov_b64 exec, 1\n\tds_add_u32 %0, %1\n\ts_mov_b64 exec, -1" :: "v"(lcnt + 4 * (i)), "v"(1u) : "memory"); PIN(); } while (0)
#define PINV(a, n) do { _Pragma("unroll") for (int i_ = 0; i_ < (n); ++i_) asm volatile("" : "+v"((a)[i_])); } while (0)
#define LWAIT(i, n) do { PIN(); u32 it_ = 0; \
    while (__builtin_amdgcn_readfirstlane(__hip_atomic_load((u32*)(lds + L_CNT) + (i), __ATOMIC_RELAXED, __HIP_MEMORY_SCOPE_WORKGROUP)) < (u32)(n) && ++it_ < (1u << 22)) __builtin_amdgcn_s_sleep(1); \
    PIN(); } while (0)
#define PRIO(n) do { __builtin_amdgcn_s_setprio(n); PIN(); } while (0)
#define MKTAGS() Tags tg; { const u32 e_ = __builtin_amdgcn_readfirstlane(ep) + 1u; _Pragma("unroll") for (int i_ = 0; i_ < 4; ++i_) tg.t[i_] = (e_ + 0x4d5u * i_) & 0xfffu; }
#define RETRY(m, n, addr) do { u32 bad_ = 0; \
    _Pragma("unroll") for (int k = 0; k < (n); ++k) { strip(m[k], tg); bad_ |= or4(m[k]); } \
    if (__builtin_expect(__builtin_amdgcn_ballot_w64((bad_ & 0xfffu) != 0) != 0, 0)) { u32 spin_ = 0; do { asm volatile("; retry" ::: "memory"); \
    bool st_[n]; \
    _Pragma("unroll") for (int k = 0; k < (n); ++k) st_[k] = (or4(m[k]) & 0xfffu) != 0; \
    _Pragma("unroll") for (int k = 0; k < (n); ++k) if (st_[k]) m[k] = ldsc(rws, (addr), 0); \
    PIN(); bad_ = 0; \
    _Pragma("unroll") for (int k = 0; k < (n); ++k) { if (st_[k]) strip(m[k], tg); bad_ |= or4(m[k]); } \
    } while (__builtin_amdgcn_ballot_w64((bad_ & 0xfffu) != 0) != 0 && ++spin_ < SPIN_CAP); TS_SET(21, tsv[21] + spin_ + 1); } } while (0)

__global__ void __launch_bounds__(NT, 4) fused_kernel(Params p) {
  __shared__ __attribute__((aligned(16))) char lds[L_END];
  const int tid = threadIdx.x, lane = tid & 63, wave = __builtin_amdgcn_readfirstlane(tid >> 6);
  const int g = lane >> 4, c = lane & 15, hi = lane >> 5;
  const int wg = blockIdx.x, grp = (wg & 7) * 4 + (wg >> 6), sub = (wg >> 3) & 7;
  const int ks4 = wg & 3, jt = wg >> 2;
  const int cgq = jt >= 56 ? 8 * (jt - 56) + 3 : 8 * (jt / 7) + (jt % 7) + ((jt % 7) >= 3);
  const int mg = jt < 48 ? jt : 48;
  const int cgn = 8 * (mg / 7) + (mg % 7) + ((mg % 7) >= 3);
  const bool has_g = jt < 55 || jt == 62, has_u = jt < 48 || (jt >= 55 && jt != 62);
  const int cgg = (jt >= 48 && jt < 55) ? 8 * (jt - 48) + 3 : cgn, cgu = (jt >= 55 && jt < 62) ? 8 * (jt - 55) + 3 : cgn;
  const int gw = (wg >> 5) * 4 + ks4, sO = (wg & 4) + (((wg >> 3) + 1) & 3), sD = (wg & 4) + (((wg >> 3) + 3) & 3);
  const int w = wave & 7, wv = wave & 3;
  const rsrc_t rws = mkr(p.ws, WS_END);
  const u32 lcnt = (u32)(uintptr_t)(__attribute__((address_space(3))) char*)lds + L_CNT;
  TS_DECL;
  const u32 tz = (u32)__builtin_amdgcn_s_memrealtime();
  const u32 ep = __builtin_amdgcn_raw_buffer_load_b32(rws, 0, 0, AUX_SC1);
  const size_t kvoff = ((size_t)grp * SK + sub * 512 + w * 64) * DH;
  if (wave < 8) {
    PRIO(3);
    const u32x4 xa = *(const u32x4*)(p.x + w * DM + lane * 8);
    const u32x4 xb = *(const u32x4*)(p.x + w * DM + 512 + lane * 8);
    const int oidx = (4 * ((wg >> 4) & 1) + g) * DM + (wg >> 5) * 128 + (wg & 15) * 8 + (c & 7);
    const u32 xfin = p.x[oidx];
    PIN();
    u32x4 wqr[4];
    {
      const rsrc_t r = mkr(p.wq + (size_t)(ks4 * 256 + w * 32) * HQD + cgq * 64, 32 * HQD * 2);
      const int vo = (16 * (c >> 3) + 4 * g) * (HQD * 2) + (c & 7) * 16;
#pragma unroll
      for (int j = 0; j < 4; ++j) wqr[j] = ldnt(r, vo, j * HQD * 2);
    }
    if (tid < 32) ((u32*)(lds + L_CNT))[tid] = 0;
    BAR();
    {
      float v[16];
#pragma unroll
      for (int i = 0; i < 4; ++i) { v[2 * i] = bf_lo(xa[i]); v[2 * i + 1] = bf_hi(xa[i]); v[8 + 2 * i] = bf_lo(xb[i]); v[9 + 2 * i] = bf_hi(xb[i]); }
      float s = 0.f;
#pragma unroll
      for (int i = 0; i < 16; ++i) s += v[i];
      const float mean = wave_sum(s) * (1.f / DM);
      float q = 0.f;
#pragma unroll
      for (int i = 0; i < 16; ++i) { const float d = v[i] - mean; q += d * d; }
      const float var = rbf(wave_sum(q) * (1.f / DM));
      const float mb = rbf(mean);
      const float rs = rcp(rbf(__builtin_amdgcn_sqrtf(rbf(var + 1e-5f))));
      const u32x4 xs = (ks4 & 2) ? xb : xa;
      u32x4 o;
#pragma unroll
      for (int i = 0; i < 4; ++i) {
        const u32 d = pack_bf(bf_lo(xs[i]) - mb, bf_hi(xs[i]) - mb);
        o[i] = pack_bf(bf_lo(d) * rs, bf_hi(d) * rs);
      }
      if (hi == (ks4 & 1)) *(u32x4*)(lds + L_XN + (w * XN_STR + (lane & 31) * 8) * 2) = o;
    }
    ARRIVE(C_XN);
    TS(1);
    MKTAGS();
    u32x2 fq[8];
#pragma unroll
    for (int i = 0; i < 8; ++i)
#pragma unroll
      for (int jj = 0; jj < 2; ++jj) {
        const u32 lo = wqr[2 * jj][i >> 1], hh = wqr[2 * jj + 1][i >> 1];
        fq[i][jj] = (i & 1) ? perm_hi(lo, hh) : perm_lo(lo, hh);
      }
    PINV(fq, 8);
    LWAIT(C_XN, 8);
    {
      const u32x2 a = *(const u32x2*)(lds + L_XN + ((c & 7) * XN_STR + w * 32 + 16 * (c >> 3) + 4 * g) * 2);
      f32x4 d[8];
#pragma unroll
      for (int i = 0; i < 8; ++i) d[i] = mfma16(a, fq[i], f32x4{0, 0, 0, 0});
      store64<64>(d, (float*)(lds + L_RQ) + w * 512, g, c);
    }
    ARRIVE(C_RQ);
    TS(2);
    LWAIT(C_RQ, 8);
    {
      const float* rq = (const float*)(lds + L_RQ) + g * 1024 + w * 64 + c * 4;
      f32x4 s = *(const f32x4*)rq + *(const f32x4*)(rq + 512);
#pragma unroll
      for (int i = 0; i < 4; ++i) s[i] = add32(add16(s[i]));
      publish(rws, lane < 16 ? WS_Q + ((w * 32 + (cgq >> 1)) * 2 + (ks4 >> 1)) * 2048 + win((ks4 & 1) * 512 + (cgq & 1) * 256 + c * 16) : OOB, s, tg);
    }
    TS(3);
    PRIO(1);
    const rsrc_t rv = mkr(p.vc + kvoff, 64 * DH * 2);
    u32x4 gr[8], vr[16];
    {
      const bool has_gu = w < 4 ? has_g : has_u;
      const rsrc_t r = mkr((w < 4 ? p.wg : p.wu) + (size_t)(ks4 * 256 + wv * 64) * FF + (has_gu ? (w < 4 ? cgg : cgu) * 64 : 0), has_gu ? 64 * FF * 2 : 0);
      const int vo = (32 * (c >> 3) + 8 * g) * (FF * 2) + (c & 7) * 16;
#pragma unroll
      for (int j = 0; j < 8; ++j) { PACE(168 + j * 7 + w); gr[j] = ldnt(r, vo, j * FF * 2); }
    }
    PRIO(0);
#pragma unroll
    for (int n = 0; n < 8; ++n) { PACE(224 + n * 8 + w); vr[n] = ldnt(rv, lane * 16, n * 1024); }
    PIN();
    TS(4);
    PRIO(3);
    {
      const u32x4 a = *(const u32x4*)(lds + L_XN + ((c & 7) * XN_STR + wv * 64 + 32 * (c >> 3) + 8 * g) * 2);
      u32x4 f[8];
#pragma unroll
      for (int jj = 0; jj < 4; ++jj) tr_pair(gr, f, jj);
      f32x4 d[8];
#pragma unroll
      for (int i = 0; i < 8; ++i) d[i] = mfma32(a, f[i], f32x4{0, 0, 0, 0});
      store64<RG_STR>(d, (float*)(lds + L_RG) + w * 8 * RG_STR, g, c);
    }
    ARRIVE(C_RG);
    TS(5);
    PRIO(0);
#pragma unroll
    for (int n = 8; n < 16; ++n) { PACE(224 + n * 8 + w); vr[n] = ldnt(rv, lane * 16, n * 1024); }
    ARRIVE(C_VI);
    TS(6);
    PRIO(3);
    u32x4 vt[8];
#pragma unroll
    for (int jj = 0; jj < 4; ++jj) tr_pair(vr, vt, jj);
    PINV(vt, 8);
    LWAIT(C_P + w, 1);
    TS(7);
    f32x4 acc[8];
    {
      const u32x4 pf = *(const u32x4*)(lds + L_P + w * 1024 + lane * 16);
#pragma unroll
      for (int i = 0; i < 8; ++i) acc[i] = mfma32(pf, vt[i], f32x4{0, 0, 0, 0});
    }
    PIN();
    TS(8);
    const u32x4 pf1 = *(const u32x4*)(lds + L_P + w * 1024 + (lane ^ 8) * 16);
#pragma unroll
    for (int jj = 0; jj < 4; ++jj) { tr_pair(vr + 8, vt, jj); PIN(); }
#pragma unroll
    for (int i = 0; i < 8; ++i) acc[i] = mfma32(pf1, vt[i], acc[i]);
    store_acc(acc, lds + L_RO + w * R_WS, g, c);
    ARRIVE(C_RO);
    TS(9);
    LWAIT(C_F, 1);
    LWAIT(C_RO, 8);
    TS(10);
    {
      const int g2 = w >> 2, i = lane & 7, cl = (lane >> 3) & 3;
      const char* ro = lds + L_RO + hi * 4 * R_WS + g2 * R_GS + i * R_IS + (wv * 4 + cl) * 16;
      const char* fp = lds + L_F + hi * 128 + g2 * 16;
      f32x4 o = *(const f32x4*)ro * *(const f32x4*)fp;
#pragma unroll
      for (int k = 1; k < 4; ++k) o += *(const f32x4*)(ro + k * R_WS) * *(const f32x4*)(fp + k * 32);
#pragma unroll
      for (int r = 0; r < 4; ++r) o[r] = add32(o[r]);
      publish(rws, hi ? OOB : WS_O + ((((grp & 3) * 2 + g2) * 4 + wv) * 4 + cl) * 16384 + sub * 2048 + win((grp >> 2) * 128) + i * 16, o, tg);
    }
    ARRIVE(C_M4);
    TS(11);
    if (w == 0) {
      LWAIT(C_S5, 4);
      TS(12);
      u32x4 ym[4], sm[4];
      const int foff = wg * 4096 + lane * 16;
#pragma unroll
      for (int k = 0; k < 4; ++k) ym[k] = ldsc(rws, WS_FY + foff, k * 1024);
#pragma unroll
      for (int k = 0; k < 4; ++k) sm[k] = ldsc(rws, WS_FS + foff, k * 1024);
      RETRY(ym, 4, WS_FY + foff + k * 1024);
      float z[2];
      {
        float v[4];
#pragma unroll
        for (int i = 0; i < 4; ++i) v[i] = (asf(ym[0][i]) + asf(ym[1][i])) + (asf(ym[2][i]) + asf(ym[3][i]));
        swap32(v[0], v[2]); swap32(v[1], v[3]);
        v[0] += v[2]; v[1] += v[3];
        swap16(v[0], v[1]);
        float t = v[0] + v[1];
        t = rbf(t + dpp<DPP_ROR8>(t));
        z[0] = rbf(t * sigm(t));
      }
      TS(13);
      RETRY(sm, 4, WS_FS + foff + k * 1024);
      TS(14);
      {
        float v[4];
#pragma unroll
        for (int i = 0; i < 4; ++i) v[i] = (asf(sm[0][i]) + asf(sm[1][i])) + (asf(sm[2][i]) + asf(sm[3][i]));
        swap32(v[0], v[2]); swap32(v[1], v[3]);
        v[0] += v[2]; v[1] += v[3];
        swap16(v[0], v[1]);
        float t = v[0] + v[1];
        z[1] = rbf(t + dpp<DPP_ROR8>(t));
      }
      const float r = bf_lo(xfin) + rbf(z[1] * z[0]);
      if (c < 8) p.out[oidx] = (u16)pack_bf(r, r);
      if (wg == 0 && lane == 0) __builtin_amdgcn_raw_buffer_store_b32(__builtin_amdgcn_readfirstlane(ep) + 1u, rws, 0, 0, AUX_SC1);
      TS(15);
      {
        PIN();
        const u32 enc = ENC_I == 0 ? 0u : __builtin_amdgcn_readfirstlane(__hip_atomic_load((u32*)(lds + L_CNT + 124), __ATOMIC_RELAXED, __HIP_MEMORY_SCOPE_WORKGROUP));
        const int due = 6000 + 4 * (int)enc;
        while ((int)((u32)__builtin_amdgcn_s_memrealtime() - tz) < due) __builtin_amdgcn_s_sleep(1);
      }
    }
  } else {
    asm volatile("s_barrier" ::: "memory");
    PRIO(2);
    const rsrc_t rk = mkr(p.kc + kvoff, 64 * DH * 2);
    const int kvo = (4 * (c & 3) + ((c >> 2) & 1)) * 256 + (4 * (c >> 3) + g) * 16;
    u32x4 kr[16];
#pragma unroll
    for (int n = 0; n < 16; ++n) { if (!(n & 1)) PACE(32 + n * 8 + w * 2); kr[n] = ldnt(rk, kvo, (16 * (n >> 2) + 2 * ((n >> 1) & 1)) * 256 + (n & 1) * 128); }
    PIN();
    TS(1);
    PRIO(3);
    if (w < 4) { USE4(kr[15]); PIN(); }
    u32x4 qm[2];
    const int qoff = WS_Q + ((grp >> 2) * 32 + (grp & 3) * 8 + w) * 4096 + win(lane * 16);
    qm[0] = ldsc(rws, qoff, 0);
    qm[1] = ldsc(rws, qoff, 2048);
    PIN();
    MKTAGS();
    RETRY(qm, 2, qoff + k * 2048);
    TS(2);
    {
      float q0 = asf(qm[0][0]) + asf(qm[1][0]), q1 = asf(qm[0][1]) + asf(qm[1][1]);
      float q2 = asf(qm[0][2]) + asf(qm[1][2]), q3 = asf(qm[0][3]) + asf(qm[1][3]);
      swap32(q0, q2); swap32(q1, q3);
      *(u32*)(lds + L_QS + (w * A_STR + (lane & 31) * 4 + 2 * hi) * 2) = pack_bf(q0 + q2, q1 + q3);
    }
    ARRIVE(C_QS);
    LWAIT(C_QS, 8);
    TS(3);
    {
      const u32x4 q0 = *(const u32x4*)(lds + L_QS + ((c & 7) * A_STR + 32 * (c >> 3) + 8 * g) * 2);
      const u32x4 q1 = *(const u32x4*)(lds + L_QS + ((c & 7) * A_STR + 64 + 32 * (c >> 3) + 8 * g) * 2);
      float sc[8];
#pragma unroll
      for (int e = 0; e < 2; ++e) {
        f32x4 t[4];
#pragma unroll
        for (int ua = 0; ua < 4; ++ua) t[ua] = mfma32(kr[(ua >> 1) * 8 + e * 4 + (ua & 1) * 2], q0, f32x4{0, 0, 0, 0});
#pragma unroll
        for (int ua = 0; ua < 4; ++ua) t[ua] = mfma32(kr[(ua >> 1) * 8 + e * 4 + (ua & 1) * 2 + 1], q1, t[ua]);
#pragma unroll
        for (int r = 0; r < 4; ++r) {
          float a = t[0][r], b = t[1][r], a2 = t[2][r], b2 = t[3][r];
          swap32(a, b); swap32(a2, b2);
          sc[e * 4 + r] = ror8_merge<0x3>(b2, b) + ror8_merge<0xc>(a, a2);
        }
      }
      float mx = -INFINITY;
#pragma unroll
      for (int i = 0; i < 4; ++i) {
        const u32 s1 = pack_bf(sc[2 * i], sc[2 * i + 1]);
        const u32 s2 = pack_bf(bf_lo(s1) * 0.08838834764831845f, bf_hi(s1) * 0.08838834764831845f);
        sc[2 * i] = bf_lo(s2); sc[2 * i + 1] = bf_hi(s2);
        mx = fmaxf(mx, fmaxf(sc[2 * i], sc[2 * i + 1]));
      }
      mx = fmaxf(mx, dpp<DPP_ROR8>(mx));
      const float mw = max32(max16(mx));
      float l = 0.f;
      u32x4 pf;
#pragma unroll
      for (int i = 0; i < 4; ++i) {
        const float e0 = __builtin_amdgcn_exp2f((sc[2 * i] - mw) * L2E), e1 = __builtin_amdgcn_exp2f((sc[2 * i + 1] - mw) * L2E);
        l += e0 + e1;
        pf[i] = pack_bf(e0, e1);
      }
      *(u32x4*)(lds + L_P + w * 1024 + lane * 16) = pf;
      ARRIVE(C_P + w);
      l += dpp<DPP_ROR8>(l);
      l = add32(add16(l));
      if (lane < 8) *(float2*)(lds + L_ST + (w * 8 + lane) * 8) = make_float2(mw, l);
    }
    ARRIVE(C_ST);
    TS(4);
    u32x4 wr[8], wt[8];
    if (w >= 4) {
      LWAIT(C_RG, 8);
      TS(5);
      {
        const float* rg = (const float*)(lds + L_RG) + ((lane >> 3) & 1) * 4 * 8 * RG_STR + (lane & 7) * RG_STR + wv * 16 + ((lane >> 4) & 1) * 8 + hi * 4;
        f32x4 s = *(const f32x4*)rg;
#pragma unroll
        for (int k = 1; k < 4; ++k) s += *(const f32x4*)(rg + k * 8 * RG_STR);
        publish(rws, ((lane & 8) ? has_u : has_g) ? WS_GU + (((lane & 8) ? cgu : cgg) * 4 + wv) * 8192 + ks4 * 2048 + win(lane * 16) : OOB, s, tg);
      }
      TS(6);
      LWAIT(C_VI, 8);
      TS(7);
      u32x4 gm[8];
      const int gbase = WS_GU + (gw * 7 + 2 * wv + (wv == 3 ? 0 : hi)) * 8192;
      const int goff0 = gbase + win((g & 1) * 256 + c * 16), goff1 = gbase + 640 + (g & 1) * 256 + c * 16;
#pragma unroll
      for (int k = 0; k < 8; ++k) gm[k] = ldsc(rws, (k & 1) ? goff1 : goff0, (k >> 1) * 2048);
      PRIO(0);
      const rsrc_t rw = mkr(p.wd + (size_t)(gw * 112 + 32 * wv) * DM + sD * 128, (wv == 3 ? 16 : 32) * DM * 2 - sD * 256);
#pragma unroll
      for (int j = 0; j < 8; ++j) { PACE(364 + j * 7 / 2 + wv); wr[j] = ldnt(rw, 8 * g * (DM * 2) + c * 16, j * DM * 2); }
      ARRIVE(C_DI);
      PRIO(3);
      TS(8);
      RETRY(gm, 8, ((k & 1) ? goff1 : goff0) + (k >> 1) * 2048);
      TS(9);
      u32x4 a;
#pragma unroll
      for (int i = 0; i < 4; ++i) {
        const int qp = i >> 1, e = (i & 1) * 2;
        const float v0 = (asf(gm[qp][e]) + asf(gm[2 + qp][e])) + (asf(gm[4 + qp][e]) + asf(gm[6 + qp][e]));
        const float v1 = (asf(gm[qp][e + 1]) + asf(gm[2 + qp][e + 1])) + (asf(gm[4 + qp][e + 1]) + asf(gm[6 + qp][e + 1]));
        const u32 gg = pack_bf(v0, v1);
        const u32 uu = asu(dpp<DPP_ROR8>(asf(gg)));
        const u32 sg = pack_bf(sigm(bf_lo(gg)), sigm(bf_hi(gg)));
        a[i] = pack_bf(bf_lo(sg) * bf_lo(uu), bf_hi(sg) * bf_hi(uu));
        if (wv == 3 && hi) a[i] = 0;
      }
      PINV(a, 4);
      TS(10);
#pragma unroll
      for (int jj = 0; jj < 4; ++jj) { tr_pair(wr, wt, jj); PIN(); }
      f32x4 d[8];
#pragma unroll
      for (int i = 0; i < 8; ++i) d[i] = mfma32(a, wt[i], f32x4{0, 0, 0, 0});
      store_acc(d, lds + L_RY + wv * R_WS, g, c);
      ARRIVE(C_Y);
      TS(11);
      LWAIT(C_Y, 4);
      {
        const int t = wv * 64 + lane;
        const char* rf = lds + L_RY + (t >> 7) * R_GS + (t & 7) * R_IS + ((t >> 3) & 15) * 16;
        f32x4 s = *(const f32x4*)rf;
#pragma unroll
        for (int k = 1; k < 4; ++k) s += *(const f32x4*)(rf + k * R_WS);
        publish(rws, WS_FY + (sD * 32 + (t >> 3)) * 4096 + gw * 128 + (t & 7) * 16, s, tg);
      }
      TS(12);
    } else {
      if (w == 0) {
        LWAIT(C_ST, 8);
        const float2 ml = *(const float2*)(lds + L_ST + lane * 8);
        float M, L;
        merge_stats(ml.x, ml.y, M, L);
        const float Lh = asf((asu(L) + 0x800u) & 0xfffff000u);
        publish(rws, lane < 8 ? WS_ST + grp * 2048 + win(sub * 128) + lane * 16 : OOB, f32x4{M, Lh, L - Lh, 0.f}, tg);
        TS(5);
        LWAIT(C_RG, 8);
        u32x4 sm[1];
        const int stoff = WS_ST + grp * 2048 + win(lane * 16);
        sm[0] = ldsc(rws, stoff, 0);
        RETRY(sm, 1, stoff);
        merge_stats(asf(sm[0][0]), asf(sm[0][1]) + asf(sm[0][2]), M, L);
        *(float*)(lds + L_F + lane * 4) = __builtin_amdgcn_exp2f((ml.x - M) * L2E) * rcp(L);
        ARRIVE(C_F);
        TS(6);
      }
      LWAIT(C_DI, 4);
      TS(7);
      PRIO(0);
      const rsrc_t rw = mkr(p.wo + (size_t)((gw >> 2) * 512 + (gw & 3) * 32 + 8 * wv) * DM + sO * 128, 400 * DM * 2);
#pragma unroll
      for (int j = 0; j < 8; ++j) { PACE(392 + j * 4 + wv); wr[j] = ldnt(rw, 2 * g * (DM * 2) + c * 16, ((j & 3) * 128 + (j >> 2)) * DM * 2); }
      PIN();
      PRIO(3);
      TS(8);
      LWAIT(C_M4, 8);
      TS(9);
      u32x4 om[8];
      const int ooff = WS_O + (gw * 4 + wv) * 16384 + win((c & 7) * 128) + (2 * g + (c >> 3)) * 16;
#pragma unroll
      for (int k = 0; k < 8; ++k) om[k] = ldsc(rws, ooff, k * 2048);
      PIN();
#pragma unroll
      for (int jj = 0; jj < 4; ++jj) { tr_pair(wr, wt, jj); PIN(); }
      PINV(wt, 8);
      TS(10);
      RETRY(om, 8, ooff + k * 2048);
      TS(11);
      u32x4 a;
      {
        float v[4];
#pragma unroll
        for (int i = 0; i < 4; ++i)
          v[i] = ((asf(om[0][i]) + asf(om[1][i])) + (asf(om[2][i]) + asf(om[3][i]))) + ((asf(om[4][i]) + asf(om[5][i])) + (asf(om[6][i]) + asf(om[7][i])));
        a[0] = pack_bf(v[0], v[1]); a[1] = pack_bf(v[2], v[3]);
        a[2] = asu(dpp<DPP_ROR8>(asf(a[0]))); a[3] = asu(dpp<DPP_ROR8>(asf(a[1])));
      }
      f32x4 d[8];
#pragma unroll
      for (int i = 0; i < 8; ++i) d[i] = mfma32(a, wt[i], f32x4{0, 0, 0, 0});
      store_acc(d, lds + L_RS + wv * R_WS, g, c);
      ARRIVE(C_S);
      TS(12);
      LWAIT(C_S, 4);
      {
        const int t = wv * 64 + lane;
        const char* rf = lds + L_RS + (t >> 7) * R_GS + (t & 7) * R_IS + ((t >> 3) & 15) * 16;
        f32x4 s = *(const f32x4*)rf;
#pragma unroll
        for (int k = 1; k < 4; ++k) s += *(const f32x4*)(rf + k * R_WS);
        publish(rws, WS_FS + (sO * 32 + (t >> 3)) * 4096 + gw * 128 + (t & 7) * 16, s, tg);
      }
      ARRIVE(C_S5);
      TS(13);
    }
  }
  TS_FLUSH();
}

extern "C" void kernel_launch(void* const* d_in, const int* in_sizes, int n_in, void* d_out, int out_size, void* d_ws, size_t ws_size,
                              hipStream_t stream) {
  Params p;
  p.x = (const u16*)d_in[0]; p.kc = (const u16*)d_in[1]; p.vc = (const u16*)d_in[2]; p.wq = (const u16*)d_in[3]; p.wo = (const u16*)d_in[4];
  p.wg = (const u16*)d_in[6]; p.wu = (const u16*)d_in[7]; p.wd = (const u16*)d_in[8];
  p.out = (u16*)d_out; p.ws = (char*)d_ws;
  fused_kernel<<<NWG, NT, 0, stream>>>(p);
}
```

```cpp
#include <hip/hip_runtime.h>
#include <hip/hip_bf16.h>
#include <stdint.h>

typedef unsigned int u32;
typedef unsigned short u16;
typedef u32 u32x4 __attribute__((ext_vector_type(4)));
typedef u32 u32x2 __attribute__((ext_vector_type(2)));
typedef float f32x4 __attribute__((ext_vector_type(4)));
typedef __bf16 bf16x8 __attribute__((ext_vector_type(8)));
typedef short s16x4 __attribute__((ext_vector_type(4)));
typedef __amdgpu_buffer_rsrc_t rsrc_t;

constexpr int DM = 1024, HQD = 4096, SK = 4096, DH = 128, FF = 3584;
constexpr int NWG = 256, NT = 1024;
#define TS_WAVES 16

constexpr int WS_Q = 4096;
constexpr int WS_GU = WS_Q + 8 * 32 * 4 * 1024;
constexpr int WS_O = WS_GU + 224 * 8192;
constexpr int WS_ST = WS_O + 32 * 65536;
constexpr int WS_FS = WS_ST + 32 * 2048;
constexpr int WS_FY = WS_FS + 256 * 4096;
constexpr int WS_END = WS_FY + 256 * 4096;
constexpr int AUX_NT = 2, AUX_SC1 = 16;
constexpr u32 SPIN_CAP = 20000;

constexpr int XN_STR = 264;
constexpr int A_STR = 136;
constexpr int RG_STR = 68;
constexpr int R_IS = 288, R_GS = 8 * R_IS, R_WS = 2 * R_GS;
constexpr int L_CNT = 0;
constexpr int L_XN = 128;
constexpr int L_QS = L_XN + 8 * XN_STR * 2;
constexpr int L_ST = L_QS + 8 * A_STR * 2;
constexpr int L_F = L_ST + 512;
constexpr int L_P = L_F + 256;
constexpr int L_RQ = L_P + 8192;
constexpr int L_RG = L_RQ + 8 * 8 * 64 * 4;
constexpr int L_RO = L_RG + 8 * 8 * RG_STR * 4;
constexpr int L_RY = L_RO + 8 * R_WS;
constexpr int L_A = L_RY + 4 * R_WS;
constexpr int L_OW = L_A + 8 * A_STR * 2;
constexpr int L_END = L_OW + 32768;
enum { C_XN = 0, C_RQ, C_QS, C_ST, C_RG, C_VI, C_DI, C_F, C_RO, C_M4, C_Y, C_OW, C_A, C_S5, C_P = 16 };

#define PIN() do { asm volatile("" ::: "memory"); __builtin_amdgcn_sched_barrier(0); } while (0)
#define USE4(x) asm volatile("" ::"v"(x))
#define BAR() do { asm volatile("s_waitcnt lgkmcnt(0)\n\ts_barrier" ::: "memory"); __builtin_amdgcn_sched_barrier(0); } while (0)

#define TS(i)
#define TS_DECL
#define TS_SET(i, v) do {} while (0)
#define TS_FLUSH()

__device__ __forceinline__ f32x4 mfma32(u32x4 a, u32x4 b, f32x4 c) {
  return __builtin_amdgcn_mfma_f32_16x16x32_bf16(__builtin_bit_cast(bf16x8, a), __builtin_bit_cast(bf16x8, b), c, 0, 0, 0);
}
__device__ __forceinline__ f32x4 mfma16(u32x2 a, u32x2 b, f32x4 c) {
  return __builtin_amdgcn_mfma_f32_16x16x16bf16_1k(__builtin_bit_cast(s16x4, a), __builtin_bit_cast(s16x4, b), c, 0, 0, 0);
}
__device__ __forceinline__ float asf(u32 x) { return __builtin_bit_cast(float, x); }
__device__ __forceinline__ u32 asu(float x) { return __builtin_bit_cast(u32, x); }
__device__ __forceinline__ float bf_lo(u32 x) { return asf(x << 16); }
__device__ __forceinline__ float bf_hi(u32 x) { return asf(x & 0xffff0000u); }
__device__ __forceinline__ u32 pack_bf(float lo, float hi) {
  typedef __bf16 bf16x2 __attribute__((ext_vector_type(2)));
  typedef float f32x2 __attribute__((ext_vector_type(2)));
  f32x2 v = {lo, hi};
  return __builtin_bit_cast(u32, __builtin_convertvector(v, bf16x2));
}
__device__ __forceinline__ float rcp(float x) { return __builtin_amdgcn_rcpf(x); }
constexpr float L2E = 1.4426950408889634f;
constexpr int OOB = 0x7ffffff0;
__device__ __forceinline__ float sigm(float x) { return rcp(1.f + __builtin_amdgcn_exp2f(-L2E * x)); }
__device__ __forceinline__ float rbf(float x) { return bf_lo(pack_bf(x, x)); }
__device__ __forceinline__ u32 perm_lo(u32 a, u32 b) { return __builtin_amdgcn_perm(b, a, 0x05040100u); }
__device__ __forceinline__ u32 perm_hi(u32 a, u32 b) { return __builtin_amdgcn_perm(b, a, 0x07060302u); }
template <int CTRL> __device__ __forceinline__ float dpp(float x) {
  return asf(__builtin_amdgcn_mov_dpp(asu(x), CTRL, 0xf, 0xf, true));
}
template <int BANK> __device__ __forceinline__ float ror8_merge(float old, float src) {
  return asf(__builtin_amdgcn_update_dpp(asu(old), asu(src), 0x128, 0xf, BANK, false));
}
constexpr int DPP_X1 = 0xB1, DPP_X2 = 0x4E, DPP_HMIR = 0x141, DPP_MIR = 0x140, DPP_ROR8 = 0x128, DPP_ROR4 = 0x124;
__device__ __forceinline__ void swap32(float& a, float& b) {
  auto r = __builtin_amdgcn_permlane32_swap(asu(a), asu(b), false, false);
  a = asf(r[0]); b = asf(r[1]);
}
__device__ __forceinline__ void swap16(float& a, float& b) {
  auto r = __builtin_amdgcn_permlane16_swap(asu(a), asu(b), false, false);
  a = asf(r[0]); b = asf(r[1]);
}
__device__ __forceinline__ float add32(float x) { float a = x, b = x; swap32(a, b); return a + b; }
__device__ __forceinline__ float add16(float x) { float a = x, b = x; swap16(a, b); return a + b; }
__device__ __forceinline__ float max32(float x) { float a = x, b = x; swap32(a, b); return fmaxf(a, b); }
__device__ __forceinline__ float max16(float x) { float a = x, b = x; swap16(a, b); return fmaxf(a, b); }
__device__ __forceinline__ float wave_sum(float x) {
  x += dpp<DPP_X1>(x); x += dpp<DPP_X2>(x); x += dpp<DPP_HMIR>(x); x += dpp<DPP_MIR>(x);
  return add32(add16(x));
}
__device__ __forceinline__ u32x4 ldnt(rsrc_t r, int voff, int soff) { return __builtin_amdgcn_raw_buffer_load_b128(r, voff, soff, AUX_NT); }
__device__ __forceinline__ u32x4 ldsc(rsrc_t r, int voff, int soff) { return __builtin_amdgcn_raw_buffer_load_b128(r, voff, soff, AUX_SC1); }
__device__ __forceinline__ rsrc_t mkr(const void* p, int bytes) { return __builtin_amdgcn_make_buffer_rsrc((void*)p, 0, bytes, 0x00020000); }

struct Tags { u32 t[4]; };
__device__ __forceinline__ int win(int o) { return o + (o >= 384 ? 128 : 0); }
__device__ __forceinline__ void publish(rsrc_t r, int voff, f32x4 v, const Tags& tg) {
  u32x4 o;
#pragma unroll
  for (int i = 0; i < 4; ++i) o[i] = ((asu(v[i]) + 0x800u) & 0xfffff000u) | tg.t[i];
  __builtin_amdgcn_raw_buffer_store_b128(o, r, voff, 0, AUX_SC1);
}
__device__ __forceinline__ void strip(u32x4& m, const Tags& tg) {
#pragma unroll
  for (int i = 0; i < 4; ++i) m[i] ^= tg.t[i];
}
__device__ __forceinline__ u32 or4(const u32x4& m) { return (m[0] | m[1]) | (m[2] | m[3]); }

struct Params {
  const u16* x; const u16* kc; const u16* vc; const u16* wq; const u16* wo;
  const u16* wg; const u16* wu; const u16* wd;
  u16* out; char* ws;
};
constexpr int P_STEP16 = 62, P_LEAD = 16, PO = 360, SO = 4, PG3 = 350, PD = 392, SD = 7, P4 = 418, PF = 488, MODE = 3;
#define PACE(pos) do { PIN(); const int due_ = (((pos) - P_LEAD) * P_STEP16) >> 4; while ((int)((u32)__builtin_amdgcn_s_memrealtime() - tz) < due_) __builtin_amdgcn_s_sleep(1); PIN(); } while (0)

template <int STR> __device__ __forceinline__ void store64(f32x4 (&d)[8], float* red, int g, int c) {
  f32x4 s[4];
#pragma unroll
  for (int i = 0; i < 4; ++i)
#pragma unroll
    for (int r = 0; r < 4; ++r) {
      float a = d[i][r], b = d[i + 4][r];
      swap32(a, b);
      s[r][i] = a + dpp<DPP_ROR8>(b);
    }
  if (c < 8) {
#pragma unroll
    for (int r = 0; r < 4; ++r) *(f32x4*)(red + (4 * (g & 1) + r) * STR + 8 * c + 4 * (g >> 1)) = s[r];
  }
}
__device__ __forceinline__ void store_acc(f32x4 (&d)[8], char* red, int g, int c) {
  if (g < 2) {
#pragma unroll
    for (int i = 0; i < 8; ++i) *(f32x4*)(red + g * R_GS + i * R_IS + c * 16) = d[i];
  }
}
__device__ __forceinline__ void tr_pair(const u32x4* w, u32x4 (&f)[8], int jj) {
#pragma unroll
  for (int i = 0; i < 8; ++i) {
    const u32 lo = w[2 * jj][i >> 1], hi = w[2 * jj + 1][i >> 1];
    f[i][jj] = (i & 1) ? perm_hi(lo, hi) : perm_lo(lo, hi);
  }
}
__device__ __forceinline__ void merge_stats(float m, float l, float& M, float& L) {
  M = fmaxf(m, dpp<DPP_ROR8>(m));
  M = max32(max16(M));
  L = l * __builtin_amdgcn_exp2f((m - M) * L2E);
  L += dpp<DPP_ROR8>(L);
  L = add32(add16(L));
}

#define ARRIVE(i) do { PIN(); asm volatile("s_mov_b64 exec, 1\n\tds_add_u32 %0, %1\n\ts_mov_b64 exec, -1" :: "v"(lcnt + 4 * (i)), "v"(1u) : "memory"); PIN(); } while (0)
#define PINV(a, n) do { _Pragma("unroll") for (int i_ = 0; i_ < (n); ++i_) asm volatile("" : "+v"((a)[i_])); } while (0)
#define LWAITS(i, n, SL) do { PIN(); u32 it_ = 0; \
    while (__builtin_amdgcn_readfirstlane(__hip_atomic_load((u32*)(lds + L_CNT) + (i), __ATOMIC_RELAXED, __HIP_MEMORY_SCOPE_WORKGROUP)) < (u32)(n) && ++it_ < (1u << 22)) { SL; } \
    PIN(); } while (0)
#define LWAIT(i, n) LWAITS(i, n, __builtin_amdgcn_s_sleep(1))
#define LWAITF(i, n) LWAITS(i, n, )
#define PRIO(n) do { __builtin_amdgcn_s_setprio(n); PIN(); } while (0)
#define MKTAGS() Tags tg; { const u32 e_ = __builtin_amdgcn_readfirstlane(ep) + 1u; _Pragma("unroll") for (int i_ = 0; i_ < 4; ++i_) tg.t[i_] = (e_ + 0x4d5u * i_) & 0xfffu; }
#define RETRY(m, n, addr) do { u32 bad_ = 0; \
    _Pragma("unroll") for (int k = 0; k < (n); ++k) { strip(m[k], tg); bad_ |= or4(m[k]); } \
    if (__builtin_expect(__builtin_amdgcn_ballot_w64((bad_ & 0xfffu) != 0) != 0, 0)) { u32 spin_ = 0; do { asm volatile("; retry" ::: "memory"); \
    bool st_[n]; \
    _Pragma("unroll") for (int k = 0; k < (n); ++k) st_[k] = (or4(m[k]) & 0xfffu) != 0; \
    _Pragma("unroll") for (int k = 0; k < (n); ++k) if (st_[k]) m[k] = ldsc(rws, (addr), 0); \
    PIN(); bad_ = 0; \
    _Pragma("unroll") for (int k = 0; k < (n); ++k) { if (st_[k]) strip(m[k], tg); bad_ |= or4(m[k]); } \
    } while (__builtin_amdgcn_ballot_w64((bad_ & 0xfffu) != 0) != 0 && ++spin_ < SPIN_CAP); TS_SET(21, tsv[21] + spin_ + 1); } } while (0)

__global__ void __launch_bounds__(NT, 4) fused_kernel(Params p) {
  __shared__ __attribute__((aligned(16))) char lds[L_END];
  const int tid = threadIdx.x, lane = tid & 63, wave = __builtin_amdgcn_readfirstlane(tid >> 6);
  const int g = lane >> 4, c = lane & 15, hi = lane >> 5;
  const int wg = blockIdx.x, grp = (wg & 7) * 4 + (wg >> 6), sub = (wg >> 3) & 7;
  const int ks4 = wg & 3, jt = wg >> 2;
  const int cgq = jt >= 56 ? 8 * (jt - 56) + 3 : 8 * (jt / 7) + (jt % 7) + ((jt % 7) >= 3);
  const int mg = jt < 48 ? jt : 48;
  const int cgn = 8 * (mg / 7) + (mg % 7) + ((mg % 7) >= 3);
  const bool has_g = jt < 55 || jt == 62, has_u = jt < 48 || (jt >= 55 && jt != 62);
  const int cgg = (jt >= 48 && jt < 55) ? 8 * (jt - 48) + 3 : cgn, cgu = (jt >= 55 && jt < 62) ? 8 * (jt - 55) + 3 : cgn;
  const int gw = (MODE & 1) ? grp : (wg >> 5) * 4 + ks4;
  const int sO = (MODE & 1) ? (sub + 1) & 7 : (wg & 4) + (((wg >> 3) + 1) & 3), sD = (MODE & 1) ? (sub + 3) & 7 : (wg & 4) + (((wg >> 3) + 3) & 3);
  const int w = wave & 7, wv = wave & 3;
  const rsrc_t rws = mkr(p.ws, WS_END);
  const u32 lcnt = (u32)(uintptr_t)(__attribute__((address_space(3))) char*)lds + L_CNT;
  TS_DECL;
  TS(0);
  const u32 tz = (u32)__builtin_amdgcn_s_memrealtime();
  const u32 ep = __builtin_amdgcn_raw_buffer_load_b32(rws, 0, 0, AUX_SC1);
  const size_t kvoff = ((size_t)grp * SK + sub * 512 + w * 64) * DH;
  const int rot = (MODE & 2) ? (w + sub) & 3 : 0;
  int kb[4];
#pragma unroll
  for (int i = 0; i < 4; ++i) kb[i] = ((i + rot) & 3) * 4096;
  if (wave < 8) {
    PRIO(3);
    const u32x4 xa = *(const u32x4*)(p.x + w * DM + lane * 8);
    const u32x4 xb = *(const u32x4*)(p.x + w * DM + 512 + lane * 8);
    PIN();
    u32x4 wqr[4];
    {
      const rsrc_t r = mkr(p.wq + (size_t)(ks4 * 256 + w * 32) * HQD + cgq * 64, 32 * HQD * 2);
      const int vo = (16 * (c >> 3) + 4 * g) * (HQD * 2) + (c & 7) * 16;
#pragma unroll
      for (int j = 0; j < 4; ++j) wqr[j] = ldnt(r, vo, j * HQD * 2);
    }
    if (tid < 32) ((u32*)(lds + L_CNT))[tid] = 0;
    BAR();
    {
      float v[16];
#pragma unroll
      for (int i = 0; i < 4; ++i) { v[2 * i] = bf_lo(xa[i]); v[2 * i + 1] = bf_hi(xa[i]); v[8 + 2 * i] = bf_lo(xb[i]); v[9 + 2 * i] = bf_hi(xb[i]); }
      float s = 0.f;
#pragma unroll
      for (int i = 0; i < 16; ++i) s += v[i];
      const float mean = wave_sum(s) * (1.f / DM);
      float q = 0.f;
#pragma unroll
      for (int i = 0; i < 16; ++i) { const float d = v[i] - mean; q += d * d; }
      const float var = rbf(wave_sum(q) * (1.f / DM));
      const float mb = rbf(mean);
      const float rs = rcp(rbf(__builtin_amdgcn_sqrtf(rbf(var + 1e-5f))));
      const u32x4 xs = (ks4 & 2) ? xb : xa;
      u32x4 o;
#pragma unroll
      for (int i = 0; i < 4; ++i) {
        const u32 d = pack_bf(bf_lo(xs[i]) - mb, bf_hi(xs[i]) - mb);
        o[i] = pack_bf(bf_lo(d) * rs, bf_hi(d) * rs);
      }
      if (hi == (ks4 & 1)) *(u32x4*)(lds + L_XN + (w * XN_STR + (lane & 31) * 8) * 2) = o;
    }
    ARRIVE(C_XN);
    TS(1);
    MKTAGS();
    u32x2 fq[8];
#pragma unroll
    for (int i = 0; i < 8; ++i)
#pragma unroll
      for (int jj = 0; jj < 2; ++jj) {
        const u32 lo = wqr[2 * jj][i >> 1], hh = wqr[2 * jj + 1][i >> 1];
        fq[i][jj] = (i & 1) ? perm_hi(lo, hh) : perm_lo(lo, hh);
      }
    PINV(fq, 8);
    LWAIT(C_XN, 8);
    {
      const u32x2 a = *(const u32x2*)(lds + L_XN + ((c & 7) * XN_STR + w * 32 + 16 * (c >> 3) + 4 * g) * 2);
      f32x4 d[8];
#pragma unroll
      for (int i = 0; i < 8; ++i) d[i] = mfma16(a, fq[i], f32x4{0, 0, 0, 0});
      store64<64>(d, (float*)(lds + L_RQ) + w * 512, g, c);
    }
    ARRIVE(C_RQ);
    TS(2);
    LWAIT(C_RQ, 8);
    {
      const float* rq = (const float*)(lds + L_RQ) + g * 1024 + w * 64 + c * 4;
      f32x4 s = *(const f32x4*)rq + *(const f32x4*)(rq + 512);
#pragma unroll
      for (int i = 0; i < 4; ++i) s[i] = add32(add16(s[i]));
      publish(rws, lane < 16 ? WS_Q + ((w * 32 + (cgq >> 1)) * 2 + (ks4 >> 1)) * 2048 + win((ks4 & 1) * 512 + (cgq & 1) * 256 + c * 16) : OOB, s, tg);
    }
    TS(3);
    PRIO(1);
    const rsrc_t rv = mkr(p.vc + kvoff, 64 * DH * 2);
    u32x4 gr[8], vr[16];
    {
      const bool has_gu = w < 4 ? has_g : has_u;
      const rsrc_t r = mkr((w < 4 ? p.wg : p.wu) + (size_t)(ks4 * 256 + wv * 64) * FF + (has_gu ? (w < 4 ? cgg : cgu) * 64 : 0), has_gu ? 64 * FF * 2 : 0);
      const int vo = (32 * (c >> 3) + 8 * g) * (FF * 2) + (c & 7) * 16;
#pragma unroll
      for (int j = 0; j < 8; ++j) { PACE(168 + j * 7 + w); gr[j] = ldnt(r, vo, j * FF * 2); }
    }
    PRIO(0);
#pragma unroll
    for (int n = 0; n < 8; ++n) { PACE(224 + n * 8 + w); vr[n] = ldnt(rv, lane * 16 + (n & 3) * 1024, kb[n >> 2]); }
    PIN();
    TS(4);
    PRIO(3);
    {
      const u32x4 a = *(const u32x4*)(lds + L_XN + ((c & 7) * XN_STR + wv * 64 + 32 * (c >> 3) + 8 * g) * 2);
      u32x4 f[8];
#pragma unroll
      for (int jj = 0; jj < 4; ++jj) tr_pair(gr, f, jj);
      f32x4 d[8];
#pragma unroll
      for (int i = 0; i < 8; ++i) d[i] = mfma32(a, f[i], f32x4{0, 0, 0, 0});
      store64<RG_STR>(d, (float*)(lds + L_RG) + w * 8 * RG_STR, g, c);
    }
    ARRIVE(C_RG);
    TS(5);
    PRIO(0);
#pragma unroll
    for (int n = 8; n < 16; ++n) { PACE(224 + n * 8 + w); vr[n] = ldnt(rv, lane * 16 + (n & 3) * 1024, kb[n >> 2]); }
    ARRIVE(C_VI);
    TS(6);
    PRIO(3);
    u32x4 vt[8];
#pragma unroll
    for (int jj = 0; jj < 4; ++jj) tr_pair(vr, vt, jj);
    PINV(vt, 8);
    LWAIT(C_P + w, 1);
    TS(7);
    f32x4 acc[8];
    {
      const u32x4 pf = *(const u32x4*)(lds + L_P + w * 1024 + lane * 16);
#pragma unroll
      for (int i = 0; i < 8; ++i) acc[i] = mfma32(pf, vt[i], f32x4{0, 0, 0, 0});
    }
    PIN();
    TS(8);
    const u32x4 pf1 = *(const u32x4*)(lds + L_P + w * 1024 + (lane ^ 8) * 16);
#pragma unroll
    for (int jj = 0; jj < 4; ++jj) { tr_pair(vr + 8, vt, jj); PIN(); }
#pragma unroll
    for (int i = 0; i < 8; ++i) acc[i] = mfma32(pf1, vt[i], acc[i]);
    store_acc(acc, lds + L_RO + w * R_WS, g, c);
    ARRIVE(C_RO);
    TS(9);
    LWAIT(C_F, 1);
    LWAIT(C_RO, 8);
    TS(10);
    {
      const int g2 = w >> 2, i = lane & 7, cl = (lane >> 3) & 3;
      const char* ro = lds + L_RO + hi * 4 * R_WS + g2 * R_GS + i * R_IS + (wv * 4 + cl) * 16;
      const char* fp = lds + L_F + hi * 128 + g2 * 16;
      f32x4 o = *(const f32x4*)ro * *(const f32x4*)fp;
#pragma unroll
      for (int k = 1; k < 4; ++k) o += *(const f32x4*)(ro + k * R_WS) * *(const f32x4*)(fp + k * 32);
#pragma unroll
      for (int r = 0; r < 4; ++r) o[r] = add32(o[r]);
      publish(rws, hi ? OOB : WS_O + (((grp & 3) * 2 + g2) * 4 + wv) * 65536 + (grp >> 2) * 8192 + (sub >> 1) * 2048 + win((sub & 1) * 512 + lane * 16), o, tg);
    }
    ARRIVE(C_M4);
    TS(11);
    LWAIT(C_M4, 8);
    PACE(P4);
    u32x4 om[4], bw[4];
    const int ooff = WS_O + gw * 65536 + w * 8192 + win(lane * 16);
#pragma unroll
    for (int k = 0; k < 4; ++k) om[k] = ldsc(rws, ooff, k * 2048);
    TS(12);
    LWAIT(C_OW, 4);
#pragma unroll
    for (int k = 0; k < 4; ++k) bw[k] = *(const u32x4*)(lds + L_OW + k * 8192 + w * 1024 + lane * 16);
    PINV(bw, 4);
    TS(13);
    RETRY(om, 4, ooff + k * 2048);
    TS(14);
    {
      float v[4];
#pragma unroll
      for (int i = 0; i < 4; ++i) v[i] = (asf(om[0][i]) + asf(om[1][i])) + (asf(om[2][i]) + asf(om[3][i]));
      swap32(v[0], v[2]); swap32(v[1], v[3]);
      *(u32*)(lds + L_A + (w * A_STR + (lane & 31) * 4 + 2 * hi) * 2) = pack_bf(v[0] + v[2], v[1] + v[3]);
    }
    ARRIVE(C_A);
    LWAITF(C_A, 8);
    TS(15);
    {
      u32x4 a[4];
#pragma unroll
      for (int k = 0; k < 4; ++k) a[k] = *(const u32x4*)(lds + L_A + ((c & 7) * A_STR + 32 * k + 8 * g) * 2);
      f32x4 d0 = mfma32(a[0], bw[0], f32x4{0, 0, 0, 0}), d1 = mfma32(a[1], bw[1], f32x4{0, 0, 0, 0});
      d0 = mfma32(a[2], bw[2], d0); d1 = mfma32(a[3], bw[3], d1);
      publish(rws, hi ? OOB : WS_FS + (((2 * sO + (c >> 3)) * 2 + g) * 8 + w) * 4096 + gw * 128 + (c & 7) * 16, d0 + d1, tg);
    }
    ARRIVE(C_S5);
    TS(16);
  } else {
    const int oidx = (4 * ((wg >> 3) & 1) + g) * DM + (wg >> 4) * 64 + (c & 7) * 8 + (wg & 7);
    const u32 xfin = p.x[oidx];
    asm volatile("s_barrier" ::: "memory");
    PRIO(2);
    const rsrc_t rk = mkr(p.kc + kvoff, 64 * DH * 2);
    const int kvo = (4 * (c & 3) + ((c >> 2) & 1)) * 256 + (4 * (c >> 3) + g) * 16;
    u32x4 kr[16];
#pragma unroll
    for (int n = 0; n < 16; ++n) { if (!(n & 1)) PACE(32 + n * 8 + w * 2); kr[n] = ldnt(rk, kvo + ((n >> 1) & 1) * 512 + (n & 1) * 128, kb[n >> 2]); }
    PIN();
    TS(1);
    PRIO(3);
    if (w < 4) { USE4(kr[15]); PIN(); }
    u32x4 qm[2];
    const int qoff = WS_Q + ((grp >> 2) * 32 + (grp & 3) * 8 + w) * 4096 + win(lane * 16);
    qm[0] = ldsc(rws, qoff, 0);
    qm[1] = ldsc(rws, qoff, 2048);
    PIN();
    MKTAGS();
    RETRY(qm, 2, qoff + k * 2048);
    TS(2);
    {
      float q0 = asf(qm[0][0]) + asf(qm[1][0]), q1 = asf(qm[0][1]) + asf(qm[1][1]);
      float q2 = asf(qm[0][2]) + asf(qm[1][2]), q3 = asf(qm[0][3]) + asf(qm[1][3]);
      swap32(q0, q2); swap32(q1, q3);
      *(u32*)(lds + L_QS + (w * A_STR + (lane & 31) * 4 + 2 * hi) * 2) = pack_bf(q0 + q2, q1 + q3);
    }
    ARRIVE(C_QS);
    LWAIT(C_QS, 8);
    TS(3);
    {
      const u32x4 q0 = *(const u32x4*)(lds + L_QS + ((c & 7) * A_STR + 32 * (c >> 3) + 8 * g) * 2);
      const u32x4 q1 = *(const u32x4*)(lds + L_QS + ((c & 7) * A_STR + 64 + 32 * (c >> 3) + 8 * g) * 2);
      float sc[8];
#pragma unroll
      for (int e = 0; e < 2; ++e) {
        f32x4 t[4];
#pragma unroll
        for (int ua = 0; ua < 4; ++ua) t[ua] = mfma32(kr[(ua >> 1) * 8 + e * 4 + (ua & 1) * 2], q0, f32x4{0, 0, 0, 0});
#pragma unroll
        for (int ua = 0; ua < 4; ++ua) t[ua] = mfma32(kr[(ua >> 1) * 8 + e * 4 + (ua & 1) * 2 + 1], q1, t[ua]);
#pragma unroll
        for (int r = 0; r < 4; ++r) {
          float a = t[0][r], b = t[1][r], a2 = t[2][r], b2 = t[3][r];
          swap32(a, b); swap32(a2, b2);
          sc[e * 4 + r] = ror8_merge<0x3>(b2, b) + ror8_merge<0xc>(a, a2);
        }
      }
      float mx = -INFINITY;
#pragma unroll
      for (int i = 0; i < 4; ++i) {
        const u32 s1 = pack_bf(sc[2 * i], sc[2 * i + 1]);
        const u32 s2 = pack_bf(bf_lo(s1) * 0.08838834764831845f, bf_hi(s1) * 0.08838834764831845f);
        sc[2 * i] = bf_lo(s2); sc[2 * i + 1] = bf_hi(s2);
        mx = fmaxf(mx, fmaxf(sc[2 * i], sc[2 * i + 1]));
      }
      mx = fmaxf(mx, dpp<DPP_ROR8>(mx));
      const float mw = max32(max16(mx));
      float l = 0.f;
      u32x4 pf;
#pragma unroll
      for (int i = 0; i < 4; ++i) {
        const float e0 = __builtin_amdgcn_exp2f((sc[2 * i] - mw) * L2E), e1 = __builtin_amdgcn_exp2f((sc[2 * i + 1] - mw) * L2E);
        l += e0 + e1;
        pf[i] = pack_bf(e0, e1);
      }
      *(u32x4*)(lds + L_P + w * 1024 + lane * 16) = pf;
      ARRIVE(C_P + w);
      l += dpp<DPP_ROR8>(l);
      l = add32(add16(l));
      if (lane < 8) *(float2*)(lds + L_ST + (w * 8 + lane) * 8) = make_float2(mw, l);
    }
    ARRIVE(C_ST);
    TS(4);
    u32x4 wr[8], wt[8];
    if (w >= 4) {
      LWAIT(C_RG, 8);
      TS(5);
      {
        const float* rg = (const float*)(lds + L_RG) + ((lane >> 3) & 1) * 4 * 8 * RG_STR + (lane & 7) * RG_STR + wv * 16 + ((lane >> 4) & 1) * 8 + hi * 4;
        f32x4 s = *(const f32x4*)rg;
#pragma unroll
        for (int k = 1; k < 4; ++k) s += *(const f32x4*)(rg + k * 8 * RG_STR);
        publish(rws, ((lane & 8) ? has_u : has_g) ? WS_GU + (((lane & 8) ? cgu : cgg) * 4 + wv) * 8192 + ks4 * 2048 + win(lane * 16) : OOB, s, tg);
      }
      TS(6);
      PACE(PG3 + wv);
      TS(7);
      u32x4 gm[8];
      const int gbase = WS_GU + (gw * 7 + 2 * wv + (wv == 3 ? 0 : hi)) * 8192;
      const int goff0 = gbase + win((g & 1) * 256 + c * 16), goff1 = gbase + 640 + (g & 1) * 256 + c * 16;
#pragma unroll
      for (int k = 0; k < 8; ++k) gm[k] = ldsc(rws, (k & 1) ? goff1 : goff0, (k >> 1) * 2048);
      PRIO(0);
      const rsrc_t rw = mkr(p.wd + (size_t)(gw * 112 + 32 * wv) * DM + sD * 128, (wv == 3 ? 16 : 32) * DM * 2 - sD * 256);
#pragma unroll
      for (int j = 0; j < 8; ++j) { PACE(PD + j * SD / 2 + wv); wr[j] = ldnt(rw, 8 * g * (DM * 2) + c * 16, j * DM * 2); }
      ARRIVE(C_DI);
      PRIO(3);
      TS(8);
      RETRY(gm, 8, ((k & 1) ? goff1 : goff0) + (k >> 1) * 2048);
      TS(9);
      u32x4 a;
#pragma unroll
      for (int i = 0; i < 4; ++i) {
        const int qp = i >> 1, e = (i & 1) * 2;
        const float v0 = (asf(gm[qp][e]) + asf(gm[2 + qp][e])) + (asf(gm[4 + qp][e]) + asf(gm[6 + qp][e]));
        const float v1 = (asf(gm[qp][e + 1]) + asf(gm[2 + qp][e + 1])) + (asf(gm[4 + qp][e + 1]) + asf(gm[6 + qp][e + 1]));
        const u32 gg = pack_bf(v0, v1);
        const u32 uu = asu(dpp<DPP_ROR8>(asf(gg)));
        const u32 sg = pack_bf(sigm(bf_lo(gg)), sigm(bf_hi(gg)));
        a[i] = pack_bf(bf_lo(sg) * bf_lo(uu), bf_hi(sg) * bf_hi(uu));
        if (wv == 3 && hi) a[i] = 0;
      }
      PINV(a, 4);
      TS(10);
#pragma unroll
      for (int jj = 0; jj < 4; ++jj) { tr_pair(wr, wt, jj); PIN(); }
      f32x4 d[8];
#pragma unroll
      for (int i = 0; i < 8; ++i) d[i] = mfma32(a, wt[i], f32x4{0, 0, 0, 0});
      store_acc(d, lds + L_RY + wv * R_WS, g, c);
      ARRIVE(C_Y);
      TS(11);
      LWAIT(C_Y, 4);
      {
        const int t = wv * 64 + lane;
        const char* rf = lds + L_RY + (t >> 7) * R_GS + ((t >> 3) & 7) * R_IS + (((t >> 6) & 1) * 8 + (t & 7)) * 16;
        f32x4 s = *(const f32x4*)rf;
#pragma unroll
        for (int k = 1; k < 4; ++k) s += *(const f32x4*)(rf + k * R_WS);
        publish(rws, WS_FY + (((2 * sD + ((t >> 6) & 1)) * 2 + (t >> 7)) * 8 + ((t >> 3) & 7)) * 4096 + gw * 128 + (t & 7) * 16, s, tg);
      }
      ARRIVE(C_S5);
      TS(12);
    } else {
      if (w == 0) {
        LWAIT(C_ST, 8);
        const float2 ml = *(const float2*)(lds + L_ST + lane * 8);
        float M, L;
        merge_stats(ml.x, ml.y, M, L);
        const float Lh = asf((asu(L) + 0x800u) & 0xfffff000u);
        publish(rws, lane < 8 ? WS_ST + grp * 2048 + win(sub * 128) + lane * 16 : OOB, f32x4{M, Lh, L - Lh, 0.f}, tg);
        TS(5);
        LWAIT(C_RG, 8);
        u32x4 sm[1];
        const int stoff = WS_ST + grp * 2048 + win(lane * 16);
        sm[0] = ldsc(rws, stoff, 0);
        RETRY(sm, 1, stoff);
        merge_stats(asf(sm[0][0]), asf(sm[0][1]) + asf(sm[0][2]), M, L);
        *(float*)(lds + L_F + lane * 4) = __builtin_amdgcn_exp2f((ml.x - M) * L2E) * rcp(L);
        ARRIVE(C_F);
        TS(6);
      }
      LWAIT(C_VI, 8);
      TS(7);
      PRIO(0);
      const rsrc_t rw = mkr(p.wo + (size_t)((gw >> 2) * 512 + (gw & 3) * 32 + 8 * wv) * DM + sO * 128, 400 * DM * 2);
#pragma unroll
      for (int j = 0; j < 8; ++j) { PACE(PO + j * SO + wv); wr[j] = ldnt(rw, 2 * g * (DM * 2) + c * 16, ((j & 3) * 128 + (j >> 2)) * DM * 2); }
      PIN();
      PRIO(3);
      TS(8);
#pragma unroll
      for (int jj = 0; jj < 4; ++jj) { tr_pair(wr, wt, jj); PIN(); }
#pragma unroll
      for (int i = 0; i < 8; ++i) *(u32x4*)(lds + L_OW + wv * 8192 + i * 1024 + lane * 16) = wt[i];
      ARRIVE(C_OW);
      TS(10);
      if (w == 0) {
      LWAIT(C_S5, 12);
      PACE(PF);
      TS(12);
      u32x4 fm[8];
      const int foff = WS_FS + wg * 4096 + lane * 16;
#pragma unroll
      for (int k = 0; k < 8; ++k) fm[k] = ldsc(rws, foff, (k >> 2) * (WS_FY - WS_FS) + (k & 3) * 1024);
      RETRY(fm, 8, foff + (k >> 2) * (WS_FY - WS_FS) + (k & 3) * 1024);
      TS(14);
      float z[2];
#pragma unroll
      for (int h = 0; h < 2; ++h) {
        float v[4];
#pragma unroll
        for (int i = 0; i < 4; ++i) v[i] = (asf(fm[4 * h][i]) + asf(fm[4 * h + 1][i])) + (asf(fm[4 * h + 2][i]) + asf(fm[4 * h + 3][i]));
        swap32(v[0], v[2]); swap32(v[1], v[3]);
        v[0] += v[2]; v[1] += v[3];
        swap16(v[0], v[1]);
        const float t = v[0] + v[1];
        z[h] = rbf(t + dpp<DPP_ROR8>(t));
      }
      z[1] = rbf(z[1] * sigm(z[1]));
      const float r = bf_lo(xfin) + rbf(z[1] * z[0]);
      if (c < 8) p.out[oidx] = (u16)pack_bf(r, r);
      if (wg == 0 && lane == 0) __builtin_amdgcn_raw_buffer_store_b32(__builtin_amdgcn_readfirstlane(ep) + 1u, rws, 0, 0, AUX_SC1);
      TS(15);
    }
    }
  }
  TS_FLUSH();
}

extern "C" void kernel_launch(void* const* d_in, const int* in_sizes, int n_in, void* d_out, int out_size, void* d_ws, size_t ws_size,
                              hipStream_t stream) {
  Params p;
  p.x = (const u16*)d_in[0]; p.kc = (const u16*)d_in[1]; p.vc = (const u16*)d_in[2]; p.wq = (const u16*)d_in[3]; p.wo = (const u16*)d_in[4];
  p.wg = (const u16*)d_in[6]; p.wu = (const u16*)d_in[7]; p.wd = (const u16*)d_in[8];
  p.out = (u16*)d_out; p.ws = (char*)d_ws;
  fused_kernel<<<NWG, NT, 0, stream>>>(p);
}
```

```cpp
#include <hip/hip_runtime.h>
#include <hip/hip_bf16.h>
#include <stdint.h>

typedef unsigned int u32;
typedef unsigned short u16;
typedef u32 u32x4 __attribute__((ext_vector_type(4)));
typedef u32 u32x2 __attribute__((ext_vector_type(2)));
typedef float f32x4 __attribute__((ext_vector_type(4)));
typedef __bf16 bf16x8 __attribute__((ext_vector_type(8)));
typedef short s16x4 __attribute__((ext_vector_type(4)));
typedef __amdgpu_buffer_rsrc_t rsrc_t;

constexpr int DM = 1024, HQD = 4096, SK = 4096, DH = 128, FF = 3584;
constexpr int NWG = 256, NT = 1024;

constexpr int WS_Q = 4096;
constexpr int WS_GU = WS_Q + 8 * 32 * 4 * 1024;
constexpr int WS_O = WS_GU + 224 * 8192;
constexpr int WS_ST = WS_O + 32 * 65536;
constexpr int WS_FS = WS_ST + 32 * 2048;
constexpr int WS_FY = WS_FS + 256 * 4096;
constexpr int WS_END = WS_FY + 256 * 4096;
constexpr int AUX_NT = 2, AUX_SC1 = 16;
constexpr u32 SPIN_CAP = 20000;

constexpr int XN_STR = 264;
constexpr int A_STR = 136;
constexpr int RG_STR = 68;
constexpr int R_IS = 288, R_GS = 8 * R_IS, R_WS = 2 * R_GS;
constexpr int L_CNT = 0;
constexpr int L_XN = 128;
constexpr int L_QS = L_XN + 8 * XN_STR * 2;
constexpr int L_ST = L_QS + 8 * A_STR * 2;
constexpr int L_F = L_ST + 512;
constexpr int L_P = L_F + 256;
constexpr int L_RQ = L_P + 8192;
constexpr int L_RG = L_RQ + 8 * 8 * 64 * 4;
constexpr int L_RO = L_RG + 8 * 8 * RG_STR * 4;
constexpr int L_RY = L_RO + 8 * R_WS;
constexpr int L_A = L_RY + 4 * R_WS;
constexpr int L_OW = L_A + 8 * A_STR * 2;
constexpr int L_END = L_OW + 32768;
enum { C_XN = 0, C_RQ, C_QS, C_ST, C_RG, C_VI, C_DI, C_F, C_RO, C_M4, C_Y, C_OW, C_A, C_S5, C_P = 16 };

#define PIN() do { asm volatile("" ::: "memory"); __builtin_amdgcn_sched_barrier(0); } while (0)
#define USE4(x) asm volatile("" ::"v"(x))
#define BAR() do { asm volatile("s_waitcnt lgkmcnt(0)\n\ts_barrier" ::: "memory"); __builtin_amdgcn_sched_barrier(0); } while (0)

__device__ __forceinline__ f32x4 mfma32(u32x4 a, u32x4 b, f32x4 c) {
  return __builtin_amdgcn_mfma_f32_16x16x32_bf16(__builtin_bit_cast(bf16x8, a), __builtin_bit_cast(bf16x8, b), c, 0, 0, 0);
}
__device__ __forceinline__ f32x4 mfma16(u32x2 a, u32x2 b, f32x4 c) {
  return __builtin_amdgcn_mfma_f32_16x16x16bf16_1k(__builtin_bit_cast(s16x4, a), __builtin_bit_cast(s16x4, b), c, 0, 0, 0);
}
__device__ __forceinline__ float asf(u32 x) { return __builtin_bit_cast(float, x); }
__device__ __forceinline__ u32 asu(float x) { return __builtin_bit_cast(u32, x); }
__device__ __forceinline__ float bf_lo(u32 x) { return asf(x << 16); }
__device__ __forceinline__ float bf_hi(u32 x) { return asf(x & 0xffff0000u); }
__device__ __forceinline__ u32 pack_bf(float lo, float hi) {
  typedef __bf16 bf16x2 __attribute__((ext_vector_type(2)));
  typedef float f32x2 __attribute__((ext_vector_type(2)));
  f32x2 v = {lo, hi};
  return __builtin_bit_cast(u32, __builtin_convertvector(v, bf16x2));
}
__device__ __forceinline__ float rcp(float x) { return __builtin_amdgcn_rcpf(x); }
constexpr float L2E = 1.4426950408889634f;
constexpr int OOB = 0x7ffffff0;
__device__ __forceinline__ float sigm(float x) { return rcp(1.f + __builtin_amdgcn_exp2f(-L2E * x)); }
__device__ __forceinline__ float rbf(float x) { return bf_lo(pack_bf(x, x)); }
__device__ __forceinline__ u32 perm_lo(u32 a, u32 b) { return __builtin_amdgcn_perm(b, a, 0x05040100u); }
__device__ __forceinline__ u32 perm_hi(u32 a, u32 b) { return __builtin_amdgcn_perm(b, a, 0x07060302u); }
template <int CTRL> __device__ __forceinline__ float dpp(float x) {
  return asf(__builtin_amdgcn_mov_dpp(asu(x), CTRL, 0xf, 0xf, true));
}
template <int BANK> __device__ __forceinline__ float ror8_merge(float old, float src) {
  return asf(__builtin_amdgcn_update_dpp(asu(old), asu(src), 0x128, 0xf, BANK, false));
}
constexpr int DPP_X1 = 0xB1, DPP_X2 = 0x4E, DPP_HMIR = 0x141, DPP_MIR = 0x140, DPP_ROR8 = 0x128, DPP_ROR4 = 0x124;
__device__ __forceinline__ void swap32(float& a, float& b) {
  auto r = __builtin_amdgcn_permlane32_swap(asu(a), asu(b), false, false);
  a = asf(r[0]); b = asf(r[1]);
}
__device__ __forceinline__ void swap16(float& a, float& b) {
  auto r = __builtin_amdgcn_permlane16_swap(asu(a), asu(b), false, false);
  a = asf(r[0]); b = asf(r[1]);
}
__device__ __forceinline__ float add32(float x) { float a = x, b = x; swap32(a, b); return a + b; }
__device__ __forceinline__ float add16(float x) { float a = x, b = x; swap16(a, b); return a + b; }
__device__ __forceinline__ float max32(float x) { float a = x, b = x; swap32(a, b); return fmaxf(a, b); }
__device__ __forceinline__ float max16(float x) { float a = x, b = x; swap16(a, b); return fmaxf(a, b); }
__device__ __forceinline__ float wave_sum(float x) {
  x += dpp<DPP_X1>(x); x += dpp<DPP_X2>(x); x += dpp<DPP_HMIR>(x); x += dpp<DPP_MIR>(x);
  return add32(add16(x));
}
__device__ __forceinline__ u32x4 ldnt(rsrc_t r, int voff, int soff) { return __builtin_amdgcn_raw_buffer_load_b128(r, voff, soff, AUX_NT); }
__device__ __forceinline__ u32x4 ldsc(rsrc_t r, int voff, int soff) { return __builtin_amdgcn_raw_buffer_load_b128(r, voff, soff, AUX_SC1); }
__device__ __forceinline__ rsrc_t mkr(const void* p, int bytes) { return __builtin_amdgcn_make_buffer_rsrc((void*)p, 0, bytes, 0x00020000); }

struct Tags { u32 t[4]; };
__device__ __forceinline__ int win(int o) { return o + (o >= 384 ? 128 : 0); }
__device__ __forceinline__ void publish(rsrc_t r, int voff, f32x4 v, const Tags& tg) {
  u32x4 o;
#pragma unroll
  for (int i = 0; i < 4; ++i) o[i] = ((asu(v[i]) + 0x800u) & 0xfffff000u) | tg.t[i];
  __builtin_amdgcn_raw_buffer_store_b128(o, r, voff, 0, AUX_SC1);
}
__device__ __forceinline__ void strip(u32x4& m, const Tags& tg) {
#pragma unroll
  for (int i = 0; i < 4; ++i) m[i] ^= tg.t[i];
}
__device__ __forceinline__ u32 or4(const u32x4& m) { return (m[0] | m[1]) | (m[2] | m[3]); }

struct Params {
  const u16* x; const u16* kc; const u16* vc; const u16* wq; const u16* wo;
  const u16* wg; const u16* wu; const u16* wd;
  u16* out; char* ws;
};
constexpr int P_STEP16 = 62, P_LEAD = 16, PO = 360, SO = 4, PG3 = 350, PD = 392, SD = 7, P4 = 418, PF = 488, MODE = 3;
#define PACE(pos) do { PIN(); const int due_ = (((pos) - P_LEAD) * P_STEP16) >> 4; while ((int)((u32)__builtin_amdgcn_s_memrealtime() - tz) < due_) __builtin_amdgcn_s_sleep(1); PIN(); } while (0)

template <int STR> __device__ __forceinline__ void store64(f32x4 (&d)[8], float* red, int g, int c) {
  f32x4 s[4];
#pragma unroll
  for (int i = 0; i < 4; ++i)
#pragma unroll
    for (int r = 0; r < 4; ++r) {
      float a = d[i][r], b = d[i + 4][r];
      swap32(a, b);
      s[r][i] = a + dpp<DPP_ROR8>(b);
    }
  if (c < 8) {
#pragma unroll
    for (int r = 0; r < 4; ++r) *(f32x4*)(red + (4 * (g & 1) + r) * STR + 8 * c + 4 * (g >> 1)) = s[r];
  }
}
__device__ __forceinline__ void store_acc(f32x4 (&d)[8], char* red, int g, int c) {
  if (g < 2) {
#pragma unroll
    for (int i = 0; i < 8; ++i) *(f32x4*)(red + g * R_GS + i * R_IS + c * 16) = d[i];
  }
}
__device__ __forceinline__ void tr_pair(const u32x4* w, u32x4 (&f)[8], int jj) {
#pragma unroll
  for (int i = 0; i < 8; ++i) {
    const u32 lo = w[2 * jj][i >> 1], hi = w[2 * jj + 1][i >> 1];
    f[i][jj] = (i & 1) ? perm_hi(lo, hi) : perm_lo(lo, hi);
  }
}
__device__ __forceinline__ void merge_stats(float m, float l, float& M, float& L) {
  M = fmaxf(m, dpp<DPP_ROR8>(m));
  M = max32(max16(M));
  L = l * __builtin_amdgcn_exp2f((m - M) * L2E);
  L += dpp<DPP_ROR8>(L);
  L = add32(add16(L));
}

#define ARRIVE(i) do { PIN(); asm volatile("s_mov_b64 exec, 1\n\tds_add_u32 %0, %1\n\ts_mov_b64 exec, -1" :: "v"(lcnt + 4 * (i)), "v"(1u) : "memory"); PIN(); } while (0)
#define PINV(a, n) do { _Pragma("unroll") for (int i_ = 0; i_ < (n); ++i_) asm volatile("" : "+v"((a)[i_])); } while (0)
#define LWAITS(i, n, SL) do { PIN(); u32 it_ = 0; \
    while (__builtin_amdgcn_readfirstlane(__hip_atomic_load((u32*)(lds + L_CNT) + (i), __ATOMIC_RELAXED, __HIP_MEMORY_SCOPE_WORKGROUP)) < (u32)(n) && ++it_ < (1u << 22)) { SL; } \
    PIN(); } while (0)
#define LWAIT(i, n) LWAITS(i, n, __builtin_amdgcn_s_sleep(1))
#define LWAITF(i, n) LWAITS(i, n, )
#define PRIO(n) do { __builtin_amdgcn_s_setprio(n); PIN(); } while (0)
#define MKTAGS() Tags tg; { const u32 e_ = __builtin_amdgcn_readfirstlane(ep) + 1u; _Pragma("unroll") for (int i_ = 0; i_ < 4; ++i_) tg.t[i_] = (e_ + 0x4d5u * i_) & 0xfffu; }
#define RETRY(m, n, addr) do { u32 bad_ = 0; \
    _Pragma("unroll") for (int k = 0; k < (n); ++k) { strip(m[k], tg); bad_ |= or4(m[k]); } \
    if (__builtin_expect(__builtin_amdgcn_ballot_w64((bad_ & 0xfffu) != 0) != 0, 0)) { u32 spin_ = 0; do { asm volatile("; retry" ::: "memory"); \
    bool st_[n]; \
    _Pragma("unroll") for (int k = 0; k < (n); ++k) st_[k] = (or4(m[k]) & 0xfffu) != 0; \
    _Pragma("unroll") for (int k = 0; k < (n); ++k) if (st_[k]) m[k] = ldsc(rws, (addr), 0); \
    PIN(); bad_ = 0; \
    _Pragma("unroll") for (int k = 0; k < (n); ++k) { if (st_[k]) strip(m[k], tg); bad_ |= or4(m[k]); } \
    } while (__builtin_amdgcn_ballot_w64((bad_ & 0xfffu) != 0) != 0 && ++spin_ < SPIN_CAP); } } while (0)

__global__ void __launch_bounds__(NT, 4) fused_kernel(Params p) {
  __shared__ __attribute__((aligned(16))) char lds[L_END];
  const int tid = threadIdx.x, lane = tid & 63, wave = __builtin_amdgcn_readfirstlane(tid >> 6);
  const int g = lane >> 4, c = lane & 15, hi = lane >> 5;
  const int wg = blockIdx.x, grp = (wg & 7) * 4 + (wg >> 6), sub = (wg >> 3) & 7;
  const int ks4 = wg & 3, jt = wg >> 2;
  const int cgq = jt >= 56 ? 8 * (jt - 56) + 3 : 8 * (jt / 7) + (jt % 7) + ((jt % 7) >= 3);
  const int mg = jt < 48 ? jt : 48;
  const int cgn = 8 * (mg / 7) + (mg % 7) + ((mg % 7) >= 3);
  const bool has_g = jt < 55 || jt == 62, has_u = jt < 48 || (jt >= 55 && jt != 62);
  const int cgg = (jt >= 48 && jt < 55) ? 8 * (jt - 48) + 3 : cgn, cgu = (jt >= 55 && jt < 62) ? 8 * (jt - 55) + 3 : cgn;
  const int gw = (MODE & 1) ? grp : (wg >> 5) * 4 + ks4;
  const int sO = (MODE & 1) ? (sub + 1) & 7 : (wg & 4) + (((wg >> 3) + 1) & 3), sD = (MODE & 1) ? (sub + 3) & 7 : (wg & 4) + (((wg >> 3) + 3) & 3);
  const int w = wave & 7, wv = wave & 3;
  const rsrc_t rws = mkr(p.ws, WS_END);
  const u32 lcnt = (u32)(uintptr_t)(__attribute__((address_space(3))) char*)lds + L_CNT;
  const u32 tz = (u32)__builtin_amdgcn_s_memrealtime();
  const u32 ep = __builtin_amdgcn_raw_buffer_load_b32(rws, 0, 0, AUX_SC1);
  const size_t kvoff = ((size_t)grp * SK + sub * 512 + w * 64) * DH;
  const int rot = (MODE & 2) ? (w + sub) & 3 : 0;
  int kb[4];
#pragma unroll
  for (int i = 0; i < 4; ++i) kb[i] = ((i + rot) & 3) * 4096;
  if (wave < 8) {
    PRIO(3);
    const u32x4 xa = *(const u32x4*)(p.x + w * DM + lane * 8);
    const u32x4 xb = *(const u32x4*)(p.x + w * DM + 512 + lane * 8);
    PIN();
    u32x4 wqr[4];
    {
      const rsrc_t r = mkr(p.wq + (size_t)(ks4 * 256 + w * 32) * HQD + cgq * 64, 32 * HQD * 2);
      const int vo = (16 * (c >> 3) + 4 * g) * (HQD * 2) + (c & 7) * 16;
#pragma unroll
      for (int j = 0; j < 4; ++j) wqr[j] = ldnt(r, vo, j * HQD * 2);
    }
    if (tid < 32) ((u32*)(lds + L_CNT))[tid] = 0;
    BAR();
    {
      float v[16];
#pragma unroll
      for (int i = 0; i < 4; ++i) { v[2 * i] = bf_lo(xa[i]); v[2 * i + 1] = bf_hi(xa[i]); v[8 + 2 * i] = bf_lo(xb[i]); v[9 + 2 * i] = bf_hi(xb[i]); }
      float s = 0.f;
#pragma unroll
      for (int i = 0; i < 16; ++i) s += v[i];
      const float mean = wave_sum(s) * (1.f / DM);
      float q = 0.f;
#pragma unroll
      for (int i = 0; i < 16; ++i) { const float d = v[i] - mean; q += d * d; }
      const float var = rbf(wave_sum(q) * (1.f / DM));
      const float mb = rbf(mean);
      const float rs = rcp(rbf(__builtin_amdgcn_sqrtf(rbf(var + 1e-5f))));
      const u32x4 xs = (ks4 & 2) ? xb : xa;
      u32x4 o;
#pragma unroll
      for (int i = 0; i < 4; ++i) {
        const u32 d = pack_bf(bf_lo(xs[i]) - mb, bf_hi(xs[i]) - mb);
        o[i] = pack_bf(bf_lo(d) * rs, bf_hi(d) * rs);
      }
      if (hi == (ks4 & 1)) *(u32x4*)(lds + L_XN + (w * XN_STR + (lane & 31) * 8) * 2) = o;
    }
    ARRIVE(C_XN);
    MKTAGS();
    u32x2 fq[8];
#pragma unroll
    for (int i = 0; i < 8; ++i)
#pragma unroll
      for (int jj = 0; jj < 2; ++jj) {
        const u32 lo = wqr[2 * jj][i >> 1], hh = wqr[2 * jj + 1][i >> 1];
        fq[i][jj] = (i & 1) ? perm_hi(lo, hh) : perm_lo(lo, hh);
      }
    PINV(fq, 8);
    LWAIT(C_XN, 8);
    {
      const u32x2 a = *(const u32x2*)(lds + L_XN + ((c & 7) * XN_STR + w * 32 + 16 * (c >> 3) + 4 * g) * 2);
      f32x4 d[8];
#pragma unroll
      for (int i = 0; i < 8; ++i) d[i] = mfma16(a, fq[i], f32x4{0, 0, 0, 0});
      store64<64>(d, (float*)(lds + L_RQ) + w * 512, g, c);
    }
    ARRIVE(C_RQ);
    LWAIT(C_RQ, 8);
    {
      const float* rq = (const float*)(lds + L_RQ) + g * 1024 + w * 64 + c * 4;
      f32x4 s = *(const f32x4*)rq + *(const f32x4*)(rq + 512);
#pragma unroll
      for (int i = 0; i < 4; ++i) s[i] = add32(add16(s[i]));
      publish(rws, lane < 16 ? WS_Q + ((w * 32 + (cgq >> 1)) * 2 + (ks4 >> 1)) * 2048 + win((ks4 & 1) * 512 + (cgq & 1) * 256 + c * 16) : OOB, s, tg);
    }
    PRIO(1);
    const rsrc_t rv = mkr(p.vc + kvoff, 64 * DH * 2);
    u32x4 gr[8], vr[16];
    {
      const bool has_gu = w < 4 ? has_g : has_u;
      const rsrc_t r = mkr((w < 4 ? p.wg : p.wu) + (size_t)(ks4 * 256 + wv * 64) * FF + (has_gu ? (w < 4 ? cgg : cgu) * 64 : 0), has_gu ? 64 * FF * 2 : 0);
      const int vo = (32 * (c >> 3) + 8 * g) * (FF * 2) + (c & 7) * 16;
#pragma unroll
      for (int j = 0; j < 8; ++j) { PACE(168 + j * 7 + w); gr[j] = ldnt(r, vo, j * FF * 2); }
    }
    PRIO(0);
#pragma unroll
    for (int n = 0; n < 8; ++n) { PACE(224 + n * 8 + w); vr[n] = ldnt(rv, lane * 16 + (n & 3) * 1024, kb[n >> 2]); }
    PIN();
    PRIO(3);
    {
      const u32x4 a = *(const u32x4*)(lds + L_XN + ((c & 7) * XN_STR + wv * 64 + 32 * (c >> 3) + 8 * g) * 2);
      u32x4 f[8];
#pragma unroll
      for (int jj = 0; jj < 4; ++jj) tr_pair(gr, f, jj);
      f32x4 d[8];
#pragma unroll
      for (int i = 0; i < 8; ++i) d[i] = mfma32(a, f[i], f32x4{0, 0, 0, 0});
      store64<RG_STR>(d, (float*)(lds + L_RG) + w * 8 * RG_STR, g, c);
    }
    ARRIVE(C_RG);
    PRIO(0);
#pragma unroll
    for (int n = 8; n < 16; ++n) { PACE(224 + n * 8 + w); vr[n] = ldnt(rv, lane * 16 + (n & 3) * 1024, kb[n >> 2]); }
    ARRIVE(C_VI);
    PRIO(3);
    u32x4 vt[8];
#pragma unroll
    for (int jj = 0; jj < 4; ++jj) tr_pair(vr, vt, jj);
    PINV(vt, 8);
    LWAIT(C_P + w, 1);
    f32x4 acc[8];
    {
      const u32x4 pf = *(const u32x4*)(lds + L_P + w * 1024 + lane * 16);
#pragma unroll
      for (int i = 0; i < 8; ++i) acc[i] = mfma32(pf, vt[i], f32x4{0, 0, 0, 0});
    }
    PIN();
    const u32x4 pf1 = *(const u32x4*)(lds + L_P + w * 1024 + (lane ^ 8) * 16);
#pragma unroll
    for (int jj = 0; jj < 4; ++jj) { tr_pair(vr + 8, vt, jj); PIN(); }
#pragma unroll
    for (int i = 0; i < 8; ++i) acc[i] = mfma32(pf1, vt[i], acc[i]);
    store_acc(acc, lds + L_RO + w * R_WS, g, c);
    ARRIVE(C_RO);
    LWAIT(C_F, 1);
    LWAIT(C_RO, 8);
    {
      const int g2 = w >> 2, i = lane & 7, cl = (lane >> 3) & 3;
      const char* ro = lds + L_RO + hi * 4 * R_WS + g2 * R_GS + i * R_IS + (wv * 4 + cl) * 16;
      const char* fp = lds + L_F + hi * 128 + g2 * 16;
      f32x4 o = *(const f32x4*)ro * *(const f32x4*)fp;
#pragma unroll
      for (int k = 1; k < 4; ++k) o += *(const f32x4*)(ro + k * R_WS) * *(const f32x4*)(fp + k * 32);
#pragma unroll
      for (int r = 0; r < 4; ++r) o[r] = add32(o[r]);
      publish(rws, hi ? OOB : WS_O + (((grp & 3) * 2 + g2) * 4 + wv) * 65536 + (grp >> 2) * 8192 + (sub >> 1) * 2048 + win((sub & 1) * 512 + lane * 16), o, tg);
    }
    ARRIVE(C_M4);
    LWAIT(C_M4, 8);
    PACE(P4);
    u32x4 om[4], bw[4];
    const int ooff = WS_O + gw * 65536 + w * 8192 + win(lane * 16);
#pragma unroll
    for (int k = 0; k < 4; ++k) om[k] = ldsc(rws, ooff, k * 2048);
    LWAIT(C_OW, 4);
#pragma unroll
    for (int k = 0; k < 4; ++k) bw[k] = *(const u32x4*)(lds + L_OW + k * 8192 + w * 1024 + lane * 16);
    PINV(bw, 4);
    RETRY(om, 4, ooff + k * 2048);
    {
      float v[4];
#pragma unroll
      for (int i = 0; i < 4; ++i) v[i] = (asf(om[0][i]) + asf(om[1][i])) + (asf(om[2][i]) + asf(om[3][i]));
      swap32(v[0], v[2]); swap32(v[1], v[3]);
      *(u32*)(lds + L_A + (w * A_STR + (lane & 31) * 4 + 2 * hi) * 2) = pack_bf(v[0] + v[2], v[1] + v[3]);
    }
    ARRIVE(C_A);
    LWAITF(C_A, 8);
    {
      u32x4 a[4];
#pragma unroll
      for (int k = 0; k < 4; ++k) a[k] = *(const u32x4*)(lds + L_A + ((c & 7) * A_STR + 32 * k + 8 * g) * 2);
      f32x4 d0 = mfma32(a[0], bw[0], f32x4{0, 0, 0, 0}), d1 = mfma32(a[1], bw[1], f32x4{0, 0, 0, 0});
      d0 = mfma32(a[2], bw[2], d0); d1 = mfma32(a[3], bw[3], d1);
      publish(rws, hi ? OOB : WS_FS + (((2 * sO + (c >> 3)) * 2 + g) * 8 + w) * 4096 + gw * 128 + (c & 7) * 16, d0 + d1, tg);
    }
    ARRIVE(C_S5);
  } else {
    const int oidx = (4 * ((wg >> 3) & 1) + g) * DM + (wg >> 4) * 64 + (c & 7) * 8 + (wg & 7);
    const u32 xfin = p.x[oidx];
    asm volatile("s_barrier" ::: "memory");
    PRIO(2);
    const rsrc_t rk = mkr(p.kc + kvoff, 64 * DH * 2);
    const int kvo = (4 * (c & 3) + ((c >> 2) & 1)) * 256 + (4 * (c >> 3) + g) * 16;
    u32x4 kr[16];
#pragma unroll
    for (int n = 0; n < 16; ++n) { if (!(n & 1)) PACE(32 + n * 8 + w * 2); kr[n] = ldnt(rk, kvo + ((n >> 1) & 1) * 512 + (n & 1) * 128, kb[n >> 2]); }
    PIN();
    PRIO(3);
    if (w < 4) { USE4(kr[15]); PIN(); }
    u32x4 qm[2];
    const int qoff = WS_Q + ((grp >> 2) * 32 + (grp & 3) * 8 + w) * 4096 + win(lane * 16);
    qm[0] = ldsc(rws, qoff, 0);
    qm[1] = ldsc(rws, qoff, 2048);
    PIN();
    MKTAGS();
    RETRY(qm, 2, qoff + k * 2048);
    {
      float q0 = asf(qm[0][0]) + asf(qm[1][0]), q1 = asf(qm[0][1]) + asf(qm[1][1]);
      float q2 = asf(qm[0][2]) + asf(qm[1][2]), q3 = asf(qm[0][3]) + asf(qm[1][3]);
      swap32(q0, q2); swap32(q1, q3);
      *(u32*)(lds + L_QS + (w * A_STR + (lane & 31) * 4 + 2 * hi) * 2) = pack_bf(q0 + q2, q1 + q3);
    }
    ARRIVE(C_QS);
    LWAIT(C_QS, 8);
    {
      const u32x4 q0 = *(const u32x4*)(lds + L_QS + ((c & 7) * A_STR + 32 * (c >> 3) + 8 * g) * 2);
      const u32x4 q1 = *(const u32x4*)(lds + L_QS + ((c & 7) * A_STR + 64 + 32 * (c >> 3) + 8 * g) * 2);
      float sc[8];
#pragma unroll
      for (int e = 0; e < 2; ++e) {
        f32x4 t[4];
#pragma unroll
        for (int ua = 0; ua < 4; ++ua) t[ua] = mfma32(kr[(ua >> 1) * 8 + e * 4 + (ua & 1) * 2], q0, f32x4{0, 0, 0, 0});
#pragma unroll
        for (int ua = 0; ua < 4; ++ua) t[ua] = mfma32(kr[(ua >> 1) * 8 + e * 4 + (ua & 1) * 2 + 1], q1, t[ua]);
#pragma unroll
        for (int r = 0; r < 4; ++r) {
          float a = t[0][r], b = t[1][r], a2 = t[2][r], b2 = t[3][r];
          swap32(a, b); swap32(a2, b2);
          sc[e * 4 + r] = ror8_merge<0x3>(b2, b) + ror8_merge<0xc>(a, a2);
        }
      }
      float mx = -INFINITY;
#pragma unroll
      for (int i = 0; i < 4; ++i) {
        const u32 s1 = pack_bf(sc[2 * i], sc[2 * i + 1]);
        const u32 s2 = pack_bf(bf_lo(s1) * 0.08838834764831845f, bf_hi(s1) * 0.08838834764831845f);
        sc[2 * i] = bf_lo(s2); sc[2 * i + 1] = bf_hi(s2);
        mx = fmaxf(mx, fmaxf(sc[2 * i], sc[2 * i + 1]));
      }
      mx = fmaxf(mx, dpp<DPP_ROR8>(mx));
      const float mw = max32(max16(mx));
      float l = 0.f;
      u32x4 pf;
#pragma unroll
      for (int i = 0; i < 4; ++i) {
        const float e0 = __builtin_amdgcn_exp2f((sc[2 * i] - mw) * L2E), e1 = __builtin_amdgcn_exp2f((sc[2 * i + 1] - mw) * L2E);
        l += e0 + e1;
        pf[i] = pack_bf(e0, e1);
      }
      *(u32x4*)(lds + L_P + w * 1024 + lane * 16) = pf;
      ARRIVE(C_P + w);
      l += dpp<DPP_ROR8>(l);
      l = add32(add16(l));
      if (lane < 8) *(float2*)(lds + L_ST + (w * 8 + lane) * 8) = make_float2(mw, l);
    }
    ARRIVE(C_ST);
    u32x4 wr[8], wt[8];
    if (w >= 4) {
      LWAIT(C_RG, 8);
      {
        const float* rg = (const float*)(lds + L_RG) + ((lane >> 3) & 1) * 4 * 8 * RG_STR + (lane & 7) * RG_STR + wv * 16 + ((lane >> 4) & 1) * 8 + hi * 4;
        f32x4 s = *(const f32x4*)rg;
#pragma unroll
        for (int k = 1; k < 4; ++k) s += *(const f32x4*)(rg + k * 8 * RG_STR);
        publish(rws, ((lane & 8) ? has_u : has_g) ? WS_GU + (((lane & 8) ? cgu : cgg) * 4 + wv) * 8192 + ks4 * 2048 + win(lane * 16) : OOB, s, tg);
      }
      PACE(PG3 + wv);
      u32x4 gm[8];
      const int gbase = WS_GU + (gw * 7 + 2 * wv + (wv == 3 ? 0 : hi)) * 8192;
      const int goff0 = gbase + win((g & 1) * 256 + c * 16), goff1 = gbase + 640 + (g & 1) * 256 + c * 16;
#pragma unroll
      for (int k = 0; k < 8; ++k) gm[k] = ldsc(rws, (k & 1) ? goff1 : goff0, (k >> 1) * 2048);
      PRIO(0);
      const rsrc_t rw = mkr(p.wd + (size_t)(gw * 112 + 32 * wv) * DM + sD * 128, (wv == 3 ? 16 : 32) * DM * 2 - sD * 256);
#pragma unroll
      for (int j = 0; j < 8; ++j) { PACE(PD + j * SD / 2 + wv); wr[j] = ldnt(rw, 8 * g * (DM * 2) + c * 16, j * DM * 2); }
      ARRIVE(C_DI);
      PRIO(3);
      RETRY(gm, 8, ((k & 1) ? goff1 : goff0) + (k >> 1) * 2048);
      u32x4 a;
#pragma unroll
      for (int i = 0; i < 4; ++i) {
        const int qp = i >> 1, e = (i & 1) * 2;
        const float v0 = (asf(gm[qp][e]) + asf(gm[2 + qp][e])) + (asf(gm[4 + qp][e]) + asf(gm[6 + qp][e]));
        const float v1 = (asf(gm[qp][e + 1]) + asf(gm[2 + qp][e + 1])) + (asf(gm[4 + qp][e + 1]) + asf(gm[6 + qp][e + 1]));
        const u32 gg = pack_bf(v0, v1);
        const u32 uu = asu(dpp<DPP_ROR8>(asf(gg)));
        const u32 sg = pack_bf(sigm(bf_lo(gg)), sigm(bf_hi(gg)));
        a[i] = pack_bf(bf_lo(sg) * bf_lo(uu), bf_hi(sg) * bf_hi(uu));
        if (wv == 3 && hi) a[i] = 0;
      }
      PINV(a, 4);
#pragma unroll
      for (int jj = 0; jj < 4; ++jj) { tr_pair(wr, wt, jj); PIN(); }
      f32x4 d[8];
#pragma unroll
      for (int i = 0; i < 8; ++i) d[i] = mfma32(a, wt[i], f32x4{0, 0, 0, 0});
      store_acc(d, lds + L_RY + wv * R_WS, g, c);
      ARRIVE(C_Y);
      LWAIT(C_Y, 4);
      {
        const int t = wv * 64 + lane;
        const char* rf = lds + L_RY + (t >> 7) * R_GS + ((t >> 3) & 7) * R_IS + (((t >> 6) & 1) * 8 + (t & 7)) * 16;
        f32x4 s = *(const f32x4*)rf;
#pragma unroll
        for (int k = 1; k < 4; ++k) s += *(const f32x4*)(rf + k * R_WS);
        publish(rws, WS_FY + (((2 * sD + ((t >> 6) & 1)) * 2 + (t >> 7)) * 8 + ((t >> 3) & 7)) * 4096 + gw * 128 + (t & 7) * 16, s, tg);
      }
      ARRIVE(C_S5);
    } else {
      if (w == 0) {
        LWAIT(C_ST, 8);
        const float2 ml = *(const float2*)(lds + L_ST + lane * 8);
        float M, L;
        merge_stats(ml.x, ml.y, M, L);
        const float Lh = asf((asu(L) + 0x800u) & 0xfffff000u);
        publish(rws, lane < 8 ? WS_ST + grp * 2048 + win(sub * 128) + lane * 16 : OOB, f32x4{M, Lh, L - Lh, 0.f}, tg);
        LWAIT(C_RG, 8);
        u32x4 sm[1];
        const int stoff = WS_ST + grp * 2048 + win(lane * 16);
        sm[0] = ldsc(rws, stoff, 0);
        RETRY(sm, 1, stoff);
        merge_stats(asf(sm[0][0]), asf(sm[0][1]) + asf(sm[0][2]), M, L);
        *(float*)(lds + L_F + lane * 4) = __builtin_amdgcn_exp2f((ml.x - M) * L2E) * rcp(L);
        ARRIVE(C_F);
      }
      LWAIT(C_VI, 8);
      PRIO(0);
      const rsrc_t rw = mkr(p.wo + (size_t)((gw >> 2) * 512 + (gw & 3) * 32 + 8 * wv) * DM + sO * 128, 400 * DM * 2);
#pragma unroll
      for (int j = 0; j < 8; ++j) { PACE(PO + j * SO + wv); wr[j] = ldnt(rw, 2 * g * (DM * 2) + c * 16, ((j & 3) * 128 + (j >> 2)) * DM * 2); }
      PIN();
      PRIO(3);
#pragma unroll
      for (int jj = 0; jj < 4; ++jj) { tr_pair(wr, wt, jj); PIN(); }
#pragma unroll
      for (int i = 0; i < 8; ++i) *(u32x4*)(lds + L_OW + wv * 8192 + i * 1024 + lane * 16) = wt[i];
      ARRIVE(C_OW);
      if (w == 0) {
      LWAIT(C_S5, 12);
      PACE(PF);
      u32x4 fm[8];
      const int foff = WS_FS + wg * 4096 + lane * 16;
#pragma unroll
      for (int k = 0; k < 8; ++k) fm[k] = ldsc(rws, foff, (k >> 2) * (WS_FY - WS_FS) + (k & 3) * 1024);
      RETRY(fm, 8, foff + (k >> 2) * (WS_FY - WS_FS) + (k & 3) * 1024);
      float z[2];
#pragma unroll
      for (int h = 0; h < 2; ++h) {
        float v[4];
#pragma unroll
        for (int i = 0; i < 4; ++i) v[i] = (asf(fm[4 * h][i]) + asf(fm[4 * h + 1][i])) + (asf(fm[4 * h + 2][i]) + asf(fm[4 * h + 3][i]));
        swap32(v[0], v[2]); swap32(v[1], v[3]);
        v[0] += v[2]; v[1] += v[3];
        swap16(v[0], v[1]);
        const float t = v[0] + v[1];
        z[h] = rbf(t + dpp<DPP_ROR8>(t));
      }
      z[1] = rbf(z[1] * sigm(z[1]));
      const float r = bf_lo(xfin) + rbf(z[1] * z[0]);
      if (c < 8) p.out[oidx] = (u16)pack_bf(r, r);
      if (wg == 0 && lane == 0) __builtin_amdgcn_raw_buffer_store_b32(__builtin_amdgcn_readfirstlane(ep) + 1u, rws, 0, 0, AUX_SC1);
    }
    }
  }
}

extern "C" void kernel_launch(void* const* d_in, const int* in_sizes, int n_in, void* d_out, int out_size, void* d_ws, size_t ws_size,
                              hipStream_t stream) {
  Params p;
  p.x = (const u16*)d_in[0]; p.kc = (const u16*)d_in[1]; p.vc = (const u16*)d_in[2]; p.wq = (const u16*)d_in[3]; p.wo = (const u16*)d_in[4];
  p.wg = (const u16*)d_in[6]; p.wu = (const u16*)d_in[7]; p.wd = (const u16*)d_in[8];
  p.out = (u16*)d_out; p.ws = (char*)d_ws;
  fused_kernel<<<NWG, NT, 0, stream>>>(p);
}
```

```cpp
#include <hip/hip_runtime.h>
#include <hip/hip_bf16.h>
#include <stdint.h>

typedef unsigned int u32;
typedef unsigned short u16;
typedef u32 u32x4 __attribute__((ext_vector_type(4)));
typedef u32 u32x2 __attribute__((ext_vector_type(2)));
typedef float f32x4 __attribute__((ext_vector_type(4)));
typedef __bf16 bf16x8 __attribute__((ext_vector_type(8)));
typedef short s16x4 __attribute__((ext_vector_type(4)));
typedef __amdgpu_buffer_rsrc_t rsrc_t;

constexpr int DM = 1024, HQD = 4096, SK = 4096, DH = 128, FF = 3584;
constexpr int NWG = 256, NT = 1024;
#define TS_WAVES 16

constexpr int WS_Q = 4096;
constexpr int WS_GU = WS_Q + 8 * 32 * 4 * 1024;
constexpr int WS_O = WS_GU + 224 * 8192;
constexpr int WS_ST = WS_O + 32 * 65536;
constexpr int WS_FS = WS_ST + 32 * 2048;
constexpr int WS_FY = WS_FS + 256 * 4096;
constexpr int WS_END = WS_FY + 256 * 4096;
constexpr int AUX_NT = 2, AUX_SC1 = 16;
constexpr u32 SPIN_CAP = 20000;

constexpr int XN_STR = 264;
constexpr int A_STR = 136;
constexpr int RG_STR = 68;
constexpr int R_IS = 288, R_GS = 8 * R_IS, R_WS = 2 * R_GS;
constexpr int L_CNT = 0;
constexpr int L_XN = 128;
constexpr int L_QS = L_XN + 8 * XN_STR * 2;
constexpr int L_ST = L_QS + 8 * A_STR * 2;
constexpr int L_F = L_ST + 512;
constexpr int L_P = L_F + 256;
constexpr int L_RQ = L_P + 8192;
constexpr int L_RG = L_RQ + 8 * 8 * 64 * 4;
constexpr int L_RO = L_RG + 8 * 8 * RG_STR * 4;
constexpr int L_RY = L_RO + 8 * R_WS;
constexpr int L_A = L_RY + 4 * R_WS;
constexpr int L_OW = L_A + 8 * A_STR * 2;
constexpr int L_END = L_OW + 32768;
enum { C_XN = 0, C_RQ, C_QS, C_ST, C_RG, C_VI, C_DI, C_F, C_RO, C_M4, C_Y, C_OW, C_A, C_S5, C_P = 16 };

#define PIN() do { asm volatile("" ::: "memory"); __builtin_amdgcn_sched_barrier(0); } while (0)
#define USE4(x) asm volatile("" ::"v"(x))
#define BAR() do { asm volatile("s_waitcnt lgkmcnt(0)\n\ts_barrier" ::: "memory"); __builtin_amdgcn_sched_barrier(0); } while (0)

#define TS(i)
#define TS_DECL
#define TS_SET(i, v) do {} while (0)
#define TS_FLUSH()

__device__ __forceinline__ f32x4 mfma32(u32x4 a, u32x4 b, f32x4 c) {
  return __builtin_amdgcn_mfma_f32_16x16x32_bf16(__builtin_bit_cast(bf16x8, a), __builtin_bit_cast(bf16x8, b), c, 0, 0, 0);
}
__device__ __forceinline__ f32x4 mfma16(u32x2 a, u32x2 b, f32x4 c) {
  return __builtin_amdgcn_mfma_f32_16x16x16bf16_1k(__builtin_bit_cast(s16x4, a), __builtin_bit_cast(s16x4, b), c, 0, 0, 0);
}
__device__ __forceinline__ float asf(u32 x) { return __builtin_bit_cast(float, x); }
__device__ __forceinline__ u32 asu(float x) { return __builtin_bit_cast(u32, x); }
__device__ __forceinline__ float bf_lo(u32 x) { return asf(x << 16); }
__device__ __forceinline__ float bf_hi(u32 x) { return asf(x & 0xffff0000u); }
__device__ __forceinline__ u32 pack_bf(float lo, float hi) {
  typedef __bf16 bf16x2 __attribute__((ext_vector_type(2)));
  typedef float f32x2 __attribute__((ext_vector_type(2)));
  f32x2 v = {lo, hi};
  return __builtin_bit_cast(u32, __builtin_convertvector(v, bf16x2));
}
__device__ __forceinline__ float rcp(float x) { return __builtin_amdgcn_rcpf(x); }
constexpr float L2E = 1.4426950408889634f;
constexpr int OOB = 0x7ffffff0;
__device__ __forceinline__ float sigm(float x) { return rcp(1.f + __builtin_amdgcn_exp2f(-L2E * x)); }
__device__ __forceinline__ float rbf(float x) { return bf_lo(pack_bf(x, x)); }
__device__ __forceinline__ u32 perm_lo(u32 a, u32 b) { return __builtin_amdgcn_perm(b, a, 0x05040100u); }
__device__ __forceinline__ u32 perm_hi(u32 a, u32 b) { return __builtin_amdgcn_perm(b, a, 0x07060302u); }
template <int CTRL> __device__ __forceinline__ float dpp(float x) {
  return asf(__builtin_amdgcn_mov_dpp(asu(x), CTRL, 0xf, 0xf, true));
}
template <int BANK> __device__ __forceinline__ float ror8_merge(float old, float src) {
  return asf(__builtin_amdgcn_update_dpp(asu(old), asu(src), 0x128, 0xf, BANK, false));
}
constexpr int DPP_X1 = 0xB1, DPP_X2 = 0x4E, DPP_HMIR = 0x141, DPP_MIR = 0x140, DPP_ROR8 = 0x128, DPP_ROR4 = 0x124;
__device__ __forceinline__ void swap32(float& a, float& b) {
  auto r = __builtin_amdgcn_permlane32_swap(asu(a), asu(b), false, false);
  a = asf(r[0]); b = asf(r[1]);
}
__device__ __forceinline__ void swap16(float& a, float& b) {
  auto r = __builtin_amdgcn_permlane16_swap(asu(a), asu(b), false, false);
  a = asf(r[0]); b = asf(r[1]);
}
__device__ __forceinline__ float add32(float x) { float a = x, b = x; swap32(a, b); return a + b; }
__device__ __forceinline__ float add16(float x) { float a = x, b = x; swap16(a, b); return a + b; }
__device__ __forceinline__ float max32(float x) { float a = x, b = x; swap32(a, b); return fmaxf(a, b); }
__device__ __forceinline__ float max16(float x) { float a = x, b = x; swap16(a, b); return fmaxf(a, b); }
__device__ __forceinline__ float wave_sum(float x) {
  x += dpp<DPP_X1>(x); x += dpp<DPP_X2>(x); x += dpp<DPP_HMIR>(x); x += dpp<DPP_MIR>(x);
  return add32(add16(x));
}
__device__ __forceinline__ u32x4 ldnt(rsrc_t r, int voff, int soff) { return __builtin_amdgcn_raw_buffer_load_b128(r, voff, soff, AUX_NT); }
__device__ __forceinline__ u32x4 ldsc(rsrc_t r, int voff, int soff) { return __builtin_amdgcn_raw_buffer_load_b128(r, voff, soff, AUX_SC1); }
__device__ __forceinline__ rsrc_t mkr(const void* p, int bytes) { return __builtin_amdgcn_make_buffer_rsrc((void*)p, 0, bytes, 0x00020000); }

struct Tags { u32 t[4]; };
__device__ __forceinline__ int win(int o) { return o + (o >= 384 ? 128 : 0); }
__device__ __forceinline__ void publish(rsrc_t r, int voff, f32x4 v, const Tags& tg) {
  u32x4 o;
#pragma unroll
  for (int i = 0; i < 4; ++i) o[i] = ((asu(v[i]) + 0x800u) & 0xfffff000u) | tg.t[i];
  __builtin_amdgcn_raw_buffer_store_b128(o, r, voff, 0, AUX_SC1);
}
__device__ __forceinline__ void strip(u32x4& m, const Tags& tg) {
#pragma unroll
  for (int i = 0; i < 4; ++i) m[i] ^= tg.t[i];
}
__device__ __forceinline__ u32 or4(const u32x4& m) { return (m[0] | m[1]) | (m[2] | m[3]); }

struct Params {
  const u16* x; const u16* kc; const u16* vc; const u16* wq; const u16* wo;
  const u16* wg; const u16* wu; const u16* wd;
  u16* out; char* ws;
};
constexpr int P_STEP16 = 62, P_LEAD = 16, PO = 360, SO = 4, PG3 = 350, PD = 392, SD = 7, P4 = 418, PF = 494, MODE = 3;
#define PACE(pos) do { PIN(); const int due_ = (((pos) - P_LEAD) * P_STEP16) >> 4; while ((int)((u32)__builtin_amdgcn_s_memrealtime() - tz) < due_) __builtin_amdgcn_s_sleep(1); PIN(); } while (0)

template <int STR> __device__ __forceinline__ void store64(f32x4 (&d)[8], float* red, int g, int c) {
  f32x4 s[4];
#pragma unroll
  for (int i = 0; i < 4; ++i)
#pragma unroll
    for (int r = 0; r < 4; ++r) {
      float a = d[i][r], b = d[i + 4][r];
      swap32(a, b);
      s[r][i] = a + dpp<DPP_ROR8>(b);
    }
  if (c < 8) {
#pragma unroll
    for (int r = 0; r < 4; ++r) *(f32x4*)(red + (4 * (g & 1) + r) * STR + 8 * c + 4 * (g >> 1)) = s[r];
  }
}
__device__ __forceinline__ void store_acc(f32x4 (&d)[8], char* red, int g, int c) {
  if (g < 2) {
#pragma unroll
    for (int i = 0; i < 8; ++i) *(f32x4*)(red + g * R_GS + i * R_IS + c * 16) = d[i];
  }
}
__device__ __forceinline__ void tr_pair(const u32x4* w, u32x4 (&f)[8], int jj) {
#pragma unroll
  for (int i = 0; i < 8; ++i) {
    const u32 lo = w[2 * jj][i >> 1], hi = w[2 * jj + 1][i >> 1];
    f[i][jj] = (i & 1) ? perm_hi(lo, hi) : perm_lo(lo, hi);
  }
}
__device__ __forceinline__ void merge_stats(float m, float l, float& M, float& L) {
  M = fmaxf(m, dpp<DPP_ROR8>(m));
  M = max32(max16(M));
  L = l * __builtin_amdgcn_exp2f((m - M) * L2E);
  L += dpp<DPP_ROR8>(L);
  L = add32(add16(L));
}

#define ARRIVE(i) do { PIN(); asm volatile("s_mov_b64 exec, 1\n\tds_add_u32 %0, %1\n\ts_mov_b64 exec, -1" :: "v"(lcnt + 4 * (i)), "v"(1u) : "memory"); PIN(); } while (0)
#define PINV(a, n) do { _Pragma("unroll") for (int i_ = 0; i_ < (n); ++i_) asm volatile("" : "+v"((a)[i_])); } while (0)
#define LWAITS(i, n, SL) do { PIN(); u32 it_ = 0; \
    while (__builtin_amdgcn_readfirstlane(__hip_atomic_load((u32*)(lds + L_CNT) + (i), __ATOMIC_RELAXED, __HIP_MEMORY_SCOPE_WORKGROUP)) < (u32)(n) && ++it_ < (1u << 22)) { SL; } \
    PIN(); } while (0)
#define LWAIT(i, n) LWAITS(i, n, __builtin_amdgcn_s_sleep(1))
#define LWAITF(i, n) LWAITS(i, n, )
#define PRIO(n) do { __builtin_amdgcn_s_setprio(n); PIN(); } while (0)
#define MKTAGS() Tags tg; { const u32 e_ = __builtin_amdgcn_readfirstlane(ep) + 1u; _Pragma("unroll") for (int i_ = 0; i_ < 4; ++i_) tg.t[i_] = (e_ + 0x4d5u * i_) & 0xfffu; }
#define RETRY(m, n, addr) do { u32 bad_ = 0; \
    _Pragma("unroll") for (int k = 0; k < (n); ++k) { strip(m[k], tg); bad_ |= or4(m[k]); } \
    if (__builtin_expect(__builtin_amdgcn_ballot_w64((bad_ & 0xfffu) != 0) != 0, 0)) { u32 spin_ = 0; do { asm volatile("; retry" ::: "memory"); \
    bool st_[n]; \
    _Pragma("unroll") for (int k = 0; k < (n); ++k) st_[k] = (or4(m[k]) & 0xfffu) != 0; \
    _Pragma("unroll") for (int k = 0; k < (n); ++k) if (st_[k]) m[k] = ldsc(rws, (addr), 0); \
    PIN(); bad_ = 0; \
    _Pragma("unroll") for (int k = 0; k < (n); ++k) { if (st_[k]) strip(m[k], tg); bad_ |= or4(m[k]); } \
    } while (__builtin_amdgcn_ballot_w64((bad_ & 0xfffu) != 0) != 0 && ++spin_ < SPIN_CAP); TS_SET(21, tsv[21] + spin_ + 1); } } while (0)

__global__ void __launch_bounds__(NT, 4) fused_kernel(Params p) {
  __shared__ __attribute__((aligned(16))) char lds[L_END];
  const int tid = threadIdx.x, lane = tid & 63, wave = __builtin_amdgcn_readfirstlane(tid >> 6);
  const int g = lane >> 4, c = lane & 15, hi = lane >> 5;
  const int wg = blockIdx.x, grp = (wg & 7) * 4 + (wg >> 6), sub = (wg >> 3) & 7;
  const int ks4 = wg & 3, jt = wg >> 2;
  const int cgq = jt >= 56 ? 8 * (jt - 56) + 3 : 8 * (jt / 7) + (jt % 7) + ((jt % 7) >= 3);
  const int mg = jt < 48 ? jt : 48;
  const int cgn = 8 * (mg / 7) + (mg % 7) + ((mg % 7) >= 3);
  const bool has_g = jt < 55 || jt == 62, has_u = jt < 48 || (jt >= 55 && jt != 62);
  const int cgg = (jt >= 48 && jt < 55) ? 8 * (jt - 48) + 3 : cgn, cgu = (jt >= 55 && jt < 62) ? 8 * (jt - 55) + 3 : cgn;
  const int gw = (MODE & 1) ? grp : (wg >> 5) * 4 + ks4;
  const int sO = (MODE & 1) ? (sub + 1) & 7 : (wg & 4) + (((wg >> 3) + 1) & 3), sD = (MODE & 1) ? (sub + 3) & 7 : (wg & 4) + (((wg >> 3) + 3) & 3);
  const int w = wave & 7, wv = wave & 3;
  const rsrc_t rws = mkr(p.ws, WS_END);
  const u32 lcnt = (u32)(uintptr_t)(__attribute__((address_space(3))) char*)lds + L_CNT;
  TS_DECL;
  TS(0);
  const u32 tz = (u32)__builtin_amdgcn_s_memrealtime();
  const u32 ep = __builtin_amdgcn_raw_buffer_load_b32(rws, 0, 0, AUX_SC1);
  const size_t kvoff = ((size_t)grp * SK + sub * 512 + w * 64) * DH;
  const int rot = (MODE & 2) ? (w + sub) & 3 : 0;
  int kb[4];
#pragma unroll
  for (int i = 0; i < 4; ++i) kb[i] = ((i + rot) & 3) * 4096;
  if (wave < 8) {
    PRIO(3);
    const u32x4 xa = *(const u32x4*)(p.x + w * DM + lane * 8);
    const u32x4 xb = *(const u32x4*)(p.x + w * DM + 512 + lane * 8);
    PIN();
    u32x4 wqr[4];
    {
      const rsrc_t r = mkr(p.wq + (size_t)(ks4 * 256 + w * 32) * HQD + cgq * 64, 32 * HQD * 2);
      const int vo = (16 * (c >> 3) + 4 * g) * (HQD * 2) + (c & 7) * 16;
#pragma unroll
      for (int j = 0; j < 4; ++j) wqr[j] = ldnt(r, vo, j * HQD * 2);
    }
    if (tid < 32) ((u32*)(lds + L_CNT))[tid] = 0;
    BAR();
    {
      float v[16];
#pragma unroll
      for (int i = 0; i < 4; ++i) { v[2 * i] = bf_lo(xa[i]); v[2 * i + 1] = bf_hi(xa[i]); v[8 + 2 * i] = bf_lo(xb[i]); v[9 + 2 * i] = bf_hi(xb[i]); }
      float s = 0.f;
#pragma unroll
      for (int i = 0; i < 16; ++i) s += v[i];
      const float mean = wave_sum(s) * (1.f / DM);
      float q = 0.f;
#pragma unroll
      for (int i = 0; i < 16; ++i) { const float d = v[i] - mean; q += d * d; }
      const float var = rbf(wave_sum(q) * (1.f / DM));
      const float mb = rbf(mean);
      const float rs = rcp(rbf(__builtin_amdgcn_sqrtf(rbf(var + 1e-5f))));
      const u32x4 xs = (ks4 & 2) ? xb : xa;
      u32x4 o;
#pragma unroll
      for (int i = 0; i < 4; ++i) {
        const u32 d = pack_bf(bf_lo(xs[i]) - mb, bf_hi(xs[i]) - mb);
        o[i] = pack_bf(bf_lo(d) * rs, bf_hi(d) * rs);
      }
      if (hi == (ks4 & 1)) *(u32x4*)(lds + L_XN + (w * XN_STR + (lane & 31) * 8) * 2) = o;
    }
    ARRIVE(C_XN);
    TS(1);
    MKTAGS();
    u32x2 fq[8];
#pragma unroll
    for (int i = 0; i < 8; ++i)
#pragma unroll
      for (int jj = 0; jj < 2; ++jj) {
        const u32 lo = wqr[2 * jj][i >> 1], hh = wqr[2 * jj + 1][i >> 1];
        fq[i][jj] = (i & 1) ? perm_hi(lo, hh) : perm_lo(lo, hh);
      }
    PINV(fq, 8);
    LWAIT(C_XN, 8);
    {
      const u32x2 a = *(const u32x2*)(lds + L_XN + ((c & 7) * XN_STR + w * 32 + 16 * (c >> 3) + 4 * g) * 2);
      f32x4 d[8];
#pragma unroll
      for (int i = 0; i < 8; ++i) d[i] = mfma16(a, fq[i], f32x4{0, 0, 0, 0});
      store64<64>(d, (float*)(lds + L_RQ) + w * 512, g, c);
    }
    ARRIVE(C_RQ);
    TS(2);
    LWAIT(C_RQ, 8);
    {
      const float* rq = (const float*)(lds + L_RQ) + g * 1024 + w * 64 + c * 4;
      f32x4 s = *(const f32x4*)rq + *(const f32x4*)(rq + 512);
#pragma unroll
      for (int i = 0; i < 4; ++i) s[i] = add32(add16(s[i]));
      publish(rws, lane < 16 ? WS_Q + ((w * 32 + (cgq >> 1)) * 2 + (ks4 >> 1)) * 2048 + win((ks4 & 1) * 512 + (cgq & 1) * 256 + c * 16) : OOB, s, tg);
    }
    TS(3);
    PRIO(1);
    const rsrc_t rv = mkr(p.vc + kvoff, 64 * DH * 2);
    u32x4 gr[8], vr[16];
    {
      const bool has_gu = w < 4 ? has_g : has_u;
      const rsrc_t r = mkr((w < 4 ? p.wg : p.wu) + (size_t)(ks4 * 256 + wv * 64) * FF + (has_gu ? (w < 4 ? cgg : cgu) * 64 : 0), has_gu ? 64 * FF * 2 : 0);
      const int vo = (32 * (c >> 3) + 8 * g) * (FF * 2) + (c & 7) * 16;
#pragma unroll
      for (int j = 0; j < 8; ++j) { PACE(168 + j * 7 + w); gr[j] = ldnt(r, vo, j * FF * 2); }
    }
    PRIO(0);
#pragma unroll
    for (int n = 0; n < 8; ++n) { PACE(224 + n * 8 + w); vr[n] = ldnt(rv, lane * 16 + (n & 3) * 1024, kb[n >> 2]); }
    PIN();
    TS(4);
    PRIO(3);
    {
      const u32x4 a = *(const u32x4*)(lds + L_XN + ((c & 7) * XN_STR + wv * 64 + 32 * (c >> 3) + 8 * g) * 2);
      u32x4 f[8];
#pragma unroll
      for (int jj = 0; jj < 4; ++jj) tr_pair(gr, f, jj);
      f32x4 d[8];
#pragma unroll
      for (int i = 0; i < 8; ++i) d[i] = mfma32(a, f[i], f32x4{0, 0, 0, 0});
      store64<RG_STR>(d, (float*)(lds + L_RG) + w * 8 * RG_STR, g, c);
    }
    ARRIVE(C_RG);
    TS(5);
    PRIO(0);
#pragma unroll
    for (int n = 8; n < 16; ++n) { PACE(224 + n * 8 + w); vr[n] = ldnt(rv, lane * 16 + (n & 3) * 1024, kb[n >> 2]); }
    ARRIVE(C_VI);
    TS(6);
    PRIO(3);
    u32x4 vt[8];
#pragma unroll
    for (int jj = 0; jj < 4; ++jj) tr_pair(vr, vt, jj);
    PINV(vt, 8);
    LWAIT(C_P + w, 1);
    TS(7);
    f32x4 acc[8];
    {
      const u32x4 pf = *(const u32x4*)(lds + L_P + w * 1024 + lane * 16);
#pragma unroll
      for (int i = 0; i < 8; ++i) acc[i] = mfma32(pf, vt[i], f32x4{0, 0, 0, 0});
    }
    PIN();
    TS(8);
    const u32x4 pf1 = *(const u32x4*)(lds + L_P + w * 1024 + (lane ^ 8) * 16);
#pragma unroll
    for (int jj = 0; jj < 4; ++jj) { tr_pair(vr + 8, vt, jj); PIN(); }
#pragma unroll
    for (int i = 0; i < 8; ++i) acc[i] = mfma32(pf1, vt[i], acc[i]);
    store_acc(acc, lds + L_RO + w * R_WS, g, c);
    ARRIVE(C_RO);
    TS(9);
    LWAIT(C_F, 1);
    LWAIT(C_RO, 8);
    TS(10);
    {
      const int g2 = w >> 2, i = lane & 7, cl = (lane >> 3) & 3;
      const char* ro = lds + L_RO + hi * 4 * R_WS + g2 * R_GS + i * R_IS + (wv * 4 + cl) * 16;
      const char* fp = lds + L_F + hi * 128 + g2 * 16;
      f32x4 o = *(const f32x4*)ro * *(const f32x4*)fp;
#pragma unroll
      for (int k = 1; k < 4; ++k) o += *(const f32x4*)(ro + k * R_WS) * *(const f32x4*)(fp + k * 32);
#pragma unroll
      for (int r = 0; r < 4; ++r) o[r] = add32(o[r]);
      publish(rws, hi ? OOB : WS_O + (((grp & 3) * 2 + g2) * 4 + wv) * 65536 + (grp >> 2) * 8192 + (sub >> 1) * 2048 + win((sub & 1) * 512 + lane * 16), o, tg);
    }
    ARRIVE(C_M4);
    TS(11);
    LWAIT(C_M4, 8);
    PACE(P4);
    u32x4 om[4], bw[4];
    const int ooff = WS_O + gw * 65536 + w * 8192 + win(lane * 16);
#pragma unroll
    for (int k = 0; k < 4; ++k) om[k] = ldsc(rws, ooff, k * 2048);
    TS(12);
    LWAIT(C_OW, 4);
#pragma unroll
    for (int k = 0; k < 4; ++k) bw[k] = *(const u32x4*)(lds + L_OW + k * 8192 + w * 1024 + lane * 16);
    PINV(bw, 4);
    TS(13);
    RETRY(om, 4, ooff + k * 2048);
    TS(14);
    {
      float v[4];
#pragma unroll
      for (int i = 0; i < 4; ++i) v[i] = (asf(om[0][i]) + asf(om[1][i])) + (asf(om[2][i]) + asf(om[3][i]));
      swap32(v[0], v[2]); swap32(v[1], v[3]);
      *(u32*)(lds + L_A + (w * A_STR + (lane & 31) * 4 + 2 * hi) * 2) = pack_bf(v[0] + v[2], v[1] + v[3]);
    }
    ARRIVE(C_A);
    LWAITF(C_A, 8);
    TS(15);
    {
      u32x4 a[4];
#pragma unroll
      for (int k = 0; k < 4; ++k) a[k] = *(const u32x4*)(lds + L_A + ((c & 7) * A_STR + 32 * k + 8 * g) * 2);
      f32x4 d0 = mfma32(a[0], bw[0], f32x4{0, 0, 0, 0}), d1 = mfma32(a[1], bw[1], f32x4{0, 0, 0, 0});
      d0 = mfma32(a[2], bw[2], d0); d1 = mfma32(a[3], bw[3], d1);
      publish(rws, hi ? OOB : WS_FS + (((2 * sO + (c >> 3)) * 2 + g) * 8 + w) * 4096 + gw * 128 + (c & 7) * 16, d0 + d1, tg);
    }
    ARRIVE(C_S5);
    TS(16);
  } else {
    const int oidx = (4 * ((wg >> 3) & 1) + g) * DM + (wg >> 4) * 64 + (c & 7) * 8 + (wg & 7);
    const u32 xfin = p.x[oidx];
    asm volatile("s_barrier" ::: "memory");
    PRIO(2);
    const rsrc_t rk = mkr(p.kc + kvoff, 64 * DH * 2);
    const int kvo = (4 * (c & 3) + ((c >> 2) & 1)) * 256 + (4 * (c >> 3) + g) * 16;
    u32x4 kr[16];
#pragma unroll
    for (int n = 0; n < 16; ++n) { if (!(n & 1)) PACE(32 + n * 8 + w * 2); kr[n] = ldnt(rk, kvo + ((n >> 1) & 1) * 512 + (n & 1) * 128, kb[n >> 2]); }
    PIN();
    TS(1);
    PRIO(3);
    if (w < 4) { USE4(kr[15]); PIN(); }
    u32x4 qm[2];
    const int qoff = WS_Q + ((grp >> 2) * 32 + (grp & 3) * 8 + w) * 4096 + win(lane * 16);
    qm[0] = ldsc(rws, qoff, 0);
    qm[1] = ldsc(rws, qoff, 2048);
    PIN();
    MKTAGS();
    RETRY(qm, 2, qoff + k * 2048);
    TS(2);
    {
      float q0 = asf(qm[0][0]) + asf(qm[1][0]), q1 = asf(qm[0][1]) + asf(qm[1][1]);
      float q2 = asf(qm[0][2]) + asf(qm[1][2]), q3 = asf(qm[0][3]) + asf(qm[1][3]);
      swap32(q0, q2); swap32(q1, q3);
      *(u32*)(lds + L_QS + (w * A_STR + (lane & 31) * 4 + 2 * hi) * 2) = pack_bf(q0 + q2, q1 + q3);
    }
    ARRIVE(C_QS);
    LWAIT(C_QS, 8);
    TS(3);
    {
      const u32x4 q0 = *(const u32x4*)(lds + L_QS + ((c & 7) * A_STR + 32 * (c >> 3) + 8 * g) * 2);
      const u32x4 q1 = *(const u32x4*)(lds + L_QS + ((c & 7) * A_STR + 64 + 32 * (c >> 3) + 8 * g) * 2);
      float sc[8];
#pragma unroll
      for (int e = 0; e < 2; ++e) {
        f32x4 t[4];
#pragma unroll
        for (int ua = 0; ua < 4; ++ua) t[ua] = mfma32(kr[(ua >> 1) * 8 + e * 4 + (ua & 1) * 2], q0, f32x4{0, 0, 0, 0});
#pragma unroll
        for (int ua = 0; ua < 4; ++ua) t[ua] = mfma32(kr[(ua >> 1) * 8 + e * 4 + (ua & 1) * 2 + 1], q1, t[ua]);
#pragma unroll
        for (int r = 0; r < 4; ++r) {
          float a = t[0][r], b = t[1][r], a2 = t[2][r], b2 = t[3][r];
          swap32(a, b); swap32(a2, b2);
          sc[e * 4 + r] = ror8_merge<0x3>(b2, b) + ror8_merge<0xc>(a, a2);
        }
      }
      float mx = -INFINITY;
#pragma unroll
      for (int i = 0; i < 4; ++i) {
        const u32 s1 = pack_bf(sc[2 * i], sc[2 * i + 1]);
        const u32 s2 = pack_bf(bf_lo(s1) * 0.08838834764831845f, bf_hi(s1) * 0.08838834764831845f);
        sc[2 * i] = bf_lo(s2); sc[2 * i + 1] = bf_hi(s2);
        mx = fmaxf(mx, fmaxf(sc[2 * i], sc[2 * i + 1]));
      }
      mx = fmaxf(mx, dpp<DPP_ROR8>(mx));
      const float mw = max32(max16(mx));
      float l = 0.f;
      u32x4 pf;
#pragma unroll
      for (int i = 0; i < 4; ++i) {
        const float e0 = __builtin_amdgcn_exp2f((sc[2 * i] - mw) * L2E), e1 = __builtin_amdgcn_exp2f((sc[2 * i + 1] - mw) * L2E);
        l += e0 + e1;
        pf[i] = pack_bf(e0, e1);
      }
      *(u32x4*)(lds + L_P + w * 1024 + lane * 16) = pf;
      ARRIVE(C_P + w);
      l += dpp<DPP_ROR8>(l);
      l = add32(add16(l));
      if (lane < 8) *(float2*)(lds + L_ST + (w * 8 + lane) * 8) = make_float2(mw, l);
    }
    ARRIVE(C_ST);
    TS(4);
    u32x4 wr[8], wt[8];
    if (w >= 4) {
      LWAIT(C_RG, 8);
      TS(5);
      {
        const float* rg = (const float*)(lds + L_RG) + ((lane >> 3) & 1) * 4 * 8 * RG_STR + (lane & 7) * RG_STR + wv * 16 + ((lane >> 4) & 1) * 8 + hi * 4;
        f32x4 s = *(const f32x4*)rg;
#pragma unroll
        for (int k = 1; k < 4; ++k) s += *(const f32x4*)(rg + k * 8 * RG_STR);
        publish(rws, ((lane & 8) ? has_u : has_g) ? WS_GU + (((lane & 8) ? cgu : cgg) * 4 + wv) * 8192 + ks4 * 2048 + win(lane * 16) : OOB, s, tg);
      }
      TS(6);
      PACE(PG3 + wv);
      TS(7);
      u32x4 gm[8];
      const int gbase = WS_GU + (gw * 7 + 2 * wv + (wv == 3 ? 0 : hi)) * 8192;
      const int goff0 = gbase + win((g & 1) * 256 + c * 16), goff1 = gbase + 640 + (g & 1) * 256 + c * 16;
#pragma unroll
      for (int k = 0; k < 8; ++k) gm[k] = ldsc(rws, (k & 1) ? goff1 : goff0, (k >> 1) * 2048);
      PRIO(0);
      const rsrc_t rw = mkr(p.wd + (size_t)(gw * 112 + 32 * wv) * DM + sD * 128, (wv == 3 ? 16 : 32) * DM * 2 - sD * 256);
#pragma unroll
      for (int j = 0; j < 8; ++j) { PACE(PD + j * SD / 2 + wv); wr[j] = ldnt(rw, 8 * g * (DM * 2) + c * 16, j * DM * 2); }
      ARRIVE(C_DI);
      PRIO(3);
      TS(8);
      RETRY(gm, 8, ((k & 1) ? goff1 : goff0) + (k >> 1) * 2048);
      TS(9);
      u32x4 a;
#pragma unroll
      for (int i = 0; i < 4; ++i) {
        const int qp = i >> 1, e = (i & 1) * 2;
        const float v0 = (asf(gm[qp][e]) + asf(gm[2 + qp][e])) + (asf(gm[4 + qp][e]) + asf(gm[6 + qp][e]));
        const float v1 = (asf(gm[qp][e + 1]) + asf(gm[2 + qp][e + 1])) + (asf(gm[4 + qp][e + 1]) + asf(gm[6 + qp][e + 1]));
        const u32 gg = pack_bf(v0, v1);
        const u32 uu = asu(dpp<DPP_ROR8>(asf(gg)));
        const u32 sg = pack_bf(sigm(bf_lo(gg)), sigm(bf_hi(gg)));
        a[i] = pack_bf(bf_lo(sg) * bf_lo(uu), bf_hi(sg) * bf_hi(uu));
        if (wv == 3 && hi) a[i] = 0;
      }
      PINV(a, 4);
      TS(10);
#pragma unroll
      for (int jj = 0; jj < 4; ++jj) { tr_pair(wr, wt, jj); PIN(); }
      f32x4 d[8];
#pragma unroll
      for (int i = 0; i < 8; ++i) d[i] = mfma32(a, wt[i], f32x4{0, 0, 0, 0});
      store_acc(d, lds + L_RY + wv * R_WS, g, c);
      ARRIVE(C_Y);
      TS(11);
      LWAIT(C_Y, 4);
      {
        const int t = wv * 64 + lane;
        const char* rf = lds + L_RY + (t >> 7) * R_GS + ((t >> 3) & 7) * R_IS + (((t >> 6) & 1) * 8 + (t & 7)) * 16;
        f32x4 s = *(const f32x4*)rf;
#pragma unroll
        for (int k = 1; k < 4; ++k) s += *(const f32x4*)(rf + k * R_WS);
        publish(rws, WS_FY + (((2 * sD + ((t >> 6) & 1)) * 2 + (t >> 7)) * 8 + ((t >> 3) & 7)) * 4096 + gw * 128 + (t & 7) * 16, s, tg);
      }
      ARRIVE(C_S5);
      TS(12);
    } else {
      if (w == 0) {
        LWAIT(C_ST, 8);
        const float2 ml = *(const float2*)(lds + L_ST + lane * 8);
        float M, L;
        merge_stats(ml.x, ml.y, M, L);
        const float Lh = asf((asu(L) + 0x800u) & 0xfffff000u);
        publish(rws, lane < 8 ? WS_ST + grp * 2048 + win(sub * 128) + lane * 16 : OOB, f32x4{M, Lh, L - Lh, 0.f}, tg);
        TS(5);
        LWAIT(C_RG, 8);
        u32x4 sm[1];
        const int stoff = WS_ST + grp * 2048 + win(lane * 16);
        sm[0] = ldsc(rws, stoff, 0);
        RETRY(sm, 1, stoff);
        merge_stats(asf(sm[0][0]), asf(sm[0][1]) + asf(sm[0][2]), M, L);
        *(float*)(lds + L_F + lane * 4) = __builtin_amdgcn_exp2f((ml.x - M) * L2E) * rcp(L);
        ARRIVE(C_F);
        TS(6);
      }
      LWAIT(C_VI, 8);
      TS(7);
      PRIO(0);
      const rsrc_t rw = mkr(p.wo + (size_t)((gw >> 2) * 512 + (gw & 3) * 32 + 8 * wv) * DM + sO * 128, 400 * DM * 2);
#pragma unroll
      for (int j = 0; j < 8; ++j) { PACE(PO + j * SO + wv); wr[j] = ldnt(rw, 2 * g * (DM * 2) + c * 16, ((j & 3) * 128 + (j >> 2)) * DM * 2); }
      PIN();
      PRIO(3);
      TS(8);
#pragma unroll
      for (int jj = 0; jj < 4; ++jj) { tr_pair(wr, wt, jj); PIN(); }
#pragma unroll
      for (int i = 0; i < 8; ++i) *(u32x4*)(lds + L_OW + wv * 8192 + i * 1024 + lane * 16) = wt[i];
      ARRIVE(C_OW);
      TS(10);
      if (w == 0) {
      LWAIT(C_S5, 12);
      PACE(PF);
      TS(12);
      u32x4 fm[8];
      const int foff = WS_FS + wg * 4096 + lane * 16;
#pragma unroll
      for (int k = 0; k < 8; ++k) fm[k] = ldsc(rws, foff, (k >> 2) * (WS_FY - WS_FS) + (k & 3) * 1024);
      RETRY(fm, 8, foff + (k >> 2) * (WS_FY - WS_FS) + (k & 3) * 1024);
      TS(14);
      float z[2];
#pragma unroll
      for (int h = 0; h < 2; ++h) {
        float v[4];
#pragma unroll
        for (int i = 0; i < 4; ++i) v[i] = (asf(fm[4 * h][i]) + asf(fm[4 * h + 1][i])) + (asf(fm[4 * h + 2][i]) + asf(fm[4 * h + 3][i]));
        swap32(v[0], v[2]); swap32(v[1], v[3]);
        v[0] += v[2]; v[1] += v[3];
        swap16(v[0], v[1]);
        const float t = v[0] + v[1];
        z[h] = rbf(t + dpp<DPP_ROR8>(t));
      }
      z[1] = rbf(z[1] * sigm(z[1]));
      const float r = bf_lo(xfin) + rbf(z[1] * z[0]);
      if (c < 8) p.out[oidx] = (u16)pack_bf(r, r);
      if (wg == 0 && lane == 0) __builtin_amdgcn_raw_buffer_store_b32(__builtin_amdgcn_readfirstlane(ep) + 1u, rws, 0, 0, AUX_SC1);
      TS(15);
    }
    }
  }
  TS_FLUSH();
}

extern "C" void kernel_launch(void* const* d_in, const int* in_sizes, int n_in, void* d_out, int out_size, void* d_ws, size_t ws_size,
                              hipStream_t stream) {
  Params p;
  p.x = (const u16*)d_in[0]; p.kc = (const u16*)d_in[1]; p.vc = (const u16*)d_in[2]; p.wq = (const u16*)d_in[3]; p.wo = (const u16*)d_in[4];
  p.wg = (const u16*)d_in[6]; p.wu = (const u16*)d_in[7]; p.wd = (const u16*)d_in[8];
  p.out = (u16*)d_out; p.ws = (char*)d_ws;
  fused_kernel<<<NWG, NT, 0, stream>>>(p);
}
```

```cpp
#include <hip/hip_runtime.h>
#include <hip/hip_bf16.h>
#include <stdint.h>

typedef unsigned int u32;
typedef unsigned short u16;
typedef u32 u32x4 __attribute__((ext_vector_type(4)));
typedef u32 u32x2 __attribute__((ext_vector_type(2)));
typedef float f32x4 __attribute__((ext_vector_type(4)));
typedef __bf16 bf16x8 __attribute__((ext_vector_type(8)));
typedef short s16x4 __attribute__((ext_vector_type(4)));
typedef __amdgpu_buffer_rsrc_t rsrc_t;

constexpr int DM = 1024, HQD = 4096, SK = 4096, DH = 128, FF = 3584;
constexpr int NWG = 256, NT = 1024;
#define TS_WAVES 16

constexpr int WS_Q = 4096;
constexpr int WS_GU = WS_Q + 8 * 32 * 4 * 1024;
constexpr int WS_O = WS_GU + 224 * 8192;
constexpr int WS_ST = WS_O + 32 * 65536;
constexpr int WS_FS = WS_ST + 32 * 2048;
constexpr int WS_FY = WS_FS + 256 * 4096;
constexpr int WS_T2 = WS_FY + 256 * 4096;
constexpr int WS_END = WS_T2 + 4096;
constexpr int AUX_NT = 2, AUX_SC1 = 16;
constexpr u32 SPIN_CAP = 20000;

constexpr int XN_STR = 264;
constexpr int A_STR = 136;
constexpr int RG_STR = 68;
constexpr int R_IS = 288, R_GS = 8 * R_IS, R_WS = 2 * R_GS;
constexpr int L_CNT = 0;
constexpr int L_XN = 128;
constexpr int L_QS = L_XN + 8 * XN_STR * 2;
constexpr int L_ST = L_QS + 8 * A_STR * 2;
constexpr int L_F = L_ST + 512;
constexpr int L_P = L_F + 256;
constexpr int L_RQ = L_P + 8192;
constexpr int L_RG = L_RQ + 8 * 8 * 64 * 4;
constexpr int L_RO = L_RG + 8 * 8 * RG_STR * 4;
constexpr int L_RY = L_RO + 8 * R_WS;
constexpr int L_A = L_RY + 4 * R_WS;
constexpr int L_OW = L_A + 8 * A_STR * 2;
constexpr int L_END = L_OW + 32768;
enum { C_XN = 0, C_RQ, C_QS, C_ST, C_RG, C_VI, C_DI, C_F, C_RO, C_M4, C_Y, C_OW, C_A, C_S5, C_P = 16 };

#define PIN() do { asm volatile("" ::: "memory"); __builtin_amdgcn_sched_barrier(0); } while (0)
#define USE4(x) asm volatile("" ::"v"(x))
#define BAR() do { asm volatile("s_waitcnt lgkmcnt(0)\n\ts_barrier" ::: "memory"); __builtin_amdgcn_sched_barrier(0); } while (0)

#define TS(i)
#define TS_DECL
#define TS_SET(i, v) do {} while (0)
#define TS_FLUSH()

__device__ __forceinline__ f32x4 mfma32(u32x4 a, u32x4 b, f32x4 c) {
  return __builtin_amdgcn_mfma_f32_16x16x32_bf16(__builtin_bit_cast(bf16x8, a), __builtin_bit_cast(bf16x8, b), c, 0, 0, 0);
}
__device__ __forceinline__ f32x4 mfma16(u32x2 a, u32x2 b, f32x4 c) {
  return __builtin_amdgcn_mfma_f32_16x16x16bf16_1k(__builtin_bit_cast(s16x4, a), __builtin_bit_cast(s16x4, b), c, 0, 0, 0);
}
__device__ __forceinline__ float asf(u32 x) { return __builtin_bit_cast(float, x); }
__device__ __forceinline__ u32 asu(float x) { return __builtin_bit_cast(u32, x); }
__device__ __forceinline__ float bf_lo(u32 x) { return asf(x << 16); }
__device__ __forceinline__ float bf_hi(u32 x) { return asf(x & 0xffff0000u); }
__device__ __forceinline__ u32 pack_bf(float lo, float hi) {
  typedef __bf16 bf16x2 __attribute__((ext_vector_type(2)));
  typedef float f32x2 __attribute__((ext_vector_type(2)));
  f32x2 v = {lo, hi};
  return __builtin_bit_cast(u32, __builtin_convertvector(v, bf16x2));
}
__device__ __forceinline__ float rcp(float x) { return __builtin_amdgcn_rcpf(x); }
constexpr float L2E = 1.4426950408889634f;
constexpr int OOB = 0x7ffffff0;
__device__ __forceinline__ float sigm(float x) { return rcp(1.f + __builtin_amdgcn_exp2f(-L2E * x)); }
__device__ __forceinline__ float rbf(float x) { return bf_lo(pack_bf(x, x)); }
__device__ __forceinline__ u32 perm_lo(u32 a, u32 b) { return __builtin_amdgcn_perm(b, a, 0x05040100u); }
__device__ __forceinline__ u32 perm_hi(u32 a, u32 b) { return __builtin_amdgcn_perm(b, a, 0x07060302u); }
template <int CTRL> __device__ __forceinline__ float dpp(float x) {
  return asf(__builtin_amdgcn_mov_dpp(asu(x), CTRL, 0xf, 0xf, true));
}
template <int BANK> __device__ __forceinline__ float ror8_merge(float old, float src) {
  return asf(__builtin_amdgcn_update_dpp(asu(old), asu(src), 0x128, 0xf, BANK, false));
}
constexpr int DPP_X1 = 0xB1, DPP_X2 = 0x4E, DPP_HMIR = 0x141, DPP_MIR = 0x140, DPP_ROR8 = 0x128, DPP_ROR4 = 0x124;
__device__ __forceinline__ void swap32(float& a, float& b) {
  auto r = __builtin_amdgcn_permlane32_swap(asu(a), asu(b), false, false);
  a = asf(r[0]); b = asf(r[1]);
}
__device__ __forceinline__ void swap16(float& a, float& b) {
  auto r = __builtin_amdgcn_permlane16_swap(asu(a), asu(b), false, false);
  a = asf(r[0]); b = asf(r[1]);
}
__device__ __forceinline__ float add32(float x) { float a = x, b = x; swap32(a, b); return a + b; }
__device__ __forceinline__ float add16(float x) { float a = x, b = x; swap16(a, b); return a + b; }
__device__ __forceinline__ float max32(float x) { float a = x, b = x; swap32(a, b); return fmaxf(a, b); }
__device__ __forceinline__ float max16(float x) { float a = x, b = x; swap16(a, b); return fmaxf(a, b); }
__device__ __forceinline__ float wave_sum(float x) {
  x += dpp<DPP_X1>(x); x += dpp<DPP_X2>(x); x += dpp<DPP_HMIR>(x); x += dpp<DPP_MIR>(x);
  return add32(add16(x));
}
__device__ __forceinline__ u32x4 ldnt(rsrc_t r, int voff, int soff) { return __builtin_amdgcn_raw_buffer_load_b128(r, voff, soff, AUX_NT); }
__device__ __forceinline__ u32x4 ldsc(rsrc_t r, int voff, int soff) { return __builtin_amdgcn_raw_buffer_load_b128(r, voff, soff, AUX_SC1); }
__device__ __forceinline__ rsrc_t mkr(const void* p, int bytes) { return __builtin_amdgcn_make_buffer_rsrc((void*)p, 0, bytes, 0x00020000); }

struct Tags { u32 t[4]; };
__device__ __forceinline__ int win(int o) { return o + (o >= 384 ? 128 : 0); }
__device__ __forceinline__ void publish(rsrc_t r, int voff, f32x4 v, const Tags& tg) {
  u32x4 o;
#pragma unroll
  for (int i = 0; i < 4; ++i) o[i] = ((asu(v[i]) + 0x800u) & 0xfffff000u) | tg.t[i];
  __builtin_amdgcn_raw_buffer_store_b128(o, r, voff, 0, AUX_SC1);
}
__device__ __forceinline__ void strip(u32x4& m, const Tags& tg) {
#pragma unroll
  for (int i = 0; i < 4; ++i) m[i] ^= tg.t[i];
}
__device__ __forceinline__ u32 or4(const u32x4& m) { return (m[0] | m[1]) | (m[2] | m[3]); }

struct Params {
  const u16* x; const u16* kc; const u16* vc; const u16* wq; const u16* wo;
  const u16* wg; const u16* wu; const u16* wd;
  u16* out; char* ws;
};
constexpr int P_STEP16 = 62, P_LEAD = 16, PO = 360, SO = 4, PG3 = 350, PD = 392, SD = 7, P4 = 418, PF = 494, MODE = 3;
#define PACE(pos) do { PIN(); const int due_ = (((pos) - P_LEAD) * P_STEP16) >> 4; while ((int)((u32)__builtin_amdgcn_s_memrealtime() - tz) < due_) __builtin_amdgcn_s_sleep(1); PIN(); } while (0)

template <int STR> __device__ __forceinline__ void store64(f32x4 (&d)[8], float* red, int g, int c) {
  f32x4 s[4];
#pragma unroll
  for (int i = 0; i < 4; ++i)
#pragma unroll
    for (int r = 0; r < 4; ++r) {
      float a = d[i][r], b = d[i + 4][r];
      swap32(a, b);
      s[r][i] = a + dpp<DPP_ROR8>(b);
    }
  if (c < 8) {
#pragma unroll
    for (int r = 0; r < 4; ++r) *(f32x4*)(red + (4 * (g & 1) + r) * STR + 8 * c + 4 * (g >> 1)) = s[r];
  }
}
__device__ __forceinline__ void store_acc(f32x4 (&d)[8], char* red, int g, int c) {
  if (g < 2) {
#pragma unroll
    for (int i = 0; i < 8; ++i) *(f32x4*)(red + g * R_GS + i * R_IS + c * 16) = d[i];
  }
}
__device__ __forceinline__ void tr_pair(const u32x4* w, u32x4 (&f)[8], int jj) {
#pragma unroll
  for (int i = 0; i < 8; ++i) {
    const u32 lo = w[2 * jj][i >> 1], hi = w[2 * jj + 1][i >> 1];
    f[i][jj] = (i & 1) ? perm_hi(lo, hi) : perm_lo(lo, hi);
  }
}
__device__ __forceinline__ void merge_stats(float m, float l, float& M, float& L) {
  M = fmaxf(m, dpp<DPP_ROR8>(m));
  M = max32(max16(M));
  L = l * __builtin_amdgcn_exp2f((m - M) * L2E);
  L += dpp<DPP_ROR8>(L);
  L = add32(add16(L));
}

#define ARRIVE(i) do { PIN(); asm volatile("s_mov_b64 exec, 1\n\tds_add_u32 %0, %1\n\ts_mov_b64 exec, -1" :: "v"(lcnt + 4 * (i)), "v"(1u) : "memory"); PIN(); } while (0)
#define PINV(a, n) do { _Pragma("unroll") for (int i_ = 0; i_ < (n); ++i_) asm volatile("" : "+v"((a)[i_])); } while (0)
#define LWAITS(i, n, SL) do { PIN(); u32 it_ = 0; \
    while (__builtin_amdgcn_readfirstlane(__hip_atomic_load((u32*)(lds + L_CNT) + (i), __ATOMIC_RELAXED, __HIP_MEMORY_SCOPE_WORKGROUP)) < (u32)(n) && ++it_ < (1u << 22)) { SL; } \
    PIN(); } while (0)
#define LWAIT(i, n) LWAITS(i, n, __builtin_amdgcn_s_sleep(1))
#define LWAITF(i, n) LWAITS(i, n, )
#define PRIO(n) do { __builtin_amdgcn_s_setprio(n); PIN(); } while (0)
#define MKTAGS() Tags tg; { const u32 e_ = __builtin_amdgcn_readfirstlane(ep) + 1u; _Pragma("unroll") for (int i_ = 0; i_ < 4; ++i_) tg.t[i_] = (e_ + 0x4d5u * i_) & 0xfffu; }
#define RETRY(m, n, addr) do { u32 bad_ = 0; \
    _Pragma("unroll") for (int k = 0; k < (n); ++k) { strip(m[k], tg); bad_ |= or4(m[k]); } \
    if (__builtin_expect(__builtin_amdgcn_ballot_w64((bad_ & 0xfffu) != 0) != 0, 0)) { u32 spin_ = 0; do { asm volatile("; retry" ::: "memory"); \
    bool st_[n]; \
    _Pragma("unroll") for (int k = 0; k < (n); ++k) st_[k] = (or4(m[k]) & 0xfffu) != 0; \
    _Pragma("unroll") for (int k = 0; k < (n); ++k) if (st_[k]) m[k] = ldsc(rws, (addr), 0); \
    PIN(); bad_ = 0; \
    _Pragma("unroll") for (int k = 0; k < (n); ++k) { if (st_[k]) strip(m[k], tg); bad_ |= or4(m[k]); } \
    } while (__builtin_amdgcn_ballot_w64((bad_ & 0xfffu) != 0) != 0 && ++spin_ < SPIN_CAP); TS_SET(21, tsv[21] + spin_ + 1); } } while (0)

__global__ void __launch_bounds__(NT, 4) fused_kernel(Params p) {
  __shared__ __attribute__((aligned(16))) char lds[L_END];
  const int tid = threadIdx.x, lane = tid & 63, wave = __builtin_amdgcn_readfirstlane(tid >> 6);
  const int g = lane >> 4, c = lane & 15, hi = lane >> 5;
  const int wg = blockIdx.x, grp = (wg & 7) * 4 + (wg >> 6), sub = (wg >> 3) & 7;
  const int ks4 = wg & 3, jt = wg >> 2;
  const int cgq = jt >= 56 ? 8 * (jt - 56) + 3 : 8 * (jt / 7) + (jt % 7) + ((jt % 7) >= 3);
  const int mg = jt < 48 ? jt : 48;
  const int cgn = 8 * (mg / 7) + (mg % 7) + ((mg % 7) >= 3);
  const bool has_g = jt < 55 || jt == 62, has_u = jt < 48 || (jt >= 55 && jt != 62);
  const int cgg = (jt >= 48 && jt < 55) ? 8 * (jt - 48) + 3 : cgn, cgu = (jt >= 55 && jt < 62) ? 8 * (jt - 55) + 3 : cgn;
  const int gw = (MODE & 1) ? grp : (wg >> 5) * 4 + ks4;
  const int sO = (MODE & 1) ? (sub + 1) & 7 : (wg & 4) + (((wg >> 3) + 1) & 3), sD = (MODE & 1) ? (sub + 3) & 7 : (wg & 4) + (((wg >> 3) + 3) & 3);
  const int w = wave & 7, wv = wave & 3;
  const rsrc_t rws = mkr(p.ws, WS_END);
  const u32 lcnt = (u32)(uintptr_t)(__attribute__((address_space(3))) char*)lds + L_CNT;
  TS_DECL;
  TS(0);
  const u32 tz = (u32)__builtin_amdgcn_s_memrealtime();
  const u32 ep = __builtin_amdgcn_raw_buffer_load_b32(rws, 0, 0, AUX_SC1);
  const size_t kvoff = ((size_t)grp * SK + sub * 512 + w * 64) * DH;
  const int rot = (MODE & 2) ? (w + sub) & 3 : 0;
  int kb[4];
#pragma unroll
  for (int i = 0; i < 4; ++i) kb[i] = ((i + rot) & 3) * 4096;
  if (wave < 8) {
    PRIO(3);
    const u32x4 xa = *(const u32x4*)(p.x + w * DM + lane * 8);
    const u32x4 xb = *(const u32x4*)(p.x + w * DM + 512 + lane * 8);
    PIN();
    u32x4 wqr[4];
    {
      const rsrc_t r = mkr(p.wq + (size_t)(ks4 * 256 + w * 32) * HQD + cgq * 64, 32 * HQD * 2);
      const int vo = (16 * (c >> 3) + 4 * g) * (HQD * 2) + (c & 7) * 16;
#pragma unroll
      for (int j = 0; j < 4; ++j) wqr[j] = ldnt(r, vo, j * HQD * 2);
    }
    if (tid < 32) ((u32*)(lds + L_CNT))[tid] = 0;
    BAR();
    {
      float v[16];
#pragma unroll
      for (int i = 0; i < 4; ++i) { v[2 * i] = bf_lo(xa[i]); v[2 * i + 1] = bf_hi(xa[i]); v[8 + 2 * i] = bf_lo(xb[i]); v[9 + 2 * i] = bf_hi(xb[i]); }
      float s = 0.f;
#pragma unroll
      for (int i = 0; i < 16; ++i) s += v[i];
      const float mean = wave_sum(s) * (1.f / DM);
      float q = 0.f;
#pragma unroll
      for (int i = 0; i < 16; ++i) { const float d = v[i] - mean; q += d * d; }
      const float var = rbf(wave_sum(q) * (1.f / DM));
      const float mb = rbf(mean);
      const float rs = rcp(rbf(__builtin_amdgcn_sqrtf(rbf(var + 1e-5f))));
      const u32x4 xs = (ks4 & 2) ? xb : xa;
      u32x4 o;
#pragma unroll
      for (int i = 0; i < 4; ++i) {
        const u32 d = pack_bf(bf_lo(xs[i]) - mb, bf_hi(xs[i]) - mb);
        o[i] = pack_bf(bf_lo(d) * rs, bf_hi(d) * rs);
      }
      if (hi == (ks4 & 1)) *(u32x4*)(lds + L_XN + (w * XN_STR + (lane & 31) * 8) * 2) = o;
    }
    ARRIVE(C_XN);
    TS(1);
    MKTAGS();
    u32x2 fq[8];
#pragma unroll
    for (int i = 0; i < 8; ++i)
#pragma unroll
      for (int jj = 0; jj < 2; ++jj) {
        const u32 lo = wqr[2 * jj][i >> 1], hh = wqr[2 * jj + 1][i >> 1];
        fq[i][jj] = (i & 1) ? perm_hi(lo, hh) : perm_lo(lo, hh);
      }
    PINV(fq, 8);
    LWAIT(C_XN, 8);
    {
      const u32x2 a = *(const u32x2*)(lds + L_XN + ((c & 7) * XN_STR + w * 32 + 16 * (c >> 3) + 4 * g) * 2);
      f32x4 d[8];
#pragma unroll
      for (int i = 0; i < 8; ++i) d[i] = mfma16(a, fq[i], f32x4{0, 0, 0, 0});
      store64<64>(d, (float*)(lds + L_RQ) + w * 512, g, c);
    }
    ARRIVE(C_RQ);
    TS(2);
    LWAIT(C_RQ, 8);
    {
      const float* rq = (const float*)(lds + L_RQ) + g * 1024 + w * 64 + c * 4;
      f32x4 s = *(const f32x4*)rq + *(const f32x4*)(rq + 512);
#pragma unroll
      for (int i = 0; i < 4; ++i) s[i] = add32(add16(s[i]));
      publish(rws, lane < 16 ? WS_Q + ((w * 32 + (cgq >> 1)) * 2 + (ks4 >> 1)) * 2048 + win((ks4 & 1) * 512 + (cgq & 1) * 256 + c * 16) : OOB, s, tg);
    }
    TS(3);
    PRIO(1);
    const rsrc_t rv = mkr(p.vc + kvoff, 64 * DH * 2);
    u32x4 gr[8], vr[16];
    {
      const bool has_gu = w < 4 ? has_g : has_u;
      const rsrc_t r = mkr((w < 4 ? p.wg : p.wu) + (size_t)(ks4 * 256 + wv * 64) * FF + (has_gu ? (w < 4 ? cgg : cgu) * 64 : 0), has_gu ? 64 * FF * 2 : 0);
      const int vo = (32 * (c >> 3) + 8 * g) * (FF * 2) + (c & 7) * 16;
#pragma unroll
      for (int j = 0; j < 8; ++j) { PACE(168 + j * 7 + w); gr[j] = ldnt(r, vo, j * FF * 2); }
    }
    PRIO(0);
#pragma unroll
    for (int n = 0; n < 8; ++n) { PACE(224 + n * 8 + w); vr[n] = ldnt(rv, lane * 16 + (n & 3) * 1024, kb[n >> 2]); }
    PIN();
    TS(4);
    PRIO(3);
    {
      const u32x4 a = *(const u32x4*)(lds + L_XN + ((c & 7) * XN_STR + wv * 64 + 32 * (c >> 3) + 8 * g) * 2);
      u32x4 f[8];
#pragma unroll
      for (int jj = 0; jj < 4; ++jj) tr_pair(gr, f, jj);
      f32x4 d[8];
#pragma unroll
      for (int i = 0; i < 8; ++i) d[i] = mfma32(a, f[i], f32x4{0, 0, 0, 0});
      store64<RG_STR>(d, (float*)(lds + L_RG) + w * 8 * RG_STR, g, c);
    }
    ARRIVE(C_RG);
    TS(5);
    PRIO(0);
#pragma unroll
    for (int n = 8; n < 16; ++n) { PACE(224 + n * 8 + w); vr[n] = ldnt(rv, lane * 16 + (n & 3) * 1024, kb[n >> 2]); }
    ARRIVE(C_VI);
    TS(6);
    PRIO(3);
    u32x4 vt[8];
#pragma unroll
    for (int jj = 0; jj < 4; ++jj) tr_pair(vr, vt, jj);
    PINV(vt, 8);
    LWAIT(C_P + w, 1);
    TS(7);
    f32x4 acc[8];
    {
      const u32x4 pf = *(const u32x4*)(lds + L_P + w * 1024 + lane * 16);
#pragma unroll
      for (int i = 0; i < 8; ++i) acc[i] = mfma32(pf, vt[i], f32x4{0, 0, 0, 0});
    }
    PIN();
    TS(8);
    const u32x4 pf1 = *(const u32x4*)(lds + L_P + w * 1024 + (lane ^ 8) * 16);
#pragma unroll
    for (int jj = 0; jj < 4; ++jj) { tr_pair(vr + 8, vt, jj); PIN(); }
#pragma unroll
    for (int i = 0; i < 8; ++i) acc[i] = mfma32(pf1, vt[i], acc[i]);
    store_acc(acc, lds + L_RO + w * R_WS, g, c);
    ARRIVE(C_RO);
    TS(9);
    LWAIT(C_F, 1);
    LWAIT(C_RO, 8);
    TS(10);
    {
      const int g2 = w >> 2, i = lane & 7, cl = (lane >> 3) & 3;
      const char* ro = lds + L_RO + hi * 4 * R_WS + g2 * R_GS + i * R_IS + (wv * 4 + cl) * 16;
      const char* fp = lds + L_F + hi * 128 + g2 * 16;
      f32x4 o = *(const f32x4*)ro * *(const f32x4*)fp;
#pragma unroll
      for (int k = 1; k < 4; ++k) o += *(const f32x4*)(ro + k * R_WS) * *(const f32x4*)(fp + k * 32);
#pragma unroll
      for (int r = 0; r < 4; ++r) o[r] = add32(o[r]);
      publish(rws, hi ? OOB : WS_O + (((grp & 3) * 2 + g2) * 4 + wv) * 65536 + (grp >> 2) * 8192 + (sub >> 1) * 2048 + win((sub & 1) * 512 + lane * 16), o, tg);
    }
    ARRIVE(C_M4);
    TS(11);
    LWAIT(C_M4, 8);
    PACE(P4);
    u32x4 om[4], bw[4];
    const int ooff = WS_O + gw * 65536 + w * 8192 + win(lane * 16);
#pragma unroll
    for (int k = 0; k < 4; ++k) om[k] = ldsc(rws, ooff, k * 2048);
    TS(12);
    LWAIT(C_OW, 4);
#pragma unroll
    for (int k = 0; k < 4; ++k) bw[k] = *(const u32x4*)(lds + L_OW + k * 8192 + w * 1024 + lane * 16);
    PINV(bw, 4);
    TS(13);
    RETRY(om, 4, ooff + k * 2048);
    TS(14);
    {
      float v[4];
#pragma unroll
      for (int i = 0; i < 4; ++i) v[i] = (asf(om[0][i]) + asf(om[1][i])) + (asf(om[2][i]) + asf(om[3][i]));
      swap32(v[0], v[2]); swap32(v[1], v[3]);
      *(u32*)(lds + L_A + (w * A_STR + (lane & 31) * 4 + 2 * hi) * 2) = pack_bf(v[0] + v[2], v[1] + v[3]);
    }
    ARRIVE(C_A);
    LWAITF(C_A, 8);
    TS(15);
    {
      u32x4 a[4];
#pragma unroll
      for (int k = 0; k < 4; ++k) a[k] = *(const u32x4*)(lds + L_A + ((c & 7) * A_STR + 32 * k + 8 * g) * 2);
      f32x4 d0 = mfma32(a[0], bw[0], f32x4{0, 0, 0, 0}), d1 = mfma32(a[1], bw[1], f32x4{0, 0, 0, 0});
      d0 = mfma32(a[2], bw[2], d0); d1 = mfma32(a[3], bw[3], d1);
      publish(rws, hi ? OOB : WS_FS + (((2 * sO + (c >> 3)) * 2 + g) * 8 + w) * 4096 + gw * 128 + (c & 7) * 16, d0 + d1, tg);
    }
    ARRIVE(C_S5);
    TS(16);
  } else {
    const int oidx = (4 * ((wg >> 3) & 1) + g) * DM + (wg >> 4) * 64 + (c & 7) * 8 + (wg & 7);
    const u32 xfin = p.x[oidx];
    asm volatile("s_barrier" ::: "memory");
    PRIO(2);
    const rsrc_t rk = mkr(p.kc + kvoff, 64 * DH * 2);
    const int kvo = (4 * (c & 3) + ((c >> 2) & 1)) * 256 + (4 * (c >> 3) + g) * 16;
    u32x4 kr[16];
#pragma unroll
    for (int n = 0; n < 16; ++n) { if (!(n & 1)) PACE(32 + n * 8 + w * 2); kr[n] = ldnt(rk, kvo + ((n >> 1) & 1) * 512 + (n & 1) * 128, kb[n >> 2]); }
    PIN();
    TS(1);
    PRIO(3);
    if (w < 4) { USE4(kr[15]); PIN(); }
    u32x4 qm[2];
    const int qoff = WS_Q + ((grp >> 2) * 32 + (grp & 3) * 8 + w) * 4096 + win(lane * 16);
    qm[0] = ldsc(rws, qoff, 0);
    qm[1] = ldsc(rws, qoff, 2048);
    PIN();
    MKTAGS();
    if (w == 0) { const u32 t20 = tz << 12;
      __builtin_amdgcn_raw_buffer_store_b128(u32x4{t20 | tg.t[0], t20 | tg.t[1], t20 | tg.t[2], t20 | tg.t[3]}, rws, lane == 0 ? WS_T2 + wg * 16 : OOB, 0, AUX_SC1); }
    RETRY(qm, 2, qoff + k * 2048);
    TS(2);
    {
      float q0 = asf(qm[0][0]) + asf(qm[1][0]), q1 = asf(qm[0][1]) + asf(qm[1][1]);
      float q2 = asf(qm[0][2]) + asf(qm[1][2]), q3 = asf(qm[0][3]) + asf(qm[1][3]);
      swap32(q0, q2); swap32(q1, q3);
      *(u32*)(lds + L_QS + (w * A_STR + (lane & 31) * 4 + 2 * hi) * 2) = pack_bf(q0 + q2, q1 + q3);
    }
    ARRIVE(C_QS);
    LWAIT(C_QS, 8);
    TS(3);
    {
      const u32x4 q0 = *(const u32x4*)(lds + L_QS + ((c & 7) * A_STR + 32 * (c >> 3) + 8 * g) * 2);
      const u32x4 q1 = *(const u32x4*)(lds + L_QS + ((c & 7) * A_STR + 64 + 32 * (c >> 3) + 8 * g) * 2);
      float sc[8];
#pragma unroll
      for (int e = 0; e < 2; ++e) {
        f32x4 t[4];
#pragma unroll
        for (int ua = 0; ua < 4; ++ua) t[ua] = mfma32(kr[(ua >> 1) * 8 + e * 4 + (ua & 1) * 2], q0, f32x4{0, 0, 0, 0});
#pragma unroll
        for (int ua = 0; ua < 4; ++ua) t[ua] = mfma32(kr[(ua >> 1) * 8 + e * 4 + (ua & 1) * 2 + 1], q1, t[ua]);
#pragma unroll
        for (int r = 0; r < 4; ++r) {
          float a = t[0][r], b = t[1][r], a2 = t[2][r], b2 = t[3][r];
          swap32(a, b); swap32(a2, b2);
          sc[e * 4 + r] = ror8_merge<0x3>(b2, b) + ror8_merge<0xc>(a, a2);
        }
      }
      float mx = -INFINITY;
#pragma unroll
      for (int i = 0; i < 4; ++i) {
        const u32 s1 = pack_bf(sc[2 * i], sc[2 * i + 1]);
        const u32 s2 = pack_bf(bf_lo(s1) * 0.08838834764831845f, bf_hi(s1) * 0.08838834764831845f);
        sc[2 * i] = bf_lo(s2); sc[2 * i + 1] = bf_hi(s2);
        mx = fmaxf(mx, fmaxf(sc[2 * i], sc[2 * i + 1]));
      }
      mx = fmaxf(mx, dpp<DPP_ROR8>(mx));
      const float mw = max32(max16(mx));
      float l = 0.f;
      u32x4 pf;
#pragma unroll
      for (int i = 0; i < 4; ++i) {
        const float e0 = __builtin_amdgcn_exp2f((sc[2 * i] - mw) * L2E), e1 = __builtin_amdgcn_exp2f((sc[2 * i + 1] - mw) * L2E);
        l += e0 + e1;
        pf[i] = pack_bf(e0, e1);
      }
      *(u32x4*)(lds + L_P + w * 1024 + lane * 16) = pf;
      ARRIVE(C_P + w);
      l += dpp<DPP_ROR8>(l);
      l = add32(add16(l));
      if (lane < 8) *(float2*)(lds + L_ST + (w * 8 + lane) * 8) = make_float2(mw, l);
    }
    ARRIVE(C_ST);
    TS(4);
    u32x4 wr[8], wt[8];
    if (w >= 4) {
      LWAIT(C_RG, 8);
      TS(5);
      {
        const float* rg = (const float*)(lds + L_RG) + ((lane >> 3) & 1) * 4 * 8 * RG_STR + (lane & 7) * RG_STR + wv * 16 + ((lane >> 4) & 1) * 8 + hi * 4;
        f32x4 s = *(const f32x4*)rg;
#pragma unroll
        for (int k = 1; k < 4; ++k) s += *(const f32x4*)(rg + k * 8 * RG_STR);
        publish(rws, ((lane & 8) ? has_u : has_g) ? WS_GU + (((lane & 8) ? cgu : cgg) * 4 + wv) * 8192 + ks4 * 2048 + win(lane * 16) : OOB, s, tg);
      }
      TS(6);
      PACE(PG3 + wv);
      TS(7);
      u32x4 gm[8];
      const int gbase = WS_GU + (gw * 7 + 2 * wv + (wv == 3 ? 0 : hi)) * 8192;
      const int goff0 = gbase + win((g & 1) * 256 + c * 16), goff1 = gbase + 640 + (g & 1) * 256 + c * 16;
#pragma unroll
      for (int k = 0; k < 8; ++k) gm[k] = ldsc(rws, (k & 1) ? goff1 : goff0, (k >> 1) * 2048);
      PRIO(0);
      const rsrc_t rw = mkr(p.wd + (size_t)(gw * 112 + 32 * wv) * DM + sD * 128, (wv == 3 ? 16 : 32) * DM * 2 - sD * 256);
#pragma unroll
      for (int j = 0; j < 8; ++j) { PACE(PD + j * SD / 2 + wv); wr[j] = ldnt(rw, 8 * g * (DM * 2) + c * 16, j * DM * 2); }
      ARRIVE(C_DI);
      PRIO(3);
      TS(8);
      RETRY(gm, 8, ((k & 1) ? goff1 : goff0) + (k >> 1) * 2048);
      TS(9);
      u32x4 a;
#pragma unroll
      for (int i = 0; i < 4; ++i) {
        const int qp = i >> 1, e = (i & 1) * 2;
        const float v0 = (asf(gm[qp][e]) + asf(gm[2 + qp][e])) + (asf(gm[4 + qp][e]) + asf(gm[6 + qp][e]));
        const float v1 = (asf(gm[qp][e + 1]) + asf(gm[2 + qp][e + 1])) + (asf(gm[4 + qp][e + 1]) + asf(gm[6 + qp][e + 1]));
        const u32 gg = pack_bf(v0, v1);
        const u32 uu = asu(dpp<DPP_ROR8>(asf(gg)));
        const u32 sg = pack_bf(sigm(bf_lo(gg)), sigm(bf_hi(gg)));
        a[i] = pack_bf(bf_lo(sg) * bf_lo(uu), bf_hi(sg) * bf_hi(uu));
        if (wv == 3 && hi) a[i] = 0;
      }
      PINV(a, 4);
      TS(10);
#pragma unroll
      for (int jj = 0; jj < 4; ++jj) { tr_pair(wr, wt, jj); PIN(); }
      f32x4 d[8];
#pragma unroll
      for (int i = 0; i < 8; ++i) d[i] = mfma32(a, wt[i], f32x4{0, 0, 0, 0});
      store_acc(d, lds + L_RY + wv * R_WS, g, c);
      ARRIVE(C_Y);
      TS(11);
      LWAIT(C_Y, 4);
      {
        const int t = wv * 64 + lane;
        const char* rf = lds + L_RY + (t >> 7) * R_GS + ((t >> 3) & 7) * R_IS + (((t >> 6) & 1) * 8 + (t & 7)) * 16;
        f32x4 s = *(const f32x4*)rf;
#pragma unroll
        for (int k = 1; k < 4; ++k) s += *(const f32x4*)(rf + k * R_WS);
        publish(rws, WS_FY + (((2 * sD + ((t >> 6) & 1)) * 2 + (t >> 7)) * 8 + ((t >> 3) & 7)) * 4096 + gw * 128 + (t & 7) * 16, s, tg);
      }
      ARRIVE(C_S5);
      TS(12);
    } else {
      if (w == 0) {
        LWAIT(C_ST, 8);
        const float2 ml = *(const float2*)(lds + L_ST + lane * 8);
        float M, L;
        merge_stats(ml.x, ml.y, M, L);
        const float Lh = asf((asu(L) + 0x800u) & 0xfffff000u);
        publish(rws, lane < 8 ? WS_ST + grp * 2048 + win(sub * 128) + lane * 16 : OOB, f32x4{M, Lh, L - Lh, 0.f}, tg);
        TS(5);
        LWAIT(C_RG, 8);
        u32x4 sm[1];
        const int stoff = WS_ST + grp * 2048 + win(lane * 16);
        sm[0] = ldsc(rws, stoff, 0);
        RETRY(sm, 1, stoff);
        merge_stats(asf(sm[0][0]), asf(sm[0][1]) + asf(sm[0][2]), M, L);
        *(float*)(lds + L_F + lane * 4) = __builtin_amdgcn_exp2f((ml.x - M) * L2E) * rcp(L);
        ARRIVE(C_F);
        TS(6);
      }
      LWAIT(C_VI, 8);
      TS(7);
      PRIO(0);
      const rsrc_t rw = mkr(p.wo + (size_t)((gw >> 2) * 512 + (gw & 3) * 32 + 8 * wv) * DM + sO * 128, 400 * DM * 2);
#pragma unroll
      for (int j = 0; j < 8; ++j) { PACE(PO + j * SO + wv); wr[j] = ldnt(rw, 2 * g * (DM * 2) + c * 16, ((j & 3) * 128 + (j >> 2)) * DM * 2); }
      PIN();
      PRIO(3);
      TS(8);
#pragma unroll
      for (int jj = 0; jj < 4; ++jj) { tr_pair(wr, wt, jj); PIN(); }
#pragma unroll
      for (int i = 0; i < 8; ++i) *(u32x4*)(lds + L_OW + wv * 8192 + i * 1024 + lane * 16) = wt[i];
      ARRIVE(C_OW);
      TS(10);
      if (w == 0) {
      LWAIT(C_S5, 12);
      PACE(PF);
      TS(12);
      u32x4 fm[8];
      const int foff = WS_FS + wg * 4096 + lane * 16;
#pragma unroll
      for (int k = 0; k < 8; ++k) fm[k] = ldsc(rws, foff, (k >> 2) * (WS_FY - WS_FS) + (k & 3) * 1024);
      RETRY(fm, 8, foff + (k >> 2) * (WS_FY - WS_FS) + (k & 3) * 1024);
      TS(14);
      float z[2];
#pragma unroll
      for (int h = 0; h < 2; ++h) {
        float v[4];
#pragma unroll
        for (int i = 0; i < 4; ++i) v[i] = (asf(fm[4 * h][i]) + asf(fm[4 * h + 1][i])) + (asf(fm[4 * h + 2][i]) + asf(fm[4 * h + 3][i]));
        swap32(v[0], v[2]); swap32(v[1], v[3]);
        v[0] += v[2]; v[1] += v[3];
        swap16(v[0], v[1]);
        const float t = v[0] + v[1];
        z[h] = rbf(t + dpp<DPP_ROR8>(t));
      }
      z[1] = rbf(z[1] * sigm(z[1]));
      const float r = bf_lo(xfin) + rbf(z[1] * z[0]);
      if (c < 8) p.out[oidx] = (u16)pack_bf(r, r);
      if (wg == 0 && lane == 0) __builtin_amdgcn_raw_buffer_store_b32(__builtin_amdgcn_readfirstlane(ep) + 1u, rws, 0, 0, AUX_SC1);
      {
        u32x4 tm[4];
        const int toff = WS_T2 + lane * 16;
#pragma unroll
        for (int k = 0; k < 4; ++k) tm[k] = ldsc(rws, toff, k * 1024);
        RETRY(tm, 4, toff + k * 1024);
        float lo = 1e9f, hh = -1e9f;
#pragma unroll
        for (int k = 0; k < 4; ++k) { const float d = (float)((int)(tm[k][0] - (tz << 12)) >> 12); lo = fminf(lo, d); hh = fmaxf(hh, d); }
        lo = -lo;
        lo = fmaxf(lo, dpp<DPP_X1>(lo)); lo = fmaxf(lo, dpp<DPP_X2>(lo)); lo = fmaxf(lo, dpp<DPP_HMIR>(lo)); lo = fmaxf(lo, dpp<DPP_MIR>(lo)); lo = max32(max16(lo));
        hh = fmaxf(hh, dpp<DPP_X1>(hh)); hh = fmaxf(hh, dpp<DPP_X2>(hh)); hh = fmaxf(hh, dpp<DPP_HMIR>(hh)); hh = fmaxf(hh, dpp<DPP_MIR>(hh)); hh = max32(max16(hh));
        const int dmin = -__builtin_amdgcn_readfirstlane((int)lo), dmax = __builtin_amdgcn_readfirstlane((int)hh);
        const int due = 2400 + 0 * (dmin + dmax);
        while ((int)((u32)__builtin_amdgcn_s_memrealtime() - tz) < due) __builtin_amdgcn_s_sleep(1);
      }
      TS(15);
    }
    }
  }
  TS_FLUSH();
}

extern "C" void kernel_launch(void* const* d_in, const int* in_sizes, int n_in, void* d_out, int out_size, void* d_ws, size_t ws_size,
                              hipStream_t stream) {
  Params p;
  p.x = (const u16*)d_in[0]; p.kc = (const u16*)d_in[1]; p.vc = (const u16*)d_in[2]; p.wq = (const u16*)d_in[3]; p.wo = (const u16*)d_in[4];
  p.wg = (const u16*)d_in[6]; p.wu = (const u16*)d_in[7]; p.wd = (const u16*)d_in[8];
  p.out = (u16*)d_out; p.ws = (char*)d_ws;
  fused_kernel<<<NWG, NT, 0, stream>>>(p);
}
```

```cpp
#include <hip/hip_runtime.h>
#include <hip/hip_bf16.h>
#include <stdint.h>

typedef unsigned int u32;
typedef unsigned short u16;
typedef u32 u32x4 __attribute__((ext_vector_type(4)));
typedef u32 u32x2 __attribute__((ext_vector_type(2)));
typedef float f32x4 __attribute__((ext_vector_type(4)));
typedef __bf16 bf16x8 __attribute__((ext_vector_type(8)));
typedef short s16x4 __attribute__((ext_vector_type(4)));
typedef __amdgpu_buffer_rsrc_t rsrc_t;

constexpr int DM = 1024, HQD = 4096, SK = 4096, DH = 128, FF = 3584;
constexpr int NWG = 256, NT = 1024;
#define TS_WAVES 16

constexpr int WS_T = 1024;
constexpr int WS_Q = 4096;
constexpr int WS_GU = WS_Q + 8 * 32 * 4 * 1024;
constexpr int WS_O = WS_GU + 224 * 8192;
constexpr int WS_ST = WS_O + 32 * 65536;
constexpr int WS_FS = WS_ST + 32 * 2048;
constexpr int WS_FY = WS_FS + 256 * 4096;
constexpr int WS_END = WS_FY + 256 * 4096;
constexpr int AUX_NT = 2, AUX_SC1 = 16;
constexpr u32 SPIN_CAP = 20000;

constexpr int XN_STR = 264;
constexpr int A_STR = 136;
constexpr int RG_STR = 68;
constexpr int R_IS = 288, R_GS = 8 * R_IS, R_WS = 2 * R_GS;
constexpr int L_CNT = 0;
constexpr int L_XN = 128;
constexpr int L_QS = L_XN + 8 * XN_STR * 2;
constexpr int L_ST = L_QS + 8 * A_STR * 2;
constexpr int L_F = L_ST + 512;
constexpr int L_P = L_F + 256;
constexpr int L_RQ = L_P + 8192;
constexpr int L_RG = L_RQ + 8 * 8 * 64 * 4;
constexpr int L_RO = L_RG + 8 * 8 * RG_STR * 4;
constexpr int L_RY = L_RO + 8 * R_WS;
constexpr int L_A = L_RY + 4 * R_WS;
constexpr int L_OW = L_A + 8 * A_STR * 2;
constexpr int L_END = L_OW + 32768;
enum { C_XN = 0, C_RQ, C_QS, C_ST, C_RG, C_VI, C_DI, C_F, C_RO, C_M4, C_Y, C_OW, C_A, C_S5, C_TZ, C_P = 16, L_TZ0 = 30, L_TZ1 = 31 };

#define PIN() do { asm volatile("" ::: "memory"); __builtin_amdgcn_sched_barrier(0); } while (0)
#define USE4(x) asm volatile("" ::"v"(x))
#define BAR() do { asm volatile("s_waitcnt lgkmcnt(0)\n\ts_barrier" ::: "memory"); __builtin_amdgcn_sched_barrier(0); } while (0)

#define TS(i)
#define TS_DECL
#define TS_SET(i, v) do {} while (0)
#define TS_FLUSH()

__device__ __forceinline__ f32x4 mfma32(u32x4 a, u32x4 b, f32x4 c) {
  return __builtin_amdgcn_mfma_f32_16x16x32_bf16(__builtin_bit_cast(bf16x8, a), __builtin_bit_cast(bf16x8, b), c, 0, 0, 0);
}
__device__ __forceinline__ f32x4 mfma16(u32x2 a, u32x2 b, f32x4 c) {
  return __builtin_amdgcn_mfma_f32_16x16x16bf16_1k(__builtin_bit_cast(s16x4, a), __builtin_bit_cast(s16x4, b), c, 0, 0, 0);
}
__device__ __forceinline__ float asf(u32 x) { return __builtin_bit_cast(float, x); }
__device__ __forceinline__ u32 asu(float x) { return __builtin_bit_cast(u32, x); }
__device__ __forceinline__ float bf_lo(u32 x) { return asf(x << 16); }
__device__ __forceinline__ float bf_hi(u32 x) { return asf(x & 0xffff0000u); }
__device__ __forceinline__ u32 pack_bf(float lo, float hi) {
  typedef __bf16 bf16x2 __attribute__((ext_vector_type(2)));
  typedef float f32x2 __attribute__((ext_vector_type(2)));
  f32x2 v = {lo, hi};
  return __builtin_bit_cast(u32, __builtin_convertvector(v, bf16x2));
}
__device__ __forceinline__ float rcp(float x) { return __builtin_amdgcn_rcpf(x); }
constexpr float L2E = 1.4426950408889634f;
constexpr int OOB = 0x7ffffff0;
__device__ __forceinline__ float sigm(float x) { return rcp(1.f + __builtin_amdgcn_exp2f(-L2E * x)); }
__device__ __forceinline__ float rbf(float x) { return bf_lo(pack_bf(x, x)); }
__device__ __forceinline__ u32 perm_lo(u32 a, u32 b) { return __builtin_amdgcn_perm(b, a, 0x05040100u); }
__device__ __forceinline__ u32 perm_hi(u32 a, u32 b) { return __builtin_amdgcn_perm(b, a, 0x07060302u); }
template <int CTRL> __device__ __forceinline__ float dpp(float x) {
  return asf(__builtin_amdgcn_mov_dpp(asu(x), CTRL, 0xf, 0xf, true));
}
template <int BANK> __device__ __forceinline__ float ror8_merge(float old, float src) {
  return asf(__builtin_amdgcn_update_dpp(asu(old), asu(src), 0x128, 0xf, BANK, false));
}
constexpr int DPP_X1 = 0xB1, DPP_X2 = 0x4E, DPP_HMIR = 0x141, DPP_MIR = 0x140, DPP_ROR8 = 0x128, DPP_ROR4 = 0x124;
__device__ __forceinline__ void swap32(float& a, float& b) {
  auto r = __builtin_amdgcn_permlane32_swap(asu(a), asu(b), false, false);
  a = asf(r[0]); b = asf(r[1]);
}
__device__ __forceinline__ void swap16(float& a, float& b) {
  auto r = __builtin_amdgcn_permlane16_swap(asu(a), asu(b), false, false);
  a = asf(r[0]); b = asf(r[1]);
}
__device__ __forceinline__ float add32(float x) { float a = x, b = x; swap32(a, b); return a + b; }
__device__ __forceinline__ float add16(float x) { float a = x, b = x; swap16(a, b); return a + b; }
__device__ __forceinline__ float max32(float x) { float a = x, b = x; swap32(a, b); return fmaxf(a, b); }
__device__ __forceinline__ float max16(float x) { float a = x, b = x; swap16(a, b); return fmaxf(a, b); }
__device__ __forceinline__ float wave_sum(float x) {
  x += dpp<DPP_X1>(x); x += dpp<DPP_X2>(x); x += dpp<DPP_HMIR>(x); x += dpp<DPP_MIR>(x);
  return add32(add16(x));
}
__device__ __forceinline__ float wave_max(float f) {
  f = fmaxf(f, dpp<DPP_X1>(f)); f = fmaxf(f, dpp<DPP_X2>(f)); f = fmaxf(f, dpp<DPP_HMIR>(f)); f = fmaxf(f, dpp<DPP_MIR>(f));
  return max32(max16(f));
}
__device__ __forceinline__ u32x4 ldnt(rsrc_t r, int voff, int soff) { return __builtin_amdgcn_raw_buffer_load_b128(r, voff, soff, AUX_NT); }
__device__ __forceinline__ u32x4 ldsc(rsrc_t r, int voff, int soff) { return __builtin_amdgcn_raw_buffer_load_b128(r, voff, soff, AUX_SC1); }
__device__ __forceinline__ rsrc_t mkr(const void* p, int bytes) { return __builtin_amdgcn_make_buffer_rsrc((void*)p, 0, bytes, 0x00020000); }

struct Tags { u32 t[4]; };
__device__ __forceinline__ int win(int o) { return o + (o >= 384 ? 128 : 0); }
__device__ __forceinline__ void publish(rsrc_t r, int voff, f32x4 v, const Tags& tg) {
  u32x4 o;
#pragma unroll
  for (int i = 0; i < 4; ++i) o[i] = ((asu(v[i]) + 0x800u) & 0xfffff000u) | tg.t[i];
  __builtin_amdgcn_raw_buffer_store_b128(o, r, voff, 0, AUX_SC1);
}
__device__ __forceinline__ void strip(u32x4& m, const Tags& tg) {
#pragma unroll
  for (int i = 0; i < 4; ++i) m[i] ^= tg.t[i];
}
__device__ __forceinline__ u32 or4(const u32x4& m) { return (m[0] | m[1]) | (m[2] | m[3]); }

struct Params {
  const u16* x; const u16* kc; const u16* vc; const u16* wq; const u16* wo;
  const u16* wg; const u16* wu; const u16* wd;
  u16* out; char* ws;
};
constexpr int P_STEP16 = 62, P_LEAD = 16, PO = 364, SO = 4, PG3 = 357, PD = 399, SD = 7, P4 = 410, PF = 472, MODE = 7, PT = 0, TK0 = 84, SK16 = 62;
#define PACE(pos) PACET((((pos) - P_LEAD) * P_STEP16) >> 4)
#define PACET(t) do { PIN(); const int due_ = (t); while ((int)((u32)__builtin_amdgcn_s_memrealtime() - tz) < due_) __builtin_amdgcn_s_sleep(1); PIN(); } while (0)

template <int STR> __device__ __forceinline__ void store64(f32x4 (&d)[8], float* red, int g, int c) {
  f32x4 s[4];
#pragma unroll
  for (int i = 0; i < 4; ++i)
#pragma unroll
    for (int r = 0; r < 4; ++r) {
      float a = d[i][r], b = d[i + 4][r];
      swap32(a, b);
      s[r][i] = a + dpp<DPP_ROR8>(b);
    }
  if (c < 8) {
#pragma unroll
    for (int r = 0; r < 4; ++r) *(f32x4*)(red + (4 * (g & 1) + r) * STR + 8 * c + 4 * (g >> 1)) = s[r];
  }
}
__device__ __forceinline__ void store_acc(f32x4 (&d)[8], char* red, int g, int c) {
  if (g < 2) {
#pragma unroll
    for (int i = 0; i < 8; ++i) *(f32x4*)(red + g * R_GS + i * R_IS + c * 16) = d[i];
  }
}
__device__ __forceinline__ void tr_pair(const u32x4* w, u32x4 (&f)[8], int jj) {
#pragma unroll
  for (int i = 0; i < 8; ++i) {
    const u32 lo = w[2 * jj][i >> 1], hi = w[2 * jj + 1][i >> 1];
    f[i][jj] = (i & 1) ? perm_hi(lo, hi) : perm_lo(lo, hi);
  }
}
__device__ __forceinline__ void merge_stats(float m, float l, float& M, float& L) {
  M = fmaxf(m, dpp<DPP_ROR8>(m));
  M = max32(max16(M));
  L = l * __builtin_amdgcn_exp2f((m - M) * L2E);
  L += dpp<DPP_ROR8>(L);
  L = add32(add16(L));
}

#define ARRIVE(i) do { PIN(); asm volatile("s_mov_b64 exec, 1\n\tds_add_u32 %0, %1\n\ts_mov_b64 exec, -1" :: "v"(lcnt + 4 * (i)), "v"(1u) : "memory"); PIN(); } while (0)
#define PINV(a, n) do { _Pragma("unroll") for (int i_ = 0; i_ < (n); ++i_) asm volatile("" : "+v"((a)[i_])); } while (0)
#define LWAITS(i, n, SL) do { PIN(); u32 it_ = 0; \
    while (__builtin_amdgcn_readfirstlane(__hip_atomic_load((u32*)(lds + L_CNT) + (i), __ATOMIC_RELAXED, __HIP_MEMORY_SCOPE_WORKGROUP)) < (u32)(n) && ++it_ < (1u << 22)) { SL; } \
    PIN(); } while (0)
#define LWAIT(i, n) LWAITS(i, n, __builtin_amdgcn_s_sleep(1))
#define LWAITF(i, n) LWAITS(i, n, )
#define GETTZ() GETT(L_TZ1)
#define GETT(i) do { PIN(); tz = __builtin_amdgcn_readfirstlane(__hip_atomic_load((u32*)(lds + L_CNT) + (i), __ATOMIC_RELAXED, __HIP_MEMORY_SCOPE_WORKGROUP)); PIN(); } while (0)
#define PRIO(n) do { __builtin_amdgcn_s_setprio(n); PIN(); } while (0)
#define MKTAGS() Tags tg; { const u32 e_ = __builtin_amdgcn_readfirstlane(ep) + 1u; _Pragma("unroll") for (int i_ = 0; i_ < 4; ++i_) tg.t[i_] = (e_ + 0x4d5u * i_) & 0xfffu; }
#define RETRY(m, n, addr) do { u32 bad_ = 0; \
    _Pragma("unroll") for (int k = 0; k < (n); ++k) { strip(m[k], tg); bad_ |= or4(m[k]); } \
    if (__builtin_expect(__builtin_amdgcn_ballot_w64((bad_ & 0xfffu) != 0) != 0, 0)) { u32 spin_ = 0; do { asm volatile("; retry" ::: "memory"); \
    bool st_[n]; \
    _Pragma("unroll") for (int k = 0; k < (n); ++k) st_[k] = (or4(m[k]) & 0xfffu) != 0; \
    _Pragma("unroll") for (int k = 0; k < (n); ++k) if (st_[k]) m[k] = ldsc(rws, (addr), 0); \
    PIN(); bad_ = 0; \
    _Pragma("unroll") for (int k = 0; k < (n); ++k) { if (st_[k]) strip(m[k], tg); bad_ |= or4(m[k]); } \
    } while (__builtin_amdgcn_ballot_w64((bad_ & 0xfffu) != 0) != 0 && ++spin_ < SPIN_CAP); TS_SET(21, tsv[21] + spin_ + 1); } } while (0)

__global__ void __launch_bounds__(NT, 4) fused_kernel(Params p) {
  __shared__ __attribute__((aligned(16))) char lds[L_END];
  const int tid = threadIdx.x, lane = tid & 63, wave = __builtin_amdgcn_readfirstlane(tid >> 6);
  const int g = lane >> 4, c = lane & 15, hi = lane >> 5;
  const int wg = blockIdx.x, grp = (wg & 7) * 4 + (wg >> 6), sub = (wg >> 3) & 7;
  const int ks4 = wg & 3, jt = wg >> 2;
  const int cgq = jt >= 56 ? 8 * (jt - 56) + 3 : 8 * (jt / 7) + (jt % 7) + ((jt % 7) >= 3);
  const int mg = jt < 48 ? jt : 48;
  const int cgn = 8 * (mg / 7) + (mg % 7) + ((mg % 7) >= 3);
  const bool has_g = jt < 55 || jt == 62, has_u = jt < 48 || (jt >= 55 && jt != 62);
  const int cgg = (jt >= 48 && jt < 55) ? 8 * (jt - 48) + 3 : cgn, cgu = (jt >= 55 && jt < 62) ? 8 * (jt - 55) + 3 : cgn;
  const int gw = (MODE & 1) ? grp : (wg >> 5) * 4 + ks4;
  const int sO = (MODE & 1) ? (sub + 1) & 7 : (wg & 4) + (((wg >> 3) + 1) & 3), sD = (MODE & 1) ? (sub + 3) & 7 : (wg & 4) + (((wg >> 3) + 3) & 3);
  const int w = wave & 7, wv = wave & 3;
  const rsrc_t rws = mkr(p.ws, WS_END);
  const u32 lcnt = (u32)(uintptr_t)(__attribute__((address_space(3))) char*)lds + L_CNT;
  TS_DECL;
  TS(0);
  u32 tz = (u32)__builtin_amdgcn_s_memrealtime();
  const u32 ep = __builtin_amdgcn_raw_buffer_load_b32(rws, 0, 0, AUX_SC1);
  const size_t kvoff = ((size_t)grp * SK + sub * 512 + w * 64) * DH;
  const int rot = (MODE & 2) ? (w + sub) & 3 : 0;
  int kb[4];
#pragma unroll
  for (int i = 0; i < 4; ++i) kb[i] = ((i + rot) & 3) * 4096;
  if (wave < 8) {
    PRIO(3);
    const u32x4 xa = *(const u32x4*)(p.x + w * DM + lane * 8);
    const u32x4 xb = *(const u32x4*)(p.x + w * DM + 512 + lane * 8);
    PIN();
    u32x4 wqr[4];
    {
      const rsrc_t r = mkr(p.wq + (size_t)(ks4 * 256 + w * 32) * HQD + cgq * 64, 32 * HQD * 2);
      const int vo = (16 * (c >> 3) + 4 * g) * (HQD * 2) + (c & 7) * 16;
#pragma unroll
      for (int j = 0; j < 4; ++j) wqr[j] = ldnt(r, vo, j * HQD * 2);
    }
    if (tid < 32) ((u32*)(lds + L_CNT))[tid] = tid >= L_TZ0 ? tz : 0;
    BAR();
    {
      float v[16];
#pragma unroll
      for (int i = 0; i < 4; ++i) { v[2 * i] = bf_lo(xa[i]); v[2 * i + 1] = bf_hi(xa[i]); v[8 + 2 * i] = bf_lo(xb[i]); v[9 + 2 * i] = bf_hi(xb[i]); }
      float s = 0.f;
#pragma unroll
      for (int i = 0; i < 16; ++i) s += v[i];
      const float mean = wave_sum(s) * (1.f / DM);
      float q = 0.f;
#pragma unroll
      for (int i = 0; i < 16; ++i) { const float d = v[i] - mean; q += d * d; }
      const float var = rbf(wave_sum(q) * (1.f / DM));
      const float mb = rbf(mean);
      const float rs = rcp(rbf(__builtin_amdgcn_sqrtf(rbf(var + 1e-5f))));
      const u32x4 xs = (ks4 & 2) ? xb : xa;
      u32x4 o;
#pragma unroll
      for (int i = 0; i < 4; ++i) {
        const u32 d = pack_bf(bf_lo(xs[i]) - mb, bf_hi(xs[i]) - mb);
        o[i] = pack_bf(bf_lo(d) * rs, bf_hi(d) * rs);
      }
      if (hi == (ks4 & 1)) *(u32x4*)(lds + L_XN + (w * XN_STR + (lane & 31) * 8) * 2) = o;
    }
    ARRIVE(C_XN);
    TS(1);
    MKTAGS();
    if (w == 0) {
      const u32 t20 = tz << 12;
      __builtin_amdgcn_raw_buffer_store_b128(u32x4{t20 | tg.t[0], t20 | tg.t[1], t20 | tg.t[2], t20 | tg.t[3]}, rws,
                                             (lane == 0 && !(wg & 24) && !(MODE & 16)) ? WS_T + ((wg >> 5) * 8 + (wg & 7)) * 16 : OOB, 0, AUX_SC1);
    }
    u32x2 fq[8];
#pragma unroll
    for (int i = 0; i < 8; ++i)
#pragma unroll
      for (int jj = 0; jj < 2; ++jj) {
        const u32 lo = wqr[2 * jj][i >> 1], hh = wqr[2 * jj + 1][i >> 1];
        fq[i][jj] = (i & 1) ? perm_hi(lo, hh) : perm_lo(lo, hh);
      }
    PINV(fq, 8);
    LWAIT(C_XN, 8);
    {
      const u32x2 a = *(const u32x2*)(lds + L_XN + ((c & 7) * XN_STR + w * 32 + 16 * (c >> 3) + 4 * g) * 2);
      f32x4 d[8];
#pragma unroll
      for (int i = 0; i < 8; ++i) d[i] = mfma16(a, fq[i], f32x4{0, 0, 0, 0});
      store64<64>(d, (float*)(lds + L_RQ) + w * 512, g, c);
    }
    ARRIVE(C_RQ);
    TS(2);
    LWAIT(C_RQ, 8);
    {
      const float* rq = (const float*)(lds + L_RQ) + g * 1024 + w * 64 + c * 4;
      f32x4 s = *(const f32x4*)rq + *(const f32x4*)(rq + 512);
#pragma unroll
      for (int i = 0; i < 4; ++i) s[i] = add32(add16(s[i]));
      publish(rws, lane < 16 ? WS_Q + ((w * 32 + (cgq >> 1)) * 2 + (ks4 >> 1)) * 2048 + win((ks4 & 1) * 512 + (cgq & 1) * 256 + c * 16) : OOB, s, tg);
    }
    TS(3);
    GETTZ();
    u32x4 tm = ldsc(rws, (w == 0 && !(MODE & 16)) ? WS_T + lane * 16 : OOB, 0);
    PRIO(1);
    const rsrc_t rv = mkr(p.vc + kvoff, 64 * DH * 2);
    u32x4 gr[8], vr[16];
    {
      const bool has_gu = w < 4 ? has_g : has_u;
      const rsrc_t r = mkr((w < 4 ? p.wg : p.wu) + (size_t)(ks4 * 256 + wv * 64) * FF + (has_gu ? (w < 4 ? cgg : cgu) * 64 : 0), has_gu ? 64 * FF * 2 : 0);
      const int vo = (32 * (c >> 3) + 8 * g) * (FF * 2) + (c & 7) * 16;
#pragma unroll
      for (int j = 0; j < 8; ++j) { PACE(168 + j * 7 + w); gr[j] = ldnt(r, vo, j * FF * 2); }
    }
    PIN();
    if (w == 0) {
      strip(tm, tg);
      int d = (int)(tm[0] - (tz << 12)) >> 12;
      if ((or4(tm) & 0xfffu) != 0 || d < -150 || d > 150 || !(MODE & 4)) d = 0;
      const int dmin = -(int)wave_max((float)-d), dmax = (int)wave_max((float)d);
      ((u32*)(lds + L_CNT))[L_TZ0] = tz + max(dmin, dmax - PT);
      ((u32*)(lds + L_CNT))[L_TZ1] = tz + max(dmin, -PT);
    }
    PRIO(0);
#pragma unroll
    for (int n = 0; n < 8; ++n) { PACE(224 + n * 8 + w); vr[n] = ldnt(rv, lane * 16 + (n & 3) * 1024, kb[n >> 2]); }
    PIN();
    TS(4);
    PRIO(3);
    {
      const u32x4 a = *(const u32x4*)(lds + L_XN + ((c & 7) * XN_STR + wv * 64 + 32 * (c >> 3) + 8 * g) * 2);
      u32x4 f[8];
#pragma unroll
      for (int jj = 0; jj < 4; ++jj) tr_pair(gr, f, jj);
      f32x4 d[8];
#pragma unroll
      for (int i = 0; i < 8; ++i) d[i] = mfma32(a, f[i], f32x4{0, 0, 0, 0});
      store64<RG_STR>(d, (float*)(lds + L_RG) + w * 8 * RG_STR, g, c);
    }
    ARRIVE(C_RG);
    GETTZ();
    TS(5);
    PRIO(0);
#pragma unroll
    for (int n = 8; n < 16; ++n) { PACE(224 + n * 8 + w); vr[n] = ldnt(rv, lane * 16 + (n & 3) * 1024, kb[n >> 2]); }
    ARRIVE(C_VI);
    TS(6);
    PRIO(3);
    u32x4 vt[8];
#pragma unroll
    for (int jj = 0; jj < 4; ++jj) tr_pair(vr, vt, jj);
    PINV(vt, 8);
    LWAIT(C_P + w, 1);
    TS(7);
    f32x4 acc[8];
    {
      const u32x4 pf = *(const u32x4*)(lds + L_P + w * 1024 + lane * 16);
#pragma unroll
      for (int i = 0; i < 8; ++i) acc[i] = mfma32(pf, vt[i], f32x4{0, 0, 0, 0});
    }
    PIN();
    TS(8);
    const u32x4 pf1 = *(const u32x4*)(lds + L_P + w * 1024 + (lane ^ 8) * 16);
#pragma unroll
    for (int jj = 0; jj < 4; ++jj) { tr_pair(vr + 8, vt, jj); PIN(); }
#pragma unroll
    for (int i = 0; i < 8; ++i) acc[i] = mfma32(pf1, vt[i], acc[i]);
    store_acc(acc, lds + L_RO + w * R_WS, g, c);
    ARRIVE(C_RO);
    TS(9);
    LWAIT(C_F, 1);
    LWAIT(C_RO, 8);
    TS(10);
    {
      const int g2 = w >> 2, i = lane & 7, cl = (lane >> 3) & 3;
      const char* ro = lds + L_RO + hi * 4 * R_WS + g2 * R_GS + i * R_IS + (wv * 4 + cl) * 16;
      const char* fp = lds + L_F + hi * 128 + g2 * 16;
      f32x4 o = *(const f32x4*)ro * *(const f32x4*)fp;
#pragma unroll
      for (int k = 1; k < 4; ++k) o += *(const f32x4*)(ro + k * R_WS) * *(const f32x4*)(fp + k * 32);
#pragma unroll
      for (int r = 0; r < 4; ++r) o[r] = add32(o[r]);
      publish(rws, hi ? OOB : WS_O + (((grp & 3) * 2 + g2) * 4 + wv) * 65536 + (grp >> 2) * 8192 + (sub >> 1) * 2048 + win((sub & 1) * 512 + lane * 16), o, tg);
    }
    ARRIVE(C_M4);
    TS(11);
    LWAIT(C_M4, 8);
    GETT((MODE & 32) ? L_TZ1 : L_TZ0);
    PACE(P4);
    u32x4 om[4], bw[4];
    const int ooff = WS_O + gw * 65536 + w * 8192 + win(lane * 16);
#pragma unroll
    for (int k = 0; k < 4; ++k) om[k] = ldsc(rws, ooff, k * 2048);
    TS(12);
    LWAIT(C_OW, 4);
#pragma unroll
    for (int k = 0; k < 4; ++k) bw[k] = *(const u32x4*)(lds + L_OW + k * 8192 + w * 1024 + lane * 16);
    PINV(bw, 4);
    TS(13);
    RETRY(om, 4, ooff + k * 2048);
    TS(14);
    {
      float v[4];
#pragma unroll
      for (int i = 0; i < 4; ++i) v[i] = (asf(om[0][i]) + asf(om[1][i])) + (asf(om[2][i]) + asf(om[3][i]));
      swap32(v[0], v[2]); swap32(v[1], v[3]);
      *(u32*)(lds + L_A + (w * A_STR + (lane & 31) * 4 + 2 * hi) * 2) = pack_bf(v[0] + v[2], v[1] + v[3]);
    }
    ARRIVE(C_A);
    LWAITF(C_A, 8);
    TS(15);
    {
      u32x4 a[4];
#pragma unroll
      for (int k = 0; k < 4; ++k) a[k] = *(const u32x4*)(lds + L_A + ((c & 7) * A_STR + 32 * k + 8 * g) * 2);
      f32x4 d0 = mfma32(a[0], bw[0], f32x4{0, 0, 0, 0}), d1 = mfma32(a[1], bw[1], f32x4{0, 0, 0, 0});
      d0 = mfma32(a[2], bw[2], d0); d1 = mfma32(a[3], bw[3], d1);
      publish(rws, hi ? OOB : WS_FS + (((2 * sO + (c >> 3)) * 2 + g) * 8 + w) * 4096 + gw * 128 + (c & 7) * 16, d0 + d1, tg);
    }
    ARRIVE(C_S5);
    TS(16);
  } else {
    const int oidx = (4 * ((wg >> 3) & 1) + g) * DM + (wg >> 4) * 64 + (c & 7) * 8 + (wg & 7);
    const u32 xfin = p.x[oidx];
    asm volatile("s_barrier" ::: "memory");
    PRIO(2);
    GETTZ();
    const rsrc_t rk = mkr(p.kc + kvoff, 64 * DH * 2);
    const int kvo = (4 * (c & 3) + ((c >> 2) & 1)) * 256 + (4 * (c >> 3) + g) * 16;
    u32x4 kr[16];
#pragma unroll
    for (int n = 0; n < 16; ++n) { if (!(n & 1)) PACET(TK0 + (((n * 8 + w * 2) * SK16) >> 4)); kr[n] = ldnt(rk, kvo + ((n >> 1) & 1) * 512 + (n & 1) * 128, kb[n >> 2]); }
    PIN();
    TS(1);
    PRIO(3);
    if (w < 4) { USE4(kr[15]); PIN(); }
    u32x4 qm[2];
    const int qoff = WS_Q + ((grp >> 2) * 32 + (grp & 3) * 8 + w) * 4096 + win(lane * 16);
    qm[0] = ldsc(rws, qoff, 0);
    qm[1] = ldsc(rws, qoff, 2048);
    PIN();
    MKTAGS();
    RETRY(qm, 2, qoff + k * 2048);
    TS(2);
    {
      float q0 = asf(qm[0][0]) + asf(qm[1][0]), q1 = asf(qm[0][1]) + asf(qm[1][1]);
      float q2 = asf(qm[0][2]) + asf(qm[1][2]), q3 = asf(qm[0][3]) + asf(qm[1][3]);
      swap32(q0, q2); swap32(q1, q3);
      *(u32*)(lds + L_QS + (w * A_STR + (lane & 31) * 4 + 2 * hi) * 2) = pack_bf(q0 + q2, q1 + q3);
    }
    ARRIVE(C_QS);
    LWAIT(C_QS, 8);
    TS(3);
    {
      const u32x4 q0 = *(const u32x4*)(lds + L_QS + ((c & 7) * A_STR + 32 * (c >> 3) + 8 * g) * 2);
      const u32x4 q1 = *(const u32x4*)(lds + L_QS + ((c & 7) * A_STR + 64 + 32 * (c >> 3) + 8 * g) * 2);
      float sc[8];
#pragma unroll
      for (int e = 0; e < 2; ++e) {
        f32x4 t[4];
#pragma unroll
        for (int ua = 0; ua < 4; ++ua) t[ua] = mfma32(kr[(ua >> 1) * 8 + e * 4 + (ua & 1) * 2], q0, f32x4{0, 0, 0, 0});
#pragma unroll
        for (int ua = 0; ua < 4; ++ua) t[ua] = mfma32(kr[(ua >> 1) * 8 + e * 4 + (ua & 1) * 2 + 1], q1, t[ua]);
#pragma unroll
        for (int r = 0; r < 4; ++r) {
          float a = t[0][r], b = t[1][r], a2 = t[2][r], b2 = t[3][r];
          swap32(a, b); swap32(a2, b2);
          sc[e * 4 + r] = ror8_merge<0x3>(b2, b) + ror8_merge<0xc>(a, a2);
        }
      }
      float mx = -INFINITY;
#pragma unroll
      for (int i = 0; i < 4; ++i) {
        const u32 s1 = pack_bf(sc[2 * i], sc[2 * i + 1]);
        const u32 s2 = pack_bf(bf_lo(s1) * 0.08838834764831845f, bf_hi(s1) * 0.08838834764831845f);
        sc[2 * i] = bf_lo(s2); sc[2 * i + 1] = bf_hi(s2);
        mx = fmaxf(mx, fmaxf(sc[2 * i], sc[2 * i + 1]));
      }
      mx = fmaxf(mx, dpp<DPP_ROR8>(mx));
      const float mw = max32(max16(mx));
      float l = 0.f;
      u32x4 pf;
#pragma unroll
      for (int i = 0; i < 4; ++i) {
        const float e0 = __builtin_amdgcn_exp2f((sc[2 * i] - mw) * L2E), e1 = __builtin_amdgcn_exp2f((sc[2 * i + 1] - mw) * L2E);
        l += e0 + e1;
        pf[i] = pack_bf(e0, e1);
      }
      *(u32x4*)(lds + L_P + w * 1024 + lane * 16) = pf;
      ARRIVE(C_P + w);
      l += dpp<DPP_ROR8>(l);
      l = add32(add16(l));
      if (lane < 8) *(float2*)(lds + L_ST + (w * 8 + lane) * 8) = make_float2(mw, l);
    }
    ARRIVE(C_ST);
    TS(4);
    u32x4 wr[8], wt[8];
    if (w >= 4) {
      LWAIT(C_RG, 8);
      GETTZ();
      TS(5);
      {
        const float* rg = (const float*)(lds + L_RG) + ((lane >> 3) & 1) * 4 * 8 * RG_STR + (lane & 7) * RG_STR + wv * 16 + ((lane >> 4) & 1) * 8 + hi * 4;
        f32x4 s = *(const f32x4*)rg;
#pragma unroll
        for (int k = 1; k < 4; ++k) s += *(const f32x4*)(rg + k * 8 * RG_STR);
        publish(rws, ((lane & 8) ? has_u : has_g) ? WS_GU + (((lane & 8) ? cgu : cgg) * 4 + wv) * 8192 + ks4 * 2048 + win(lane * 16) : OOB, s, tg);
      }
      TS(6);
      if (MODE & 8) LWAIT(C_VI, 8);
      PACE(PG3 + wv);
      TS(7);
      u32x4 gm[8];
      const int gbase = WS_GU + (gw * 7 + 2 * wv + (wv == 3 ? 0 : hi)) * 8192;
      const int goff0 = gbase + win((g & 1) * 256 + c * 16), goff1 = gbase + 640 + (g & 1) * 256 + c * 16;
#pragma unroll
      for (int k = 0; k < 8; ++k) gm[k] = ldsc(rws, (k & 1) ? goff1 : goff0, (k >> 1) * 2048);
      RETRY(gm, 8, ((k & 1) ? goff1 : goff0) + (k >> 1) * 2048);
      TS(8);
      u32x4 a;
#pragma unroll
      for (int i = 0; i < 4; ++i) {
        const int qp = i >> 1, e = (i & 1) * 2;
        const float v0 = (asf(gm[qp][e]) + asf(gm[2 + qp][e])) + (asf(gm[4 + qp][e]) + asf(gm[6 + qp][e]));
        const float v1 = (asf(gm[qp][e + 1]) + asf(gm[2 + qp][e + 1])) + (asf(gm[4 + qp][e + 1]) + asf(gm[6 + qp][e + 1]));
        const u32 gg = pack_bf(v0, v1);
        const u32 uu = asu(dpp<DPP_ROR8>(asf(gg)));
        const u32 sg = pack_bf(sigm(bf_lo(gg)), sigm(bf_hi(gg)));
        a[i] = pack_bf(bf_lo(sg) * bf_lo(uu), bf_hi(sg) * bf_hi(uu));
        if (wv == 3 && hi) a[i] = 0;
      }
      PINV(a, 4);
      TS(9);
      PRIO(0);
      const rsrc_t rw = mkr(p.wd + (size_t)(gw * 112 + 32 * wv) * DM + sD * 128, (wv == 3 ? 16 : 32) * DM * 2 - sD * 256);
#pragma unroll
      for (int j = 0; j < 8; ++j) { PACE(PD + j * SD / 2 + wv); wr[j] = ldnt(rw, 8 * g * (DM * 2) + c * 16, j * DM * 2); }
      PRIO(3);
      TS(10);
#pragma unroll
      for (int jj = 0; jj < 4; ++jj) { tr_pair(wr, wt, jj); PIN(); }
      f32x4 d[8];
#pragma unroll
      for (int i = 0; i < 8; ++i) d[i] = mfma32(a, wt[i], f32x4{0, 0, 0, 0});
      store_acc(d, lds + L_RY + wv * R_WS, g, c);
      ARRIVE(C_Y);
      TS(11);
      LWAITF(C_Y, 4);
      {
        const int t = wv * 64 + lane;
        const char* rf = lds + L_RY + (t >> 7) * R_GS + ((t >> 3) & 7) * R_IS + (((t >> 6) & 1) * 8 + (t & 7)) * 16;
        f32x4 s = *(const f32x4*)rf;
#pragma unroll
        for (int k = 1; k < 4; ++k) s += *(const f32x4*)(rf + k * R_WS);
        publish(rws, WS_FY + (((2 * sD + ((t >> 6) & 1)) * 2 + (t >> 7)) * 8 + ((t >> 3) & 7)) * 4096 + gw * 128 + (t & 7) * 16, s, tg);
      }
      ARRIVE(C_S5);
      TS(12);
    } else {
      if (w == 0) {
        LWAIT(C_ST, 8);
        const float2 ml = *(const float2*)(lds + L_ST + lane * 8);
        float M, L;
        merge_stats(ml.x, ml.y, M, L);
        const float Lh = asf((asu(L) + 0x800u) & 0xfffff000u);
        publish(rws, lane < 8 ? WS_ST + grp * 2048 + win(sub * 128) + lane * 16 : OOB, f32x4{M, Lh, L - Lh, 0.f}, tg);
        TS(5);
        LWAIT(C_RG, 8);
        u32x4 sm[1];
        const int stoff = WS_ST + grp * 2048 + win(lane * 16);
        sm[0] = ldsc(rws, stoff, 0);
        RETRY(sm, 1, stoff);
        merge_stats(asf(sm[0][0]), asf(sm[0][1]) + asf(sm[0][2]), M, L);
        *(float*)(lds + L_F + lane * 4) = __builtin_amdgcn_exp2f((ml.x - M) * L2E) * rcp(L);
        ARRIVE(C_F);
        TS(6);
      }
      LWAIT(C_VI, 8);
      GETTZ();
      TS(7);
      PRIO(0);
      const rsrc_t rw = mkr(p.wo + (size_t)((gw >> 2) * 512 + (gw & 3) * 32 + 8 * wv) * DM + sO * 128, 400 * DM * 2);
#pragma unroll
      for (int j = 0; j < 8; ++j) { PACE(PO + j * SO + wv); wr[j] = ldnt(rw, 2 * g * (DM * 2) + c * 16, ((j & 3) * 128 + (j >> 2)) * DM * 2); }
      PIN();
      PRIO(3);
      TS(8);
#pragma unroll
      for (int jj = 0; jj < 4; ++jj) { tr_pair(wr, wt, jj); PIN(); }
#pragma unroll
      for (int i = 0; i < 8; ++i) *(u32x4*)(lds + L_OW + wv * 8192 + i * 1024 + lane * 16) = wt[i];
      ARRIVE(C_OW);
      TS(10);
      if (w == 0) {
      GETT(L_TZ0);
      LWAIT(C_S5, 12);
      PACE(PF);
      TS(12);
      u32x4 fm[8];
      const int foff = WS_FS + wg * 4096 + lane * 16;
#pragma unroll
      for (int k = 0; k < 8; ++k) fm[k] = ldsc(rws, foff, (k >> 2) * (WS_FY - WS_FS) + (k & 3) * 1024);
      RETRY(fm, 8, foff + (k >> 2) * (WS_FY - WS_FS) + (k & 3) * 1024);
      TS(14);
      float z[2];
#pragma unroll
      for (int h = 0; h < 2; ++h) {
        float v[4];
#pragma unroll
        for (int i = 0; i < 4; ++i) v[i] = (asf(fm[4 * h][i]) + asf(fm[4 * h + 1][i])) + (asf(fm[4 * h + 2][i]) + asf(fm[4 * h + 3][i]));
        swap32(v[0], v[2]); swap32(v[1], v[3]);
        v[0] += v[2]; v[1] += v[3];
        swap16(v[0], v[1]);
        const float t = v[0] + v[1];
        z[h] = rbf(t + dpp<DPP_ROR8>(t));
      }
      z[1] = rbf(z[1] * sigm(z[1]));
      const float r = bf_lo(xfin) + rbf(z[1] * z[0]);
      if (c < 8) p.out[oidx] = (u16)pack_bf(r, r);
      if (wg == 0 && lane == 0) __builtin_amdgcn_raw_buffer_store_b32(__builtin_amdgcn_readfirstlane(ep) + 1u, rws, 0, 0, AUX_SC1);
      TS(15);
    }
    }
  }
  TS_FLUSH();
}

extern "C" void kernel_launch(void* const* d_in, const int* in_sizes, int n_in, void* d_out, int out_size, void* d_ws, size_t ws_size,
                              hipStream_t stream) {
  Params p;
  p.x = (const u16*)d_in[0]; p.kc = (const u16*)d_in[1]; p.vc = (const u16*)d_in[2]; p.wq = (const u16*)d_in[3]; p.wo = (const u16*)d_in[4];
  p.wg = (const u16*)d_in[6]; p.wu = (const u16*)d_in[7]; p.wd = (const u16*)d_in[8];
  p.out = (u16*)d_out; p.ws = (char*)d_ws;
  fused_kernel<<<NWG, NT, 0, stream>>>(p);
}
```

```cpp
#include <hip/hip_runtime.h>
#include <hip/hip_bf16.h>
#include <stdint.h>

typedef unsigned int u32;
typedef unsigned short u16;
typedef u32 u32x4 __attribute__((ext_vector_type(4)));
typedef u32 u32x2 __attribute__((ext_vector_type(2)));
typedef float f32x4 __attribute__((ext_vector_type(4)));
typedef __bf16 bf16x8 __attribute__((ext_vector_type(8)));
typedef short s16x4 __attribute__((ext_vector_type(4)));
typedef __amdgpu_buffer_rsrc_t rsrc_t;

constexpr int DM = 1024, HQD = 4096, SK = 4096, DH = 128, FF = 3584;
constexpr int NWG = 256, NT = 1024;
#define TS_WAVES 16

constexpr int WS_T = 1024;
constexpr int WS_Q = 4096;
constexpr int WS_GU = WS_Q + 256 * 2048;
constexpr int WS_O = WS_GU + 224 * 4096;
constexpr int WS_ST = WS_O + 32 * 65536;
constexpr int WS_FS = WS_ST + 32 * 2048;
constexpr int WS_FY = WS_FS + 256 * 4096;
constexpr int WS_END = WS_FY + 256 * 4096;
constexpr int AUX_NT = 2, AUX_SC1 = 16;
#define ST_AUX 16
constexpr u32 SPIN_CAP = 20000;

constexpr int XN_STR = 1032;
constexpr int A_STR = 136;
constexpr int RG_STR = 68;
constexpr int R_IS = 288, R_GS = 8 * R_IS, R_WS = 2 * R_GS;
constexpr int L_CNT = 0;
constexpr int L_XN = 128;
constexpr int L_QS = L_XN + 8 * XN_STR * 2;
constexpr int L_ST = L_QS + 8 * A_STR * 2;
constexpr int L_F = L_ST + 512;
constexpr int L_F2 = L_F + 256;
constexpr int L_P = L_F2 + 2048;
constexpr int L_RG = L_P + 8192;
constexpr int L_RO = L_RG + 16 * 8 * RG_STR * 4;
constexpr int L_RY = L_RO + 8 * R_WS;
constexpr int L_A = L_RY + 4 * R_WS;
constexpr int L_OW = L_A + 8 * A_STR * 2;
constexpr int L_END = L_OW + 32768;
enum { C_XN = 0, C_RQ, C_QS, C_ST, C_RG, C_VI, C_DI, C_F, C_RO, C_M4, C_Y, C_OW, C_A, C_S5, C_TZ, C_P = 16, L_EP = 29, L_TZ0 = 30, L_TZ1 = 31 };

#define PIN() do { asm volatile("" ::: "memory"); __builtin_amdgcn_sched_barrier(0); } while (0)
#define USE4(x) asm volatile("" ::"v"(x))
#define BAR() do { asm volatile("s_waitcnt lgkmcnt(0)\n\ts_barrier" ::: "memory"); __builtin_amdgcn_sched_barrier(0); } while (0)

#define TS(i)
#define TS_DECL
#define TS_SET(i, v) do {} while (0)
#define TS_FLUSH()

__device__ __forceinline__ f32x4 mfma32(u32x4 a, u32x4 b, f32x4 c) {
  return __builtin_amdgcn_mfma_f32_16x16x32_bf16(__builtin_bit_cast(bf16x8, a), __builtin_bit_cast(bf16x8, b), c, 0, 0, 0);
}
__device__ __forceinline__ f32x4 mfma16(u32x2 a, u32x2 b, f32x4 c) {
  return __builtin_amdgcn_mfma_f32_16x16x16bf16_1k(__builtin_bit_cast(s16x4, a), __builtin_bit_cast(s16x4, b), c, 0, 0, 0);
}
__device__ __forceinline__ float asf(u32 x) { return __builtin_bit_cast(float, x); }
__device__ __forceinline__ u32 asu(float x) { return __builtin_bit_cast(u32, x); }
__device__ __forceinline__ float bf_lo(u32 x) { return asf(x << 16); }
__device__ __forceinline__ float bf_hi(u32 x) { return asf(x & 0xffff0000u); }
__device__ __forceinline__ u32 pack_bf(float lo, float hi) {
  typedef __bf16 bf16x2 __attribute__((ext_vector_type(2)));
  typedef float f32x2 __attribute__((ext_vector_type(2)));
  f32x2 v = {lo, hi};
  return __builtin_bit_cast(u32, __builtin_convertvector(v, bf16x2));
}
__device__ __forceinline__ float rcp(float x) { return __builtin_amdgcn_rcpf(x); }
constexpr float L2E = 1.4426950408889634f;
constexpr int OOB = 0x7ffffff0;
__device__ __forceinline__ float sigm(float x) { return rcp(1.f + __builtin_amdgcn_exp2f(-L2E * x)); }
__device__ __forceinline__ float rbf(float x) { return bf_lo(pack_bf(x, x)); }
__device__ __forceinline__ u32 perm_lo(u32 a, u32 b) { return __builtin_amdgcn_perm(b, a, 0x05040100u); }
__device__ __forceinline__ u32 perm_hi(u32 a, u32 b) { return __builtin_amdgcn_perm(b, a, 0x07060302u); }
template <int CTRL> __device__ __forceinline__ float dpp(float x) {
  return asf(__builtin_amdgcn_mov_dpp(asu(x), CTRL, 0xf, 0xf, true));
}
template <int BANK> __device__ __forceinline__ float ror8_merge(float old, float src) {
  return asf(__builtin_amdgcn_update_dpp(asu(old), asu(src), 0x128, 0xf, BANK, false));
}
constexpr int DPP_X1 = 0xB1, DPP_X2 = 0x4E, DPP_HMIR = 0x141, DPP_MIR = 0x140, DPP_ROR8 = 0x128, DPP_ROR4 = 0x124;
__device__ __forceinline__ void swap32(float& a, float& b) {
  auto r = __builtin_amdgcn_permlane32_swap(asu(a), asu(b), false, false);
  a = asf(r[0]); b = asf(r[1]);
}
__device__ __forceinline__ void swap16(float& a, float& b) {
  auto r = __builtin_amdgcn_permlane16_swap(asu(a), asu(b), false, false);
  a = asf(r[0]); b = asf(r[1]);
}
__device__ __forceinline__ float add32(float x) { float a = x, b = x; swap32(a, b); return a + b; }
__device__ __forceinline__ float add16(float x) { float a = x, b = x; swap16(a, b); return a + b; }
__device__ __forceinline__ float max32(float x) { float a = x, b = x; swap32(a, b); return fmaxf(a, b); }
__device__ __forceinline__ float max16(float x) { float a = x, b = x; swap16(a, b); return fmaxf(a, b); }
__device__ __forceinline__ float wave_sum(float x) {
  x += dpp<DPP_X1>(x); x += dpp<DPP_X2>(x); x += dpp<DPP_HMIR>(x); x += dpp<DPP_MIR>(x);
  return add32(add16(x));
}
__device__ __forceinline__ float wave_max(float f) {
  f = fmaxf(f, dpp<DPP_X1>(f)); f = fmaxf(f, dpp<DPP_X2>(f)); f = fmaxf(f, dpp<DPP_HMIR>(f)); f = fmaxf(f, dpp<DPP_MIR>(f));
  return max32(max16(f));
}
__device__ __forceinline__ u32x4 ldnt(rsrc_t r, int voff, int soff) { return __builtin_amdgcn_raw_buffer_load_b128(r, voff, soff, AUX_NT); }
__device__ __forceinline__ u32x4 ldsc(rsrc_t r, int voff, int soff) { return __builtin_amdgcn_raw_buffer_load_b128(r, voff, soff, AUX_SC1); }
__device__ __forceinline__ rsrc_t mkr(const void* p, int bytes) { return __builtin_amdgcn_make_buffer_rsrc((void*)p, 0, bytes, 0x00020000); }

struct Tags { u32 t[4]; };
__device__ __forceinline__ int win(int o) { return o + (o >= 384 ? 128 : 0); }
__device__ __forceinline__ void publish(rsrc_t r, int voff, f32x4 v, const Tags& tg) {
  u32x4 o;
#pragma unroll
  for (int i = 0; i < 4; ++i) o[i] = ((asu(v[i]) + 0x800u) & 0xfffff000u) | tg.t[i];
  __builtin_amdgcn_raw_buffer_store_b128(o, r, voff, 0, ST_AUX);
}
__device__ __forceinline__ void strip(u32x4& m, const Tags& tg) {
#pragma unroll
  for (int i = 0; i < 4; ++i) m[i] ^= tg.t[i];
}
__device__ __forceinline__ u32 or4(const u32x4& m) { return (m[0] | m[1]) | (m[2] | m[3]); }

struct Params {
  const u16* x; const u16* kc; const u16* vc; const u16* wq; const u16* wo;
  const u16* wg; const u16* wu; const u16* wd;
  u16* out; char* ws;
};
constexpr int P_STEP16 = 58, P_LEAD = 4, PO = 360, SO = 4, PG3 = 352, PD = 396, SD = 7, P4 = 408, PF = 480, MODE = 7, PT = 0, TK0 = 310, SK16 = 58, PQ = 0, PS = 320, TW0 = 100, SW16 = 58, PV = 228, X18 = 22, X19 = 48, X20 = 0, ABL = 0;
#define PACE(pos) PACET((((pos) - P_LEAD) * P_STEP16) >> 4)
#define PACET(t) do { PIN(); const int due_ = (t); while ((int)((u32)__builtin_amdgcn_s_memrealtime() - tz) < due_) __builtin_amdgcn_s_sleep(1); PIN(); } while (0)

template <int STR> __device__ __forceinline__ void store64(f32x4 (&d)[8], float* red, int g, int c) {
  f32x4 s[4];
#pragma unroll
  for (int i = 0; i < 4; ++i)
#pragma unroll
    for (int r = 0; r < 4; ++r) {
      float a = d[i][r], b = d[i + 4][r];
      swap32(a, b);
      s[r][i] = a + dpp<DPP_ROR8>(b);
    }
  if (c < 8) {
#pragma unroll
    for (int r = 0; r < 4; ++r) *(f32x4*)(red + (4 * (g & 1) + r) * STR + 8 * c + 4 * (g >> 1)) = s[r];
  }
}
__device__ __forceinline__ void store_acc(f32x4 (&d)[8], char* red, int g, int c) {
  if (g < 2) {
#pragma unroll
    for (int i = 0; i < 8; ++i) *(f32x4*)(red + g * R_GS + i * R_IS + c * 16) = d[i];
  }
}
__device__ __forceinline__ void tr_pair(const u32x4* w, u32x4 (&f)[8], int jj) {
#pragma unroll
  for (int i = 0; i < 8; ++i) {
    const u32 lo = w[2 * jj][i >> 1], hi = w[2 * jj + 1][i >> 1];
    f[i][jj] = (i & 1) ? perm_hi(lo, hi) : perm_lo(lo, hi);
  }
}
__device__ __forceinline__ void merge_stats(float m, float l, float& M, float& L) {
  M = fmaxf(m, dpp<DPP_ROR8>(m));
  M = max32(max16(M));
  L = l * __builtin_amdgcn_exp2f((m - M) * L2E);
  L += dpp<DPP_ROR8>(L);
  L = add32(add16(L));
}

#define ARRIVE(i) do { PIN(); asm volatile("s_mov_b64 exec, 1\n\tds_add_u32 %0, %1\n\ts_mov_b64 exec, -1" :: "v"(lcnt + 4 * (i)), "v"(1u) : "memory"); PIN(); } while (0)
#define PINV(a, n) do { _Pragma("unroll") for (int i_ = 0; i_ < (n); ++i_) asm volatile("" : "+v"((a)[i_])); } while (0)
#define LWAITS(i, n, SL) do { PIN(); u32 it_ = 0; \
    while (__builtin_amdgcn_readfirstlane(__hip_atomic_load((u32*)(lds + L_CNT) + (i), __ATOMIC_RELAXED, __HIP_MEMORY_SCOPE_WORKGROUP)) < (u32)(n) && ++it_ < (1u << 22)) { SL; } \
    PIN(); } while (0)
#define LWAIT(i, n) LWAITS(i, n, __builtin_amdgcn_s_sleep(1))
#define LWAITF(i, n) LWAITS(i, n, )
#define GETTZ() GETT(L_TZ1)
#define GETT(i) do { PIN(); tz = __builtin_amdgcn_readfirstlane(__hip_atomic_load((u32*)(lds + L_CNT) + (i), __ATOMIC_RELAXED, __HIP_MEMORY_SCOPE_WORKGROUP)); PIN(); } while (0)
#define PRIO(n) do { __builtin_amdgcn_s_setprio(n); PIN(); } while (0)
#define MKTAGS() Tags tg; const u32 e_ = __builtin_amdgcn_readfirstlane(__hip_atomic_load((u32*)(lds + L_CNT) + L_EP, __ATOMIC_RELAXED, __HIP_MEMORY_SCOPE_WORKGROUP)) + 1u; { _Pragma("unroll") for (int i_ = 0; i_ < 4; ++i_) tg.t[i_] = (e_ + 0x4d5u * i_) & 0xfffu; }
#define RETRY(m, n, addr) do { u32 bad_ = 0; \
    _Pragma("unroll") for (int k = 0; k < (n); ++k) { strip(m[k], tg); bad_ |= or4(m[k]); } \
    if (__builtin_expect(__builtin_amdgcn_ballot_w64((bad_ & 0xfffu) != 0) != 0 && !ABL, 0)) { u32 spin_ = 0; do { asm volatile("; retry" ::: "memory"); \
    bool st_[n]; \
    _Pragma("unroll") for (int k = 0; k < (n); ++k) st_[k] = (or4(m[k]) & 0xfffu) != 0; \
    _Pragma("unroll") for (int k = 0; k < (n); ++k) if (st_[k]) m[k] = ldsc(rws, (addr), 0); \
    PIN(); bad_ = 0; \
    _Pragma("unroll") for (int k = 0; k < (n); ++k) { if (st_[k]) strip(m[k], tg); bad_ |= or4(m[k]); } \
    } while (__builtin_amdgcn_ballot_w64((bad_ & 0xfffu) != 0) != 0 && ++spin_ < SPIN_CAP); TS_SET(21, tsv[21] + spin_ + 1); } } while (0)

__global__ void __launch_bounds__(NT, 4) fused_kernel(Params p) {
  __shared__ __attribute__((aligned(16))) char lds[L_END];
  const int tid = threadIdx.x, lane = tid & 63, wave = __builtin_amdgcn_readfirstlane(tid >> 6);
  const int g = lane >> 4, c = lane & 15, hi = lane >> 5;
  const int wg = blockIdx.x, grp = (wg & 7) * 4 + (wg >> 6), sub = (wg >> 3) & 7;
  const bool ta = wg < 198, th = ta && wg >= 154;
  int cgv[3];
#pragma unroll
  for (int i = 0; i < 3; ++i) {
    const int f = ta ? wg : wg - 198 + 58 * i, fq = f / 7, fr = f - 7 * fq;
    const int n = th ? ((wg - 154) >> 1) * 8 + 3 : fq * 8 + fr + (fr >= 3);
    const int sh = (int)((uintptr_t)(n < 64 ? p.wq : n < 120 ? p.wg : p.wu) >> 7);
    cgv[i] = (th ? i < 2 : f < 154) ? (n & ~7) | ((n - sh) & 7) : -1;
  }
  const int rbase = th ? (wg & 1) * 512 : ta ? 0 : 768;
  const int gw = grp, sO = (sub + 1) & 7, sD = (sub + 3) & 7;
  const int w = wave & 7, wv = wave & 3;
  const rsrc_t rws = mkr(p.ws, WS_END);
  const u32 lcnt = (u32)(uintptr_t)(__attribute__((address_space(3))) char*)lds + L_CNT;
  TS_DECL;
  TS(0);
  u32 tz = (u32)__builtin_amdgcn_s_memrealtime();
  const size_t kvoff = ((size_t)grp * SK + sub * 512 + w * 64) * DH;
  const int rot = (MODE & 2) ? (w + sub) & 3 : 0;
  int kb[4];
#pragma unroll
  for (int i = 0; i < 4; ++i) kb[i] = ((i + rot) & 3) * 4096;
  if (wave < 8) {
    PRIO(3);
    const u32 ep = __builtin_amdgcn_raw_buffer_load_b32(rws, w == 0 ? 0 : OOB, 0, AUX_SC1);
    const u32 xp = *(const u32*)(p.x + w * DM + lane * 16);
    PIN();
    u32x4 wqr[4], gr[8], vr[16];
    const int r0 = rbase + w * 32, r1 = ta ? (rbase + 256 + w * 64) & 1023 : 768 + wv * 64;
    {
      const int cg = cgv[0], st = cg < 64 ? HQD * 2 : FF * 2;
      const rsrc_t r = mkr((const char*)(cg < 64 ? p.wq : cg < 120 ? p.wg : p.wu) + r0 * st + (cg - (cg < 64 ? 0 : cg < 120 ? 64 : 120)) * 128, 31 * st + 128);
      const int vo = (16 * (c >> 3) + 4 * g) * st + (c & 7) * 16;
#pragma unroll
      for (int j = 0; j < 4; ++j) wqr[j] = ldnt(r, vo, j * st);
    }
    if (tid < 32) ((u32*)(lds + L_CNT))[tid] = tid >= L_TZ0 ? tz : 0;
    BAR();
    GETTZ();
    {
      const int cg = w < 4 ? cgv[1] : cgv[2], st = cg < 64 ? HQD * 2 : FF * 2;
      const rsrc_t r = mkr((const char*)(cg < 64 ? p.wq : cg < 120 ? p.wg : p.wu) + r1 * st + (cg - (cg < 64 ? 0 : cg < 120 ? 64 : 120)) * 128, cg < 0 ? 0 : 63 * st + 128);
      const int vo = (32 * (c >> 3) + 8 * g) * st + (c & 7) * 16;
#pragma unroll
      for (int j = 0; j < 8; ++j) { PACET(TW0 + (((j * 7 + w) * SW16) >> 4)); gr[j] = ldnt(r, vo, j * st); }
    }
    PIN();
    TS(1);
    USE4(xp);
    if (w == 0) ((u32*)(lds + L_CNT))[L_EP] = ep;
    u32x2 fq[8];
#pragma unroll
    for (int i = 0; i < 8; ++i)
#pragma unroll
      for (int jj = 0; jj < 2; ++jj) {
        const u32 lo = wqr[2 * jj][i >> 1], hh = wqr[2 * jj + 1][i >> 1];
        fq[i][jj] = (i & 1) ? perm_hi(lo, hh) : perm_lo(lo, hh);
      }
    PINV(fq, 8);
    LWAIT(C_XN, 8);
    TS(2);
    {
      const u32x2 a0 = *(const u32x2*)(lds + L_XN + ((c & 7) * XN_STR + r0 + 16 * (c >> 3) + 4 * g) * 2);
      const u32x4 a1 = *(const u32x4*)(lds + L_XN + ((c & 7) * XN_STR + r1 + 32 * (c >> 3) + 8 * g) * 2);
      f32x4 d[8];
#pragma unroll
      for (int i = 0; i < 8; ++i) d[i] = mfma16(a0, fq[i], f32x4{0, 0, 0, 0});
      if (!ta) {
        store64<RG_STR>(d, (float*)(lds + L_RG) + (8 + w) * 8 * RG_STR, g, c);
#pragma unroll
        for (int i = 0; i < 8; ++i) d[i] = f32x4{0, 0, 0, 0};
      }
      u32x4 f[8];
#pragma unroll
      for (int jj = 0; jj < 4; ++jj) tr_pair(gr, f, jj);
#pragma unroll
      for (int i = 0; i < 8; ++i) d[i] = mfma32(a1, f[i], d[i]);
      store64<RG_STR>(d, (float*)(lds + L_RG) + w * 8 * RG_STR, g, c);
    }
    ARRIVE(C_RG);
    TS(3);
    LWAIT(C_RG, 8);
    MKTAGS();
    if (w == 7) {
      const u32 t20 = tz << 12;
      __builtin_amdgcn_raw_buffer_store_b128(u32x4{t20 | tg.t[0], t20 | tg.t[1], t20 | tg.t[2], t20 | tg.t[3]}, rws,
                                             (lane == 0 && !(wg & 24) && !(MODE & 16)) ? WS_T + ((wg >> 5) * 8 + (wg & 7)) * 16 : OOB, 0, AUX_SC1);
    }
    if (w < (ta ? 2 : 6)) {
      const int t = w >> 1, cg = t == 0 ? cgv[0] : t == 1 ? cgv[1] : cgv[2], l = (w & 1) * 64 + lane;
      const bool isq = cg < 64;
      const int b = isq ? l >> 4 : l & 7, cq = isq ? l & 15 : (l >> 5) * 4 + ((l >> 3) & 1) * 2 + ((l >> 4) & 1);
      const float* rg = (const float*)(lds + L_RG) + ((t == 0 ? (ta ? 0 : 8) : 4 * t - 4) * 8 + b) * RG_STR + cq * 4;
      f32x4 s = *(const f32x4*)rg;
#pragma unroll
      for (int k = 1; k < 4; ++k) s += *(const f32x4*)(rg + k * 8 * RG_STR);
      if (t == 0) {
#pragma unroll
        for (int k = 4; k < 8; ++k) s += *(const f32x4*)(rg + k * 8 * RG_STR);
      }
      const int slot = rbase != 0, gu = cg >= 120;
      const int aq = WS_Q + (b * 32 + (cg >> 1)) * 2048 + win(slot * 512 + (cg & 1) * 256 + cq * 16);
      const int ag = WS_GU + ((cg - (gu ? 120 : 64)) * 4 + (l >> 5)) * 4096 + slot * 2048 + win(((l >> 3) & 3) * 256 + gu * 128 + b * 16);
      publish(rws, (cg < 0 || (ABL & 1)) ? OOB : isq ? aq : ag, s, tg);
    }
    TS(4);
    GETTZ();
    PRIO(0);
    const rsrc_t rv = mkr(p.vc + kvoff, 64 * DH * 2);
#pragma unroll
    for (int n = 0; n < 16; ++n) { PACE(PV + n * 8 + w); vr[n] = ldnt(rv, lane * 16 + (n & 3) * 1024, kb[n >> 2]); }
    ARRIVE(C_VI);
    TS(6);
    PRIO(3);
    u32x4 vt[8];
#pragma unroll
    for (int jj = 0; jj < 4; ++jj) tr_pair(vr, vt, jj);
    PINV(vt, 8);
    LWAIT(C_P + w, 1);
    TS(7);
    f32x4 acc[8];
    {
      const u32x4 pf = *(const u32x4*)(lds + L_P + w * 1024 + lane * 16);
#pragma unroll
      for (int i = 0; i < 8; ++i) acc[i] = mfma32(pf, vt[i], f32x4{0, 0, 0, 0});
    }
    PIN();
    TS(8);
    const u32x4 pf1 = *(const u32x4*)(lds + L_P + w * 1024 + (lane ^ 8) * 16);
#pragma unroll
    for (int jj = 0; jj < 4; ++jj) { tr_pair(vr + 8, vt, jj); PIN(); }
#pragma unroll
    for (int i = 0; i < 8; ++i) acc[i] = mfma32(pf1, vt[i], acc[i]);
    store_acc(acc, lds + L_RO + w * R_WS, g, c);
    ARRIVE(C_RO);
    TS(9);
    LWAIT(C_F, 1);
    LWAIT(C_RO, 8);
    TS(10);
    {
      const int g2 = w >> 2, i = lane & 7, cl = (lane >> 3) & 3;
      const char* ro = lds + L_RO + hi * 4 * R_WS + g2 * R_GS + i * R_IS + (wv * 4 + cl) * 16;
      const char* fp = lds + L_F + hi * 128 + g2 * 16;
      f32x4 o = *(const f32x4*)ro * *(const f32x4*)fp;
#pragma unroll
      for (int k = 1; k < 4; ++k) o += *(const f32x4*)(ro + k * R_WS) * *(const f32x4*)(fp + k * 32);
#pragma unroll
      for (int r = 0; r < 4; ++r) o[r] = add32(o[r]);
      publish(rws, (hi || (ABL & 16)) ? OOB : WS_O + (((grp & 3) * 2 + g2) * 4 + wv) * 65536 + (grp >> 2) * 8192 + (sub >> 1) * 2048 + win((sub & 1) * 512 + lane * 16), o, tg);
    }
    ARRIVE(C_M4);
    TS(11);
    u32x4 sm[1];
    const bool sv = ((lane >> 2) & 1) == ((gw >> 2) & 1);
    const int stoff = sv ? WS_ST + (w * 4 + (gw >> 3)) * 2048 + win(lane * 16) : OOB;
    sm[0] = ldsc(rws, stoff, 0);
    LWAIT(C_M4, 8);
    GETT((MODE & 32) ? L_TZ1 : L_TZ0);
    PACE(P4);
    u32x4 om[4], bw[4];
    const int ooff = (ABL & 32) ? OOB - 16384 : WS_O + gw * 65536 + w * 8192 + win(lane * 16);
#pragma unroll
    for (int k = 0; k < 4; ++k) om[k] = ldsc(rws, ooff, k * 2048);
    TS(12);
    if (!sv) sm[0] = u32x4{tg.t[0], tg.t[1], tg.t[2], tg.t[3]};
    RETRY(sm, 1, stoff);
    f32x4 fs[4];
    {
      float M, L;
      merge_stats(asf(sm[0][0]), asf(sm[0][1]) + asf(sm[0][2]), M, L);
      *(float*)(lds + L_F2 + w * 256 + lane * 4) = __builtin_amdgcn_exp2f((asf(sm[0][0]) - M) * L2E) * rcp(L);
      PIN();
#pragma unroll
      for (int k = 0; k < 4; ++k) fs[k] = *(const f32x4*)(lds + L_F2 + w * 256 + (2 * k + hi) * 32 + ((gw >> 2) & 1) * 16);
    }
    LWAIT(C_OW, 4);
#pragma unroll
    for (int k = 0; k < 4; ++k) bw[k] = *(const u32x4*)(lds + L_OW + k * 8192 + w * 1024 + lane * 16);
    PINV(bw, 4);
    PINV(fs, 4);
    TS(13);
    RETRY(om, 4, ooff + k * 2048);
    TS(14);
    {
      float v[4];
#pragma unroll
      for (int i = 0; i < 4; ++i) v[i] = (asf(om[0][i]) * fs[0][i] + asf(om[1][i]) * fs[1][i]) + (asf(om[2][i]) * fs[2][i] + asf(om[3][i]) * fs[3][i]);
      swap32(v[0], v[2]); swap32(v[1], v[3]);
      *(u32*)(lds + L_A + (w * A_STR + (lane & 31) * 4 + 2 * hi) * 2) = pack_bf(v[0] + v[2], v[1] + v[3]);
    }
    ARRIVE(C_A);
    LWAITF(C_A, 8);
    TS(15);
    {
      u32x4 a[4];
#pragma unroll
      for (int k = 0; k < 4; ++k) a[k] = *(const u32x4*)(lds + L_A + ((c & 7) * A_STR + 32 * k + 8 * g) * 2);
      f32x4 d0 = mfma32(a[0], bw[0], f32x4{0, 0, 0, 0}), d1 = mfma32(a[1], bw[1], f32x4{0, 0, 0, 0});
      d0 = mfma32(a[2], bw[2], d0); d1 = mfma32(a[3], bw[3], d1);
      publish(rws, hi ? OOB : WS_FS + (((2 * sO + (c >> 3)) * 2 + g) * 8 + w) * 4096 + gw * 128 + (c & 7) * 16, d0 + d1, tg);
    }
    ARRIVE(C_S5);
    TS(16);
  } else {
    PRIO(3);
    const u32x4 xa = *(const u32x4*)(p.x + w * DM + lane * 8);
    const u32x4 xb = *(const u32x4*)(p.x + w * DM + 512 + lane * 8);
    const int oidx = (4 * ((wg >> 3) & 1) + g) * DM + (wg >> 4) * 64 + (c & 7) * 8 + (wg & 7);
    const u32 xfin = p.x[oidx];
    asm volatile("s_barrier" ::: "memory");
    GETTZ();
    {
      float v[16];
#pragma unroll
      for (int i = 0; i < 4; ++i) { v[2 * i] = bf_lo(xa[i]); v[2 * i + 1] = bf_hi(xa[i]); v[8 + 2 * i] = bf_lo(xb[i]); v[9 + 2 * i] = bf_hi(xb[i]); }
      float s = 0.f;
#pragma unroll
      for (int i = 0; i < 16; ++i) s += v[i];
      const float mean = wave_sum(s) * (1.f / DM);
      float q = 0.f;
#pragma unroll
      for (int i = 0; i < 16; ++i) { const float d = v[i] - mean; q += d * d; }
      const float var = rbf(wave_sum(q) * (1.f / DM));
      const float mb = rbf(mean);
      const float rs = rcp(rbf(__builtin_amdgcn_sqrtf(rbf(var + 1e-5f))));
      u32x4 o[2];
#pragma unroll
      for (int i = 0; i < 8; ++i) {
        const u32 d = pack_bf(v[2 * i] - mb, v[2 * i + 1] - mb);
        o[i >> 2][i & 3] = pack_bf(bf_lo(d) * rs, bf_hi(d) * rs);
      }
      *(u32x4*)(lds + L_XN + (w * XN_STR + lane * 8) * 2) = o[0];
      *(u32x4*)(lds + L_XN + (w * XN_STR + 512 + lane * 8) * 2) = o[1];
    }
    ARRIVE(C_XN);
    TS(17);
    PRIO(2);
    const rsrc_t rk = mkr(p.kc + kvoff, 64 * DH * 2);
    const int kvo = (4 * (c & 3) + ((c >> 2) & 1)) * 256 + (4 * (c >> 3) + g) * 16;
    u32x4 kr[16];
#pragma unroll
    for (int n = 0; n < 16; ++n) { if (!(n & 1)) PACET(TK0 + (((n * 8 + w * 2) * SK16) >> 4) + (n * 8 + w * 2 >= X19 ? X18 : 0)); kr[n] = ldnt(rk, kvo + ((n >> 1) & 1) * 512 + (n & 1) * 128, kb[n >> 2]); }
    PIN();
    TS(1);
    PRIO(3);
    PACE(PQ + w);
    u32x4 qm[1];
    const int qoff = (ABL & 2) ? OOB : WS_Q + ((grp >> 2) * 32 + (grp & 3) * 8 + w) * 2048 + win(lane * 16);
    qm[0] = ldsc(rws, qoff, 0);
    u32x4 tm = ldsc(rws, (w == 7 && !(MODE & 16)) ? WS_T + lane * 16 : OOB, 0);
    PIN();
    LWAIT(C_RG, 8);
    MKTAGS();
    RETRY(qm, 1, qoff);
    TS(2);
    {
      float q0 = asf(qm[0][0]), q1 = asf(qm[0][1]), q2 = asf(qm[0][2]), q3 = asf(qm[0][3]);
      swap32(q0, q2); swap32(q1, q3);
      *(u32*)(lds + L_QS + (w * A_STR + (lane & 31) * 4 + 2 * hi) * 2) = pack_bf(q0 + q2, q1 + q3);
    }
    if (w == 7) {
      strip(tm, tg);
      int d = (int)(tm[0] - (tz << 12)) >> 12;
      if ((or4(tm) & 0xfffu) != 0 || d < -150 || d > 150 || !(MODE & 4)) d = 0;
      ((u32*)(lds + L_CNT))[L_TZ0] = tz + (int)wave_max((float)d);
    }
    ARRIVE(C_QS);
    LWAIT(C_QS, 8);
    TS(3);
    {
      const u32x4 q0 = *(const u32x4*)(lds + L_QS + ((c & 7) * A_STR + 32 * (c >> 3) + 8 * g) * 2);
      const u32x4 q1 = *(const u32x4*)(lds + L_QS + ((c & 7) * A_STR + 64 + 32 * (c >> 3) + 8 * g) * 2);
      float sc[8];
#pragma unroll
      for (int e = 0; e < 2; ++e) {
        f32x4 t[4];
#pragma unroll
        for (int ua = 0; ua < 4; ++ua) t[ua] = mfma32(kr[(ua >> 1) * 8 + e * 4 + (ua & 1) * 2], q0, f32x4{0, 0, 0, 0});
#pragma unroll
        for (int ua = 0; ua < 4; ++ua) t[ua] = mfma32(kr[(ua >> 1) * 8 + e * 4 + (ua & 1) * 2 + 1], q1, t[ua]);
#pragma unroll
        for (int r = 0; r < 4; ++r) {
          float a = t[0][r], b = t[1][r], a2 = t[2][r], b2 = t[3][r];
          swap32(a, b); swap32(a2, b2);
          sc[e * 4 + r] = ror8_merge<0x3>(b2, b) + ror8_merge<0xc>(a, a2);
        }
      }
      float mx = -INFINITY;
#pragma unroll
      for (int i = 0; i < 4; ++i) {
        const u32 s1 = pack_bf(sc[2 * i], sc[2 * i + 1]);
        const u32 s2 = pack_bf(bf_lo(s1) * 0.08838834764831845f, bf_hi(s1) * 0.08838834764831845f);
        sc[2 * i] = bf_lo(s2); sc[2 * i + 1] = bf_hi(s2);
        mx = fmaxf(mx, fmaxf(sc[2 * i], sc[2 * i + 1]));
      }
      mx = fmaxf(mx, dpp<DPP_ROR8>(mx));
      const float mw = max32(max16(mx));
      float l = 0.f;
      u32x4 pf;
#pragma unroll
      for (int i = 0; i < 4; ++i) {
        const float e0 = __builtin_amdgcn_exp2f((sc[2 * i] - mw) * L2E), e1 = __builtin_amdgcn_exp2f((sc[2 * i + 1] - mw) * L2E);
        l += e0 + e1;
        pf[i] = pack_bf(e0, e1);
      }
      *(u32x4*)(lds + L_P + w * 1024 + lane * 16) = pf;
      ARRIVE(C_P + w);
      l += dpp<DPP_ROR8>(l);
      l = add32(add16(l));
      if (lane < 8) *(float2*)(lds + L_ST + (w * 8 + lane) * 8) = make_float2(mw, l);
    }
    ARRIVE(C_ST);
    TS(4);
    u32x4 wr[8], wt[8];
    if (w >= 4) {
      if (MODE & 8) LWAIT(C_VI, 8);
      PACE(PG3 + wv);
      TS(7);
      u32x4 gm[4];
      const int gbase = (ABL & 8) ? OOB - 16384 : WS_GU + (gw * 7 + 2 * wv + (wv == 3 ? 0 : hi)) * 4096;
      const int goff0 = gbase + win((g & 1) * 256 + c * 16), goff1 = gbase + 640 + (g & 1) * 256 + c * 16;
#pragma unroll
      for (int k = 0; k < 4; ++k) gm[k] = ldsc(rws, (k & 1) ? goff1 : goff0, (k >> 1) * 2048);
      RETRY(gm, 4, ((k & 1) ? goff1 : goff0) + (k >> 1) * 2048);
      TS(8);
      u32x4 a;
#pragma unroll
      for (int i = 0; i < 4; ++i) {
        const int qp = i >> 1, e = (i & 1) * 2;
        const float v0 = asf(gm[qp][e]) + asf(gm[2 + qp][e]), v1 = asf(gm[qp][e + 1]) + asf(gm[2 + qp][e + 1]);
        const u32 gg = pack_bf(v0, v1);
        const u32 uu = asu(dpp<DPP_ROR8>(asf(gg)));
        const u32 sg = pack_bf(sigm(bf_lo(gg)), sigm(bf_hi(gg)));
        a[i] = pack_bf(bf_lo(sg) * bf_lo(uu), bf_hi(sg) * bf_hi(uu));
        if (wv == 3 && hi) a[i] = 0;
      }
      PINV(a, 4);
      TS(9);
      PRIO(0);
      const rsrc_t rw = mkr(p.wd + (size_t)(gw * 112 + 32 * wv) * DM + sD * 128, (wv == 3 ? 16 : 32) * DM * 2 - sD * 256);
#pragma unroll
      for (int j = 0; j < 8; ++j) { PACE(PD + j * SD / 2 + wv); wr[j] = ldnt(rw, 8 * g * (DM * 2) + c * 16, j * DM * 2); }
      PRIO(3);
      TS(10);
#pragma unroll
      for (int jj = 0; jj < 4; ++jj) { tr_pair(wr, wt, jj); PIN(); }
      f32x4 d[8];
#pragma unroll
      for (int i = 0; i < 8; ++i) d[i] = mfma32(a, wt[i], f32x4{0, 0, 0, 0});
      store_acc(d, lds + L_RY + wv * R_WS, g, c);
      ARRIVE(C_Y);
      TS(11);
      LWAITF(C_Y, 4);
      {
        const int t = wv * 64 + lane;
        const char* rf = lds + L_RY + (t >> 7) * R_GS + ((t >> 3) & 7) * R_IS + (((t >> 6) & 1) * 8 + (t & 7)) * 16;
        f32x4 s = *(const f32x4*)rf;
#pragma unroll
        for (int k = 1; k < 4; ++k) s += *(const f32x4*)(rf + k * R_WS);
        publish(rws, WS_FY + (((2 * sD + ((t >> 6) & 1)) * 2 + (t >> 7)) * 8 + ((t >> 3) & 7)) * 4096 + gw * 128 + (t & 7) * 16, s, tg);
      }
      ARRIVE(C_S5);
      TS(12);
    } else {
      if (w == 0) {
        LWAIT(C_ST, 8);
        const float2 ml = *(const float2*)(lds + L_ST + lane * 8);
        float M, L;
        merge_stats(ml.x, ml.y, M, L);
        const float Lh = asf((asu(L) + 0x800u) & 0xfffff000u);
        publish(rws, lane < 8 ? WS_ST + grp * 2048 + win(sub * 128) + lane * 16 : OOB, f32x4{M, Lh, L - Lh, 0.f}, tg);
        *(float*)(lds + L_F + lane * 4) = __builtin_amdgcn_exp2f((ml.x - M) * L2E);
        ARRIVE(C_F);
        TS(6);
      }
      LWAIT(C_VI, 8);
      GETTZ();
      TS(7);
      PRIO(0);
      const rsrc_t rw = mkr(p.wo + (size_t)((gw >> 2) * 512 + (gw & 3) * 32 + 8 * wv) * DM + sO * 128, 400 * DM * 2);
#pragma unroll
      for (int j = 0; j < 8; ++j) { PACE(PO + j * SO + wv); wr[j] = ldnt(rw, 2 * g * (DM * 2) + c * 16, ((j & 3) * 128 + (j >> 2)) * DM * 2); }
      PIN();
      PRIO(3);
      TS(8);
#pragma unroll
      for (int jj = 0; jj < 4; ++jj) { tr_pair(wr, wt, jj); PIN(); }
#pragma unroll
      for (int i = 0; i < 8; ++i) *(u32x4*)(lds + L_OW + wv * 8192 + i * 1024 + lane * 16) = wt[i];
      ARRIVE(C_OW);
      TS(10);
      if (w == 0) {
      GETT(L_TZ0);
      LWAIT(C_S5, 12);
      PACE(PF);
      TS(12);
      u32x4 fm[8];
      const int foff = WS_FS + wg * 4096 + lane * 16;
#pragma unroll
      for (int k = 0; k < 8; ++k) fm[k] = ldsc(rws, foff, (k >> 2) * (WS_FY - WS_FS) + (k & 3) * 1024);
      RETRY(fm, 8, foff + (k >> 2) * (WS_FY - WS_FS) + (k & 3) * 1024);
      TS(14);
      float z[2];
#pragma unroll
      for (int h = 0; h < 2; ++h) {
        float v[4];
#pragma unroll
        for (int i = 0; i < 4; ++i) v[i] = (asf(fm[4 * h][i]) + asf(fm[4 * h + 1][i])) + (asf(fm[4 * h + 2][i]) + asf(fm[4 * h + 3][i]));
        swap32(v[0], v[2]); swap32(v[1], v[3]);
        v[0] += v[2]; v[1] += v[3];
        swap16(v[0], v[1]);
        const float t = v[0] + v[1];
        z[h] = rbf(t + dpp<DPP_ROR8>(t));
      }
      z[1] = rbf(z[1] * sigm(z[1]));
      const float r = bf_lo(xfin) + rbf(z[1] * z[0]);
      if (c < 8) p.out[oidx] = (u16)pack_bf(r, r);
      if (wg == 0 && lane == 0) __builtin_amdgcn_raw_buffer_store_b32(e_, rws, 0, 0, AUX_SC1);
      TS(15);
    }
    }
  }
  TS_FLUSH();
}

extern "C" void kernel_launch(void* const* d_in, const int* in_sizes, int n_in, void* d_out, int out_size, void* d_ws, size_t ws_size,
                              hipStream_t stream) {
  Params p;
  p.x = (const u16*)d_in[0]; p.kc = (const u16*)d_in[1]; p.vc = (const u16*)d_in[2]; p.wq = (const u16*)d_in[3]; p.wo = (const u16*)d_in[4];
  p.wg = (const u16*)d_in[6]; p.wu = (const u16*)d_in[7]; p.wd = (const u16*)d_in[8];
  p.out = (u16*)d_out; p.ws = (char*)d_ws;
  fused_kernel<<<NWG, NT, 0, stream>>>(p);
}
```

```cpp
#include <hip/hip_runtime.h>
#include <hip/hip_bf16.h>
#include <stdint.h>

typedef unsigned int u32;
typedef unsigned short u16;
typedef u32 u32x4 __attribute__((ext_vector_type(4)));
typedef u32 u32x2 __attribute__((ext_vector_type(2)));
typedef float f32x4 __attribute__((ext_vector_type(4)));
typedef __bf16 bf16x8 __attribute__((ext_vector_type(8)));
typedef short s16x4 __attribute__((ext_vector_type(4)));
typedef __amdgpu_buffer_rsrc_t rsrc_t;

constexpr int DM = 1024, HQD = 4096, SK = 4096, DH = 128, FF = 3584;
constexpr int NWG = 256, NT = 1024;
#define TS_WAVES 16

constexpr int WS_T = 1024;
constexpr int WS_Q = 4096;
constexpr int WS_GU = WS_Q + 256 * 2048;
constexpr int WS_O = WS_GU + 224 * 4096;
constexpr int WS_ST = WS_O + 32 * 65536;
constexpr int WS_FS = WS_ST + 32 * 2048;
constexpr int WS_FY = WS_FS + 256 * 4096;
constexpr int WS_END = WS_FY + 256 * 4096;
constexpr int AUX_NT = 2, AUX_SC1 = 16;
#define ST_AUX 16
constexpr u32 SPIN_CAP = 20000;

constexpr int XN_STR = 1032;
constexpr int A_STR = 136;
constexpr int RG_STR = 68;
constexpr int R_IS = 288, R_GS = 8 * R_IS, R_WS = 2 * R_GS;
constexpr int L_CNT = 0;
constexpr int L_XN = 128;
constexpr int L_QS = L_XN + 8 * XN_STR * 2;
constexpr int L_ST = L_QS + 8 * A_STR * 2;
constexpr int L_F = L_ST + 512;
constexpr int L_F2 = L_F + 256;
constexpr int L_P = L_F2 + 2048;
constexpr int L_RG = L_P + 8192;
constexpr int L_RO = L_RG + 16 * 8 * RG_STR * 4;
constexpr int L_RY = L_RO + 8 * R_WS;
constexpr int L_A = L_RY + 4 * R_WS;
constexpr int L_OW = L_A + 8 * A_STR * 2;
constexpr int L_END = L_OW + 32768;
enum { C_XN = 0, C_RQ, C_QS, C_ST, C_RG, C_VI, C_DI, C_F, C_RO, C_M4, C_Y, C_OW, C_A, C_S5, C_TZ, C_P = 16, L_EP = 29, L_TZ0 = 30, L_TZ1 = 31 };

#define PIN() do { asm volatile("" ::: "memory"); __builtin_amdgcn_sched_barrier(0); } while (0)
#define USE4(x) asm volatile("" ::"v"(x))
#define BAR() do { asm volatile("s_waitcnt lgkmcnt(0)\n\ts_barrier" ::: "memory"); __builtin_amdgcn_sched_barrier(0); } while (0)

#define TS(i)
#define TS_DECL
#define TS_SET(i, v) do {} while (0)
#define TS_FLUSH()

__device__ __forceinline__ f32x4 mfma32(u32x4 a, u32x4 b, f32x4 c) {
  return __builtin_amdgcn_mfma_f32_16x16x32_bf16(__builtin_bit_cast(bf16x8, a), __builtin_bit_cast(bf16x8, b), c, 0, 0, 0);
}
__device__ __forceinline__ f32x4 mfma16(u32x2 a, u32x2 b, f32x4 c) {
  return __builtin_amdgcn_mfma_f32_16x16x16bf16_1k(__builtin_bit_cast(s16x4, a), __builtin_bit_cast(s16x4, b), c, 0, 0, 0);
}
__device__ __forceinline__ float asf(u32 x) { return __builtin_bit_cast(float, x); }
__device__ __forceinline__ u32 asu(float x) { return __builtin_bit_cast(u32, x); }
__device__ __forceinline__ float bf_lo(u32 x) { return asf(x << 16); }
__device__ __forceinline__ float bf_hi(u32 x) { return asf(x & 0xffff0000u); }
__device__ __forceinline__ u32 pack_bf(float lo, float hi) {
  typedef __bf16 bf16x2 __attribute__((ext_vector_type(2)));
  typedef float f32x2 __attribute__((ext_vector_type(2)));
  f32x2 v = {lo, hi};
  return __builtin_bit_cast(u32, __builtin_convertvector(v, bf16x2));
}
__device__ __forceinline__ float rcp(float x) { return __builtin_amdgcn_rcpf(x); }
constexpr float L2E = 1.4426950408889634f;
constexpr int OOB = 0x7ffffff0;
__device__ __forceinline__ float sigm(float x) { return rcp(1.f + __builtin_amdgcn_exp2f(-L2E * x)); }
__device__ __forceinline__ float rbf(float x) { return bf_lo(pack_bf(x, x)); }
__device__ __forceinline__ u32 perm_lo(u32 a, u32 b) { return __builtin_amdgcn_perm(b, a, 0x05040100u); }
__device__ __forceinline__ u32 perm_hi(u32 a, u32 b) { return __builtin_amdgcn_perm(b, a, 0x07060302u); }
template <int CTRL> __device__ __forceinline__ float dpp(float x) {
  return asf(__builtin_amdgcn_mov_dpp(asu(x), CTRL, 0xf, 0xf, true));
}
template <int BANK> __device__ __forceinline__ float ror8_merge(float old, float src) {
  return asf(__builtin_amdgcn_update_dpp(asu(old), asu(src), 0x128, 0xf, BANK, false));
}
constexpr int DPP_X1 = 0xB1, DPP_X2 = 0x4E, DPP_HMIR = 0x141, DPP_MIR = 0x140, DPP_ROR8 = 0x128, DPP_ROR4 = 0x124;
__device__ __forceinline__ void swap32(float& a, float& b) {
  auto r = __builtin_amdgcn_permlane32_swap(asu(a), asu(b), false, false);
  a = asf(r[0]); b = asf(r[1]);
}
__device__ __forceinline__ void swap16(float& a, float& b) {
  auto r = __builtin_amdgcn_permlane16_swap(asu(a), asu(b), false, false);
  a = asf(r[0]); b = asf(r[1]);
}
__device__ __forceinline__ float add32(float x) { float a = x, b = x; swap32(a, b); return a + b; }
__device__ __forceinline__ float add16(float x) { float a = x, b = x; swap16(a, b); return a + b; }
__device__ __forceinline__ float max32(float x) { float a = x, b = x; swap32(a, b); return fmaxf(a, b); }
__device__ __forceinline__ float max16(float x) { float a = x, b = x; swap16(a, b); return fmaxf(a, b); }
__device__ __forceinline__ float wave_sum(float x) {
  x += dpp<DPP_X1>(x); x += dpp<DPP_X2>(x); x += dpp<DPP_HMIR>(x); x += dpp<DPP_MIR>(x);
  return add32(add16(x));
}
__device__ __forceinline__ float wave_max(float f) {
  f = fmaxf(f, dpp<DPP_X1>(f)); f = fmaxf(f, dpp<DPP_X2>(f)); f = fmaxf(f, dpp<DPP_HMIR>(f)); f = fmaxf(f, dpp<DPP_MIR>(f));
  return max32(max16(f));
}
__device__ __forceinline__ u32x4 ldnt(rsrc_t r, int voff, int soff) { return __builtin_amdgcn_raw_buffer_load_b128(r, voff, soff, AUX_NT); }
__device__ __forceinline__ u32x4 ldsc(rsrc_t r, int voff, int soff) { return __builtin_amdgcn_raw_buffer_load_b128(r, voff, soff, AUX_SC1); }
__device__ __forceinline__ rsrc_t mkr(const void* p, int bytes) { return __builtin_amdgcn_make_buffer_rsrc((void*)p, 0, bytes, 0x00020000); }

struct Tags { u32 t[4]; };
__device__ __forceinline__ int win(int o) { return o + (o >= 384 ? 128 : 0); }
__device__ __forceinline__ void publish(rsrc_t r, int voff, f32x4 v, const Tags& tg) {
  u32x4 o;
#pragma unroll
  for (int i = 0; i < 4; ++i) o[i] = ((asu(v[i]) + 0x800u) & 0xfffff000u) | tg.t[i];
  __builtin_amdgcn_raw_buffer_store_b128(o, r, voff, 0, ST_AUX);
}
__device__ __forceinline__ void strip(u32x4& m, const Tags& tg) {
#pragma unroll
  for (int i = 0; i < 4; ++i) m[i] ^= tg.t[i];
}
__device__ __forceinline__ u32 or4(const u32x4& m) { return (m[0] | m[1]) | (m[2] | m[3]); }

struct Params {
  const u16* x; const u16* kc; const u16* vc; const u16* wq; const u16* wo;
  const u16* wg; const u16* wu; const u16* wd;
  u16* out; char* ws;
};
constexpr int P_STEP16 = 58, P_LEAD = -3, PO = 356, SO = 4, PG3 = 290, PD = 388, SD = 7, P4 = 417, PF = 480, MODE = 199, PT = 0, TK0 = 319, SK16 = 60, PQ = 0, PS = 320, TW0 = 100, SW16 = 60, PV = 228, X18 = 22, X19 = 48, X20 = 0, ABL = 0;
#define PACE(pos) PACET((((pos) - P_LEAD) * P_STEP16) >> 4)
#define PACET(t) do { PIN(); const int due_ = (t); while ((int)((u32)__builtin_amdgcn_s_memrealtime() - tz) < due_) __builtin_amdgcn_s_sleep(1); PIN(); } while (0)

template <int STR> __device__ __forceinline__ void store64(f32x4 (&d)[8], float* red, int g, int c) {
  f32x4 s[4];
#pragma unroll
  for (int i = 0; i < 4; ++i)
#pragma unroll
    for (int r = 0; r < 4; ++r) {
      float a = d[i][r], b = d[i + 4][r];
      swap32(a, b);
      s[r][i] = a + dpp<DPP_ROR8>(b);
    }
  if (c < 8) {
#pragma unroll
    for (int r = 0; r < 4; ++r) *(f32x4*)(red + (4 * (g & 1) + r) * STR + 8 * c + 4 * (g >> 1)) = s[r];
  }
}
__device__ __forceinline__ void store_acc(f32x4 (&d)[8], char* red, int g, int c) {
  if (g < 2) {
#pragma unroll
    for (int i = 0; i < 8; ++i) *(f32x4*)(red + g * R_GS + i * R_IS + c * 16) = d[i];
  }
}
__device__ __forceinline__ void tr_pair(const u32x4* w, u32x4 (&f)[8], int jj) {
#pragma unroll
  for (int i = 0; i < 8; ++i) {
    const u32 lo = w[2 * jj][i >> 1], hi = w[2 * jj + 1][i >> 1];
    f[i][jj] = (i & 1) ? perm_hi(lo, hi) : perm_lo(lo, hi);
  }
}
__device__ __forceinline__ void merge_stats(float m, float l, float& M, float& L) {
  M = fmaxf(m, dpp<DPP_ROR8>(m));
  M = max32(max16(M));
  L = l * __builtin_amdgcn_exp2f((m - M) * L2E);
  L += dpp<DPP_ROR8>(L);
  L = add32(add16(L));
}

#define ARRIVE(i) do { PIN(); asm volatile("s_mov_b64 exec, 1\n\tds_add_u32 %0, %1\n\ts_mov_b64 exec, -1" :: "v"(lcnt + 4 * (i)), "v"(1u) : "memory"); PIN(); } while (0)
#define PINV(a, n) do { _Pragma("unroll") for (int i_ = 0; i_ < (n); ++i_) asm volatile("" : "+v"((a)[i_])); } while (0)
#define LWAITS(i, n, SL) do { PIN(); u32 it_ = 0; \
    while (__builtin_amdgcn_readfirstlane(__hip_atomic_load((u32*)(lds + L_CNT) + (i), __ATOMIC_RELAXED, __HIP_MEMORY_SCOPE_WORKGROUP)) < (u32)(n) && ++it_ < (1u << 22)) { SL; } \
    PIN(); } while (0)
#define LWAIT(i, n) LWAITS(i, n, __builtin_amdgcn_s_sleep(1))
#define LWAITF(i, n) LWAITS(i, n, )
#define GETTZ() GETT(L_TZ1)
#define GETT(i) do { PIN(); tz = __builtin_amdgcn_readfirstlane(__hip_atomic_load((u32*)(lds + L_CNT) + (i), __ATOMIC_RELAXED, __HIP_MEMORY_SCOPE_WORKGROUP)); PIN(); } while (0)
#define PRIO(n) do { __builtin_amdgcn_s_setprio(n); PIN(); } while (0)
#define MKTAGS() Tags tg; const u32 e_ = __builtin_amdgcn_readfirstlane(__hip_atomic_load((u32*)(lds + L_CNT) + L_EP, __ATOMIC_RELAXED, __HIP_MEMORY_SCOPE_WORKGROUP)) + 1u; { _Pragma("unroll") for (int i_ = 0; i_ < 4; ++i_) tg.t[i_] = (e_ + 0x4d5u * i_) & 0xfffu; }
#define RETRY(m, n, addr) do { u32 bad_ = 0; \
    _Pragma("unroll") for (int k = 0; k < (n); ++k) { strip(m[k], tg); bad_ |= or4(m[k]); } \
    if (__builtin_expect(__builtin_amdgcn_ballot_w64((bad_ & 0xfffu) != 0) != 0 && !(ABL & 127), 0)) { u32 spin_ = 0; do { asm volatile("; retry" ::: "memory"); \
    bool st_[n]; \
    _Pragma("unroll") for (int k = 0; k < (n); ++k) st_[k] = (or4(m[k]) & 0xfffu) != 0; \
    _Pragma("unroll") for (int k = 0; k < (n); ++k) if (st_[k]) m[k] = ldsc(rws, (addr), 0); \
    PIN(); bad_ = 0; \
    _Pragma("unroll") for (int k = 0; k < (n); ++k) { if (st_[k]) strip(m[k], tg); bad_ |= or4(m[k]); } \
    } while (__builtin_amdgcn_ballot_w64((bad_ & 0xfffu) != 0) != 0 && ++spin_ < SPIN_CAP); TS_SET(21, tsv[21] + spin_ + 1); } } while (0)

__global__ void __launch_bounds__(NT, 4) fused_kernel(Params p) {
  __shared__ __attribute__((aligned(16))) char lds[L_END];
  const int tid = threadIdx.x, lane = tid & 63, wave = __builtin_amdgcn_readfirstlane(tid >> 6);
  const int g = lane >> 4, c = lane & 15, hi = lane >> 5;
  const int wg = blockIdx.x, grp = (wg & 7) * 4 + (wg >> 6), sub = (wg >> 3) & 7;
  const bool ta = wg < 198, th = ta && wg >= 154;
  int cgv[3];
#pragma unroll
  for (int i = 0; i < 3; ++i) {
    const int f = ta ? wg : wg - 198 + 58 * i, fq = f / 7, fr = f - 7 * fq;
    const int n = th ? ((wg - 154) >> 1) * 8 + 3 : fq * 8 + fr + (fr >= 3);
    const int sh = (int)((uintptr_t)(n < 64 ? p.wq : n < 120 ? p.wg : p.wu) >> 7);
    cgv[i] = (th ? i < 2 : f < 154) ? (n & ~7) | ((n - sh) & 7) : -1;
  }
  const int rbase = th ? (wg & 1) * 512 : ta ? 0 : 768;
  const int gw = grp, sO = (sub + 1) & 7, sD = (sub + 3) & 7;
  const int w = wave & 7, wv = wave & 3;
  const rsrc_t rws = mkr(p.ws, WS_END);
  const u32 lcnt = (u32)(uintptr_t)(__attribute__((address_space(3))) char*)lds + L_CNT;
  TS_DECL;
  TS(0);
  u32 tz = (u32)__builtin_amdgcn_s_memrealtime();
  const size_t kvoff = ((size_t)grp * SK + sub * 512 + w * 64) * DH;
  const int rot = (MODE & 2) ? (w + sub) & 3 : 0;
  int kb[4];
#pragma unroll
  for (int i = 0; i < 4; ++i) kb[i] = ((i + rot) & 3) * 4096;
  if (wave < 8) {
    PRIO(3);
    const u32 ep = __builtin_amdgcn_raw_buffer_load_b32(rws, (w == 0 && !(ABL & 2048)) ? 0 : OOB, 0, AUX_SC1);
    u32 xp = 0;
    if (!(ABL & 4096)) xp = *(const u32*)(p.x + w * DM + lane * 16);
    PIN();
    u32x4 wqr[4], gr[8], vr[16];
    const int r0 = rbase + w * 32, r1 = ta ? (rbase + 256 + w * 64) & 1023 : 768 + wv * 64;
    {
      const int cg = cgv[0], st = cg < 64 ? HQD * 2 : FF * 2;
      const rsrc_t r = mkr((const char*)(cg < 64 ? p.wq : cg < 120 ? p.wg : p.wu) + r0 * st + (cg - (cg < 64 ? 0 : cg < 120 ? 64 : 120)) * 128, 31 * st + 128);
      const int vo = (16 * (c >> 3) + 4 * g) * st + (c & 7) * 16;
#pragma unroll
      for (int j = 0; j < 4; ++j) wqr[j] = ldnt(r, vo, j * st);
    }
    if (tid < 32) ((u32*)(lds + L_CNT))[tid] = tid >= L_TZ0 ? tz : 0;
    BAR();
    GETTZ();
    {
      const int cg = w < 4 ? cgv[1] : cgv[2], st = cg < 64 ? HQD * 2 : FF * 2;
      const rsrc_t r = mkr((const char*)(cg < 64 ? p.wq : cg < 120 ? p.wg : p.wu) + r1 * st + (cg - (cg < 64 ? 0 : cg < 120 ? 64 : 120)) * 128, cg < 0 ? 0 : 63 * st + 128);
      const int vo = (32 * (c >> 3) + 8 * g) * st + (c & 7) * 16;
#pragma unroll
      for (int j = 0; j < 8; ++j) { PACET(TW0 + (((j * 7 + w) * SW16) >> 4)); gr[j] = ldnt(r, vo, j * st); }
    }
    PIN();
    TS(1);
    USE4(xp);
    if (w == 0) ((u32*)(lds + L_CNT))[L_EP] = ep;
    u32x2 fq[8];
#pragma unroll
    for (int i = 0; i < 8; ++i)
#pragma unroll
      for (int jj = 0; jj < 2; ++jj) {
        const u32 lo = wqr[2 * jj][i >> 1], hh = wqr[2 * jj + 1][i >> 1];
        fq[i][jj] = (i & 1) ? perm_hi(lo, hh) : perm_lo(lo, hh);
      }
    PINV(fq, 8);
    LWAIT(C_XN, 8);
    TS(2);
    {
      const u32x2 a0 = *(const u32x2*)(lds + L_XN + ((c & 7) * XN_STR + r0 + 16 * (c >> 3) + 4 * g) * 2);
      const u32x4 a1 = *(const u32x4*)(lds + L_XN + ((c & 7) * XN_STR + r1 + 32 * (c >> 3) + 8 * g) * 2);
      f32x4 d[8];
#pragma unroll
      for (int i = 0; i < 8; ++i) d[i] = mfma16(a0, fq[i], f32x4{0, 0, 0, 0});
      if (!ta) {
        store64<RG_STR>(d, (float*)(lds + L_RG) + (8 + w) * 8 * RG_STR, g, c);
#pragma unroll
        for (int i = 0; i < 8; ++i) d[i] = f32x4{0, 0, 0, 0};
      }
      u32x4 f[8];
#pragma unroll
      for (int jj = 0; jj < 4; ++jj) tr_pair(gr, f, jj);
#pragma unroll
      for (int i = 0; i < 8; ++i) d[i] = mfma32(a1, f[i], d[i]);
      store64<RG_STR>(d, (float*)(lds + L_RG) + w * 8 * RG_STR, g, c);
    }
    ARRIVE(C_RG);
    TS(3);
    LWAIT(C_RG, 8);
    MKTAGS();
    if (w == 7) {
      const u32 t20 = tz << 12;
      __builtin_amdgcn_raw_buffer_store_b128(u32x4{t20 | tg.t[0], t20 | tg.t[1], t20 | tg.t[2], t20 | tg.t[3]}, rws,
                                             (lane == 0 && !(wg & 24) && !(MODE & 16)) ? WS_T + ((wg >> 5) * 8 + (wg & 7)) * 16 : OOB, 0, AUX_SC1);
    }
    if (w < (ta ? 2 : 6)) {
      const int t = w >> 1, cg = t == 0 ? cgv[0] : t == 1 ? cgv[1] : cgv[2], l = (w & 1) * 64 + lane;
      const bool isq = cg < 64;
      const int b = isq ? l >> 4 : l & 7, cq = isq ? l & 15 : (l >> 5) * 4 + ((l >> 3) & 1) * 2 + ((l >> 4) & 1);
      const float* rg = (const float*)(lds + L_RG) + ((t == 0 ? (ta ? 0 : 8) : 4 * t - 4) * 8 + b) * RG_STR + cq * 4;
      f32x4 s = *(const f32x4*)rg;
#pragma unroll
      for (int k = 1; k < 4; ++k) s += *(const f32x4*)(rg + k * 8 * RG_STR);
      if (t == 0) {
#pragma unroll
        for (int k = 4; k < 8; ++k) s += *(const f32x4*)(rg + k * 8 * RG_STR);
      }
      const int slot = rbase != 0, gu = cg >= 120;
      const int aq = WS_Q + (b * 32 + (cg >> 1)) * 2048 + win(slot * 512 + (cg & 1) * 256 + cq * 16);
      const int ag = WS_GU + ((cg - (gu ? 120 : 64)) * 4 + (l >> 5)) * 4096 + slot * 2048 + win(((l >> 3) & 3) * 256 + gu * 128 + b * 16);
      publish(rws, (cg < 0 || (ABL & 1)) ? OOB : isq ? aq : ag, s, tg);
    }
    TS(4);
    GETTZ();
    PRIO(0);
    const rsrc_t rv = mkr(p.vc + kvoff, 64 * DH * 2);
#pragma unroll
    for (int n = 0; n < 16; ++n) { if (n >= 3 && n <= 9) GETTZ(); PACE(PV + n * 8 + w + (n >= 8 ? (n - 7) * X20 : 0)); vr[n] = ldnt(rv, lane * 16 + (n & 3) * 1024, kb[n >> 2]); }
    ARRIVE(C_VI);
    TS(6);
    PRIO(3);
    u32x4 vt[8];
#pragma unroll
    for (int jj = 0; jj < 4; ++jj) tr_pair(vr, vt, jj);
    PINV(vt, 8);
    LWAIT(C_P + w, 1);
    TS(7);
    f32x4 acc[8];
    {
      const u32x4 pf = *(const u32x4*)(lds + L_P + w * 1024 + lane * 16);
#pragma unroll
      for (int i = 0; i < 8; ++i) acc[i] = mfma32(pf, vt[i], f32x4{0, 0, 0, 0});
    }
    PIN();
    TS(8);
    const u32x4 pf1 = *(const u32x4*)(lds + L_P + w * 1024 + (lane ^ 8) * 16);
#pragma unroll
    for (int jj = 0; jj < 4; ++jj) { tr_pair(vr + 8, vt, jj); PIN(); }
#pragma unroll
    for (int i = 0; i < 8; ++i) acc[i] = mfma32(pf1, vt[i], acc[i]);
    store_acc(acc, lds + L_RO + w * R_WS, g, c);
    ARRIVE(C_RO);
    TS(9);
    LWAIT(C_F, 1);
    LWAIT(C_RO, 8);
    TS(10);
    {
      const int g2 = w >> 2, i = lane & 7, cl = (lane >> 3) & 3;
      const char* ro = lds + L_RO + hi * 4 * R_WS + g2 * R_GS + i * R_IS + (wv * 4 + cl) * 16;
      const char* fp = lds + L_F + hi * 128 + g2 * 16;
      f32x4 o = *(const f32x4*)ro * *(const f32x4*)fp;
#pragma unroll
      for (int k = 1; k < 4; ++k) o += *(const f32x4*)(ro + k * R_WS) * *(const f32x4*)(fp + k * 32);
#pragma unroll
      for (int r = 0; r < 4; ++r) o[r] = add32(o[r]);
      publish(rws, (hi || (ABL & 16)) ? OOB : WS_O + (((grp & 3) * 2 + g2) * 4 + wv) * 65536 + (grp >> 2) * 8192 + (sub >> 1) * 2048 + win((sub & 1) * 512 + lane * 16), o, tg);
    }
    ARRIVE(C_M4);
    TS(11);
    u32x4 sm[1];
    const bool sv = ((lane >> 2) & 1) == ((gw >> 2) & 1);
    const int stoff = sv ? WS_ST + (w * 4 + (gw >> 3)) * 2048 + win(lane * 16) : OOB;
    sm[0] = ldsc(rws, stoff, 0);
    LWAIT(C_M4, 8);
    GETT((MODE & 32) ? L_TZ1 : L_TZ0);
    PACE(P4);
    if (MODE & 128) LWAITF(C_DI, 4);
    u32x4 om[4], bw[4];
    const int ooff = (ABL & 32) ? OOB - 16384 : WS_O + gw * 65536 + w * 8192 + win(lane * 16);
#pragma unroll
    for (int k = 0; k < 4; ++k) om[k] = ldsc(rws, ooff, k * 2048);
    TS(12);
    if (!sv) sm[0] = u32x4{tg.t[0], tg.t[1], tg.t[2], tg.t[3]};
    RETRY(sm, 1, stoff);
    f32x4 fs[4];
    {
      float M, L;
      merge_stats(asf(sm[0][0]), asf(sm[0][1]) + asf(sm[0][2]), M, L);
      *(float*)(lds + L_F2 + w * 256 + lane * 4) = __builtin_amdgcn_exp2f((asf(sm[0][0]) - M) * L2E) * rcp(L);
      PIN();
#pragma unroll
      for (int k = 0; k < 4; ++k) fs[k] = *(const f32x4*)(lds + L_F2 + w * 256 + (2 * k + hi) * 32 + ((gw >> 2) & 1) * 16);
    }
    LWAIT(C_OW, 4);
#pragma unroll
    for (int k = 0; k < 4; ++k) bw[k] = *(const u32x4*)(lds + L_OW + k * 8192 + w * 1024 + lane * 16);
    PINV(bw, 4);
    PINV(fs, 4);
    TS(13);
    RETRY(om, 4, ooff + k * 2048);
    TS(14);
    {
      float v[4];
#pragma unroll
      for (int i = 0; i < 4; ++i) v[i] = (asf(om[0][i]) * fs[0][i] + asf(om[1][i]) * fs[1][i]) + (asf(om[2][i]) * fs[2][i] + asf(om[3][i]) * fs[3][i]);
      swap32(v[0], v[2]); swap32(v[1], v[3]);
      *(u32*)(lds + L_A + (w * A_STR + (lane & 31) * 4 + 2 * hi) * 2) = pack_bf(v[0] + v[2], v[1] + v[3]);
    }
    ARRIVE(C_A);
    LWAITF(C_A, 8);
    TS(15);
    {
      u32x4 a[4];
#pragma unroll
      for (int k = 0; k < 4; ++k) a[k] = *(const u32x4*)(lds + L_A + ((c & 7) * A_STR + 32 * k + 8 * g) * 2);
      f32x4 d0 = mfma32(a[0], bw[0], f32x4{0, 0, 0, 0}), d1 = mfma32(a[1], bw[1], f32x4{0, 0, 0, 0});
      d0 = mfma32(a[2], bw[2], d0); d1 = mfma32(a[3], bw[3], d1);
      publish(rws, hi ? OOB : WS_FS + (((2 * sO + (c >> 3)) * 2 + g) * 8 + w) * 4096 + gw * 128 + (c & 7) * 16, d0 + d1, tg);
    }
    ARRIVE(C_S5);
    TS(16);
  } else {
    PRIO(3);
    const u32x4 xa = *(const u32x4*)(p.x + w * DM + lane * 8);
    const u32x4 xb = *(const u32x4*)(p.x + w * DM + 512 + lane * 8);
    const int oidx = (4 * ((wg >> 3) & 1) + g) * DM + (wg >> 4) * 64 + (c & 7) * 8 + (wg & 7);
    const u32 xfin = p.x[oidx];
    u32 tp = 0;
    if ((ABL & 128) && w == ((ABL >> 8) & 7)) {
      const char* a = (const char*)p.kc + kvoff * 2;
      if (lane == 1) a = (const char*)p.vc + kvoff * 2;
      if (lane == 2) a = (const char*)(p.wo + (size_t)((gw >> 2) * 512 + (gw & 3) * 32) * DM + sO * 128);
      if (lane == 3) a = (const char*)(p.wo + (size_t)((gw >> 2) * 512 + (gw & 3) * 32 + 384) * DM + sO * 128);
      if (lane == 4) a = (const char*)(p.wd + (size_t)(gw * 112) * DM + sD * 128);
      if (lane == 5) a = (const char*)(p.wd + (size_t)(gw * 112 + 111) * DM + sD * 128);
      if (lane == 6) a = (const char*)p.out;
      if (lane >= 8 && lane < 8 + 6) a = p.ws + (lane - 8) * 1048576 + 4096 + wg * 2048 + 1536;
      if ((PQ >> lane) & 1) tp = __builtin_nontemporal_load((const u32*)a);
    }
    asm volatile("s_barrier" ::: "memory");
    GETTZ();
    {
      float v[16];
#pragma unroll
      for (int i = 0; i < 4; ++i) { v[2 * i] = bf_lo(xa[i]); v[2 * i + 1] = bf_hi(xa[i]); v[8 + 2 * i] = bf_lo(xb[i]); v[9 + 2 * i] = bf_hi(xb[i]); }
      float s = 0.f;
#pragma unroll
      for (int i = 0; i < 16; ++i) s += v[i];
      const float mean = wave_sum(s) * (1.f / DM);
      float q = 0.f;
#pragma unroll
      for (int i = 0; i < 16; ++i) { const float d = v[i] - mean; q += d * d; }
      const float var = rbf(wave_sum(q) * (1.f / DM));
      const float mb = rbf(mean);
      const float rs = rcp(rbf(__builtin_amdgcn_sqrtf(rbf(var + 1e-5f))));
      u32x4 o[2];
#pragma unroll
      for (int i = 0; i < 8; ++i) {
        const u32 d = pack_bf(v[2 * i] - mb, v[2 * i + 1] - mb);
        o[i >> 2][i & 3] = pack_bf(bf_lo(d) * rs, bf_hi(d) * rs);
      }
      *(u32x4*)(lds + L_XN + (w * XN_STR + lane * 8) * 2) = o[0];
      *(u32x4*)(lds + L_XN + (w * XN_STR + 512 + lane * 8) * 2) = o[1];
    }
    ARRIVE(C_XN);
    TS(17);
    PRIO(2);
    const rsrc_t rk = mkr(p.kc + kvoff, 64 * DH * 2);
    const int kvo = (4 * (c & 3) + ((c >> 2) & 1)) * 256 + (4 * (c >> 3) + g) * 16;
    u32x4 kr[16];
#pragma unroll
    for (int n = 0; n < 16; ++n) { if (!(n & 1)) PACET(TK0 + (((n * 8 + w * 2) * SK16) >> 4) + (n * 8 + w * 2 >= X19 ? X18 : 0)); kr[n] = ldnt(rk, kvo + ((n >> 1) & 1) * 512 + (n & 1) * 128, kb[n >> 2]); }
    PIN();
    TS(1);
    USE4(tp);
    PRIO(3);
    u32x4 qm[1];
    const int qoff = (ABL & 2) ? OOB : WS_Q + ((grp >> 2) * 32 + (grp & 3) * 8 + w) * 2048 + win(lane * 16);
    qm[0] = ldsc(rws, qoff, 0);
    u32x4 tm = ldsc(rws, (w == 7 && !(MODE & 16)) ? WS_T + lane * 16 : OOB, 0);
    PIN();
    LWAIT(C_RG, 8);
    MKTAGS();
    RETRY(qm, 1, qoff);
    TS(2);
    {
      float q0 = asf(qm[0][0]), q1 = asf(qm[0][1]), q2 = asf(qm[0][2]), q3 = asf(qm[0][3]);
      swap32(q0, q2); swap32(q1, q3);
      *(u32*)(lds + L_QS + (w * A_STR + (lane & 31) * 4 + 2 * hi) * 2) = pack_bf(q0 + q2, q1 + q3);
    }
    if (w == 7) {
      strip(tm, tg);
      int d = (int)(tm[0] - (tz << 12)) >> 12;
      const bool ok = (or4(tm) & 0xfffu) == 0 && d >= -150 && d <= 150 && (MODE & 4);
      if (!ok) d = 0;
      if (MODE & 64) {
        const int n = __builtin_popcountll(__builtin_amdgcn_ballot_w64(ok));
        const u32 tc = tz + (int)(wave_sum((float)d) * rcp((float)(n + !n)));
        ((u32*)(lds + L_CNT))[L_TZ0 + (lane & 1)] = tc;
      } else {
        ((u32*)(lds + L_CNT))[L_TZ0] = tz + (int)wave_max((float)d);
      }
    }
    ARRIVE(C_QS);
    LWAIT(C_QS, 8);
    TS(3);
    {
      const u32x4 q0 = *(const u32x4*)(lds + L_QS + ((c & 7) * A_STR + 32 * (c >> 3) + 8 * g) * 2);
      const u32x4 q1 = *(const u32x4*)(lds + L_QS + ((c & 7) * A_STR + 64 + 32 * (c >> 3) + 8 * g) * 2);
      float sc[8];
#pragma unroll
      for (int e = 0; e < 2; ++e) {
        f32x4 t[4];
#pragma unroll
        for (int ua = 0; ua < 4; ++ua) t[ua] = mfma32(kr[(ua >> 1) * 8 + e * 4 + (ua & 1) * 2], q0, f32x4{0, 0, 0, 0});
#pragma unroll
        for (int ua = 0; ua < 4; ++ua) t[ua] = mfma32(kr[(ua >> 1) * 8 + e * 4 + (ua & 1) * 2 + 1], q1, t[ua]);
#pragma unroll
        for (int r = 0; r < 4; ++r) {
          float a = t[0][r], b = t[1][r], a2 = t[2][r], b2 = t[3][r];
          swap32(a, b); swap32(a2, b2);
          sc[e * 4 + r] = ror8_merge<0x3>(b2, b) + ror8_merge<0xc>(a, a2);
        }
      }
      float mx = -INFINITY;
#pragma unroll
      for (int i = 0; i < 4; ++i) {
        const u32 s1 = pack_bf(sc[2 * i], sc[2 * i + 1]);
        const u32 s2 = pack_bf(bf_lo(s1) * 0.08838834764831845f, bf_hi(s1) * 0.08838834764831845f);
        sc[2 * i] = bf_lo(s2); sc[2 * i + 1] = bf_hi(s2);
        mx = fmaxf(mx, fmaxf(sc[2 * i], sc[2 * i + 1]));
      }
      mx = fmaxf(mx, dpp<DPP_ROR8>(mx));
      const float mw = max32(max16(mx));
      float l = 0.f;
      u32x4 pf;
#pragma unroll
      for (int i = 0; i < 4; ++i) {
        const float e0 = __builtin_amdgcn_exp2f((sc[2 * i] - mw) * L2E), e1 = __builtin_amdgcn_exp2f((sc[2 * i + 1] - mw) * L2E);
        l += e0 + e1;
        pf[i] = pack_bf(e0, e1);
      }
      *(u32x4*)(lds + L_P + w * 1024 + lane * 16) = pf;
      ARRIVE(C_P + w);
      l += dpp<DPP_ROR8>(l);
      l = add32(add16(l));
      if (lane < 8) *(float2*)(lds + L_ST + (w * 8 + lane) * 8) = make_float2(mw, l);
    }
    ARRIVE(C_ST);
    TS(4);
    u32x4 wr[8], wt[8];
    if (w >= 4) {
      if (MODE & 8) LWAIT(C_VI, 8);
      PACE(PG3 + wv);
      TS(7);
      u32x4 gm[4];
      const int gbase = (ABL & 8) ? OOB - 16384 : WS_GU + (gw * 7 + 2 * wv + (wv == 3 ? 0 : hi)) * 4096;
      const int goff0 = gbase + win((g & 1) * 256 + c * 16), goff1 = gbase + 640 + (g & 1) * 256 + c * 16;
#pragma unroll
      for (int k = 0; k < 4; ++k) gm[k] = ldsc(rws, (k & 1) ? goff1 : goff0, (k >> 1) * 2048);
      RETRY(gm, 4, ((k & 1) ? goff1 : goff0) + (k >> 1) * 2048);
      TS(8);
      u32x4 a;
#pragma unroll
      for (int i = 0; i < 4; ++i) {
        const int qp = i >> 1, e = (i & 1) * 2;
        const float v0 = asf(gm[qp][e]) + asf(gm[2 + qp][e]), v1 = asf(gm[qp][e + 1]) + asf(gm[2 + qp][e + 1]);
        const u32 gg = pack_bf(v0, v1);
        const u32 uu = asu(dpp<DPP_ROR8>(asf(gg)));
        const u32 sg = pack_bf(sigm(bf_lo(gg)), sigm(bf_hi(gg)));
        a[i] = pack_bf(bf_lo(sg) * bf_lo(uu), bf_hi(sg) * bf_hi(uu));
        if (wv == 3 && hi) a[i] = 0;
      }
      PINV(a, 4);
      TS(9);
      GETTZ();
      PRIO(0);
      const rsrc_t rw = mkr(p.wd + (size_t)(gw * 112 + 32 * wv) * DM + sD * 128, (wv == 3 ? 16 : 32) * DM * 2 - sD * 256);
#pragma unroll
      for (int j = 0; j < 8; ++j) { PACE(PD + j * SD / 2 + wv); wr[j] = ldnt(rw, 8 * g * (DM * 2) + c * 16, j * DM * 2); }
      ARRIVE(C_DI);
      PRIO(3);
      TS(10);
#pragma unroll
      for (int jj = 0; jj < 4; ++jj) { tr_pair(wr, wt, jj); PIN(); }
      f32x4 d[8];
#pragma unroll
      for (int i = 0; i < 8; ++i) d[i] = mfma32(a, wt[i], f32x4{0, 0, 0, 0});
      store_acc(d, lds + L_RY + wv * R_WS, g, c);
      ARRIVE(C_Y);
      TS(11);
      LWAITF(C_Y, 4);
      {
        const int t = wv * 64 + lane;
        const char* rf = lds + L_RY + (t >> 7) * R_GS + ((t >> 3) & 7) * R_IS + (((t >> 6) & 1) * 8 + (t & 7)) * 16;
        f32x4 s = *(const f32x4*)rf;
#pragma unroll
        for (int k = 1; k < 4; ++k) s += *(const f32x4*)(rf + k * R_WS);
        publish(rws, WS_FY + (((2 * sD + ((t >> 6) & 1)) * 2 + (t >> 7)) * 8 + ((t >> 3) & 7)) * 4096 + gw * 128 + (t & 7) * 16, s, tg);
      }
      ARRIVE(C_S5);
      TS(12);
    } else {
      if (w == 0) {
        LWAIT(C_ST, 8);
        const float2 ml = *(const float2*)(lds + L_ST + lane * 8);
        float M, L;
        merge_stats(ml.x, ml.y, M, L);
        const float Lh = asf((asu(L) + 0x800u) & 0xfffff000u);
        publish(rws, lane < 8 ? WS_ST + grp * 2048 + win(sub * 128) + lane * 16 : OOB, f32x4{M, Lh, L - Lh, 0.f}, tg);
        *(float*)(lds + L_F + lane * 4) = __builtin_amdgcn_exp2f((ml.x - M) * L2E);
        ARRIVE(C_F);
        TS(6);
      }
      GETTZ();
      TS(7);
      PRIO(0);
      const rsrc_t rw = mkr(p.wo + (size_t)((gw >> 2) * 512 + (gw & 3) * 32 + 8 * wv) * DM + sO * 128, 400 * DM * 2);
#pragma unroll
      for (int j = 0; j < 8; ++j) { if (j == (MODE >> 8)) LWAIT(C_VI, 8); PACE(j < (MODE >> 8) ? PS + j * PT + wv : PO + (j - (MODE >> 8)) * SO + wv); wr[j] = ldnt(rw, 2 * g * (DM * 2) + c * 16, ((j & 3) * 128 + (j >> 2)) * DM * 2); }
      PIN();
      PRIO(3);
      TS(8);
#pragma unroll
      for (int jj = 0; jj < 4; ++jj) { tr_pair(wr, wt, jj); PIN(); }
#pragma unroll
      for (int i = 0; i < 8; ++i) *(u32x4*)(lds + L_OW + wv * 8192 + i * 1024 + lane * 16) = wt[i];
      ARRIVE(C_OW);
      TS(10);
      if (w == 0) {
      GETT(L_TZ0);
      LWAIT(C_S5, 12);
      PACE(PF);
      TS(12);
      u32x4 fm[8];
      const int foff = WS_FS + wg * 4096 + lane * 16;
#pragma unroll
      for (int k = 0; k < 8; ++k) fm[k] = ldsc(rws, foff, (k >> 2) * (WS_FY - WS_FS) + (k & 3) * 1024);
      RETRY(fm, 8, foff + (k >> 2) * (WS_FY - WS_FS) + (k & 3) * 1024);
      TS(14);
      float z[2];
#pragma unroll
      for (int h = 0; h < 2; ++h) {
        float v[4];
#pragma unroll
        for (int i = 0; i < 4; ++i) v[i] = (asf(fm[4 * h][i]) + asf(fm[4 * h + 1][i])) + (asf(fm[4 * h + 2][i]) + asf(fm[4 * h + 3][i]));
        swap32(v[0], v[2]); swap32(v[1], v[3]);
        v[0] += v[2]; v[1] += v[3];
        swap16(v[0], v[1]);
        const float t = v[0] + v[1];
        z[h] = rbf(t + dpp<DPP_ROR8>(t));
      }
      z[1] = rbf(z[1] * sigm(z[1]));
      const float r = bf_lo(xfin) + rbf(z[1] * z[0]);
      if (c < 8) p.out[oidx] = (u16)pack_bf(r, r);
      if (wg == 0 && lane == 0) __builtin_amdgcn_raw_buffer_store_b32(e_, rws, 0, 0, AUX_SC1);
      TS(15);
    }
    }
  }
  TS_FLUSH();
}

extern "C" void kernel_launch(void* const* d_in, const int* in_sizes, int n_in, void* d_out, int out_size, void* d_ws, size_t ws_size,
                              hipStream_t stream) {
  Params p;
  p.x = (const u16*)d_in[0]; p.kc = (const u16*)d_in[1]; p.vc = (const u16*)d_in[2]; p.wq = (const u16*)d_in[3]; p.wo = (const u16*)d_in[4];
  p.wg = (const u16*)d_in[6]; p.wu = (const u16*)d_in[7]; p.wd = (const u16*)d_in[8];
  p.out = (u16*)d_out; p.ws = (char*)d_ws;
  fused_kernel<<<NWG, NT, 0, stream>>>(p);
}
```

```cpp
#include <hip/hip_runtime.h>
#include <hip/hip_bf16.h>
#include <stdint.h>

typedef unsigned int u32;
typedef unsigned short u16;
typedef u32 u32x4 __attribute__((ext_vector_type(4)));
typedef u32 u32x2 __attribute__((ext_vector_type(2)));
typedef float f32x4 __attribute__((ext_vector_type(4)));
typedef __bf16 bf16x8 __attribute__((ext_vector_type(8)));
typedef short s16x4 __attribute__((ext_vector_type(4)));
typedef __amdgpu_buffer_rsrc_t rsrc_t;

constexpr int DM = 1024, HQD = 4096, SK = 4096, DH = 128, FF = 3584;
constexpr int NWG = 256, NT = 1024;
#define TS_WAVES 16

constexpr int WS_T = 1024;
constexpr int WS_Q = 4096;
constexpr int WS_GU = WS_Q + 256 * 2048;
constexpr int WS_O = WS_GU + 224 * 4096;
constexpr int WS_ST = WS_O + 32 * 65536;
constexpr int WS_FS = WS_ST + 32 * 2048;
constexpr int WS_FY = WS_FS + 256 * 4096;
constexpr int WS_END = WS_FY + 256 * 4096;
constexpr int AUX_NT = 2, AUX_SC1 = 16;
#define ST_AUX 16
#define TAGB 12
constexpr u32 TM = (1u << TAGB) - 1;
constexpr u32 SPIN_CAP = 20000;

constexpr int XN_STR = 1032;
constexpr int A_STR = 136;
constexpr int RG_STR = 68;
constexpr int R_IS = 288, R_GS = 8 * R_IS, R_WS = 2 * R_GS;
constexpr int L_CNT = 0;
constexpr int L_XN = 128;
constexpr int L_QS = L_XN + 8 * XN_STR * 2;
constexpr int L_ST = L_QS + 8 * A_STR * 2;
constexpr int L_F = L_ST + 512;
constexpr int L_F2 = L_F + 256;
constexpr int L_P = L_F2 + 2048;
constexpr int L_RG = L_P + 8192;
constexpr int L_RO = L_RG + 16 * 8 * RG_STR * 4;
constexpr int L_RY = L_RO + 8 * R_WS;
constexpr int L_A = L_RY + 4 * R_WS;
constexpr int L_OW = L_A + 8 * A_STR * 2;
constexpr int L_END = L_OW + 32768;
enum { C_XN = 0, C_RQ, C_QS, C_ST, C_RG, C_VI, C_DI, C_F, C_RO, C_M4, C_Y, C_OW, C_A, C_S5, C_TZ, C_P = 16, L_EP = 29, L_TZ0 = 30, L_TZ1 = 31 };

#define PIN() do { asm volatile("" ::: "memory"); __builtin_amdgcn_sched_barrier(0); } while (0)
#define USE4(x) asm volatile("" ::"v"(x))
#define BAR() do { asm volatile("s_waitcnt lgkmcnt(0)\n\ts_barrier" ::: "memory"); __builtin_amdgcn_sched_barrier(0); } while (0)

#define TS(i)
#define TS_DECL
#define TS_SET(i, v) do {} while (0)
#define TS_FLUSH()

__device__ __forceinline__ f32x4 mfma32(u32x4 a, u32x4 b, f32x4 c) {
  return __builtin_amdgcn_mfma_f32_16x16x32_bf16(__builtin_bit_cast(bf16x8, a), __builtin_bit_cast(bf16x8, b), c, 0, 0, 0);
}
__device__ __forceinline__ f32x4 mfma16(u32x2 a, u32x2 b, f32x4 c) {
  return __builtin_amdgcn_mfma_f32_16x16x16bf16_1k(__builtin_bit_cast(s16x4, a), __builtin_bit_cast(s16x4, b), c, 0, 0, 0);
}
__device__ __forceinline__ float asf(u32 x) { return __builtin_bit_cast(float, x); }
__device__ __forceinline__ u32 asu(float x) { return __builtin_bit_cast(u32, x); }
__device__ __forceinline__ float bf_lo(u32 x) { return asf(x << 16); }
__device__ __forceinline__ float bf_hi(u32 x) { return asf(x & 0xffff0000u); }
__device__ __forceinline__ u32 pack_bf(float lo, float hi) {
  typedef __bf16 bf16x2 __attribute__((ext_vector_type(2)));
  typedef float f32x2 __attribute__((ext_vector_type(2)));
  f32x2 v = {lo, hi};
  return __builtin_bit_cast(u32, __builtin_convertvector(v, bf16x2));
}
__device__ __forceinline__ float rcp(float x) { return __builtin_amdgcn_rcpf(x); }
constexpr float L2E = 1.4426950408889634f;
constexpr int OOB = 0x7ffffff0;
__device__ __forceinline__ float sigm(float x) { return rcp(1.f + __builtin_amdgcn_exp2f(-L2E * x)); }
__device__ __forceinline__ float rbf(float x) { return bf_lo(pack_bf(x, x)); }
__device__ __forceinline__ u32 perm_lo(u32 a, u32 b) { return __builtin_amdgcn_perm(b, a, 0x05040100u); }
__device__ __forceinline__ u32 perm_hi(u32 a, u32 b) { return __builtin_amdgcn_perm(b, a, 0x07060302u); }
template <int CTRL> __device__ __forceinline__ float dpp(float x) {
  return asf(__builtin_amdgcn_mov_dpp(asu(x), CTRL, 0xf, 0xf, true));
}
template <int BANK> __device__ __forceinline__ float ror8_merge(float old, float src) {
  return asf(__builtin_amdgcn_update_dpp(asu(old), asu(src), 0x128, 0xf, BANK, false));
}
constexpr int DPP_X1 = 0xB1, DPP_X2 = 0x4E, DPP_HMIR = 0x141, DPP_MIR = 0x140, DPP_ROR8 = 0x128, DPP_ROR4 = 0x124;
__device__ __forceinline__ void swap32(float& a, float& b) {
  auto r = __builtin_amdgcn_permlane32_swap(asu(a), asu(b), false, false);
  a = asf(r[0]); b = asf(r[1]);
}
__device__ __forceinline__ void swap16(float& a, float& b) {
  auto r = __builtin_amdgcn_permlane16_swap(asu(a), asu(b), false, false);
  a = asf(r[0]); b = asf(r[1]);
}
__device__ __forceinline__ float add32(float x) { float a = x, b = x; swap32(a, b); return a + b; }
__device__ __forceinline__ float add16(float x) { float a = x, b = x; swap16(a, b); return a + b; }
__device__ __forceinline__ float max32(float x) { float a = x, b = x; swap32(a, b); return fmaxf(a, b); }
__device__ __forceinline__ float max16(float x) { float a = x, b = x; swap16(a, b); return fmaxf(a, b); }
__device__ __forceinline__ float wave_sum(float x) {
  x += dpp<DPP_X1>(x); x += dpp<DPP_X2>(x); x += dpp<DPP_HMIR>(x); x += dpp<DPP_MIR>(x);
  return add32(add16(x));
}
__device__ __forceinline__ float wave_max(float f) {
  f = fmaxf(f, dpp<DPP_X1>(f)); f = fmaxf(f, dpp<DPP_X2>(f)); f = fmaxf(f, dpp<DPP_HMIR>(f)); f = fmaxf(f, dpp<DPP_MIR>(f));
  return max32(max16(f));
}
__device__ __forceinline__ u32x4 ldnt(rsrc_t r, int voff, int soff) { return __builtin_amdgcn_raw_buffer_load_b128(r, voff, soff, AUX_NT); }
__device__ __forceinline__ u32x4 ldsc(rsrc_t r, int voff, int soff) { return __builtin_amdgcn_raw_buffer_load_b128(r, voff, soff, AUX_SC1); }
__device__ __forceinline__ rsrc_t mkr(const void* p, int bytes) { return __builtin_amdgcn_make_buffer_rsrc((void*)p, 0, bytes, 0x00020000); }

struct Tags { u32 t[4]; };
__device__ __forceinline__ int win(int o) { return o + (o >= 384 ? 128 : 0); }
__device__ __forceinline__ void publish(rsrc_t r, int voff, f32x4 v, const Tags& tg, u32 tm = TM) {
  u32x4 o;
#pragma unroll
  for (int i = 0; i < 4; ++i) o[i] = ((asu(v[i]) + (tm + 1) / 2) & ~tm) | tg.t[i];
  __builtin_amdgcn_raw_buffer_store_b128(o, r, voff, 0, ST_AUX);
}
__device__ __forceinline__ Tags qtags(u32 e) {
  Tags t;
  t.t[0] = e & 15; t.t[1] = (e + 1 + ((e >> 4) & 7)) & 15; t.t[2] = (e + (e >> 7)) & 15; t.t[3] = (e + (e >> 11)) & 15;
  return t;
}
__device__ __forceinline__ void strip(u32x4& m, const Tags& tg) {
#pragma unroll
  for (int i = 0; i < 4; ++i) m[i] ^= tg.t[i];
}
__device__ __forceinline__ u32 or4(const u32x4& m) { return (m[0] | m[1]) | (m[2] | m[3]); }

struct Params {
  const u16* x; const u16* kc; const u16* vc; const u16* wq; const u16* wo;
  const u16* wg; const u16* wu; const u16* wd;
  u16* out; char* ws;
};
constexpr int P_STEP16 = 58, P_LEAD = 4, PO = 360, SO = 4, PG3 = 352, PD = 396, SD = 7, P4 = 408, PF = 480, MODE = 7, PT = 0, TK0 = 310, SK16 = 58, PQ = 0, PS = 320, TW0 = 100, SW16 = 58, PV = 228, X18 = 22, X19 = 48, X20 = 0, ABL = 0;
#define PACE(pos) PACET((((pos) - P_LEAD) * P_STEP16) >> 4)
#define PACET(t) do { PIN(); const int due_ = (t); while ((int)((u32)__builtin_amdgcn_s_memrealtime() - tz) < due_) __builtin_amdgcn_s_sleep(1); PIN(); } while (0)

template <int STR> __device__ __forceinline__ void store64(f32x4 (&d)[8], float* red, int g, int c) {
  f32x4 s[4];
#pragma unroll
  for (int i = 0; i < 4; ++i)
#pragma unroll
    for (int r = 0; r < 4; ++r) {
      float a = d[i][r], b = d[i + 4][r];
      swap32(a, b);
      s[r][i] = a + dpp<DPP_ROR8>(b);
    }
  if (c < 8) {
#pragma unroll
    for (int r = 0; r < 4; ++r) *(f32x4*)(red + (4 * (g & 1) + r) * STR + 8 * c + 4 * (g >> 1)) = s[r];
  }
}
__device__ __forceinline__ void store_acc(f32x4 (&d)[8], char* red, int g, int c) {
  if (g < 2) {
#pragma unroll
    for (int i = 0; i < 8; ++i) *(f32x4*)(red + g * R_GS + i * R_IS + c * 16) = d[i];
  }
}
__device__ __forceinline__ void tr_pair(const u32x4* w, u32x4 (&f)[8], int jj) {
#pragma unroll
  for (int i = 0; i < 8; ++i) {
    const u32 lo = w[2 * jj][i >> 1], hi = w[2 * jj + 1][i >> 1];
    f[i][jj] = (i & 1) ? perm_hi(lo, hi) : perm_lo(lo, hi);
  }
}
__device__ __forceinline__ void merge_stats(float m, float l, float& M, float& L) {
  M = fmaxf(m, dpp<DPP_ROR8>(m));
  M = max32(max16(M));
  L = l * __builtin_amdgcn_exp2f((m - M) * L2E);
  L += dpp<DPP_ROR8>(L);
  L = add32(add16(L));
}

#define ARRIVE(i) do { PIN(); asm volatile("s_mov_b64 exec, 1\n\tds_add_u32 %0, %1\n\ts_mov_b64 exec, -1" :: "v"(lcnt + 4 * (i)), "v"(1u) : "memory"); PIN(); } while (0)
#define PINV(a, n) do { _Pragma("unroll") for (int i_ = 0; i_ < (n); ++i_) asm volatile("" : "+v"((a)[i_])); } while (0)
#define LWAITS(i, n, SL) do { PIN(); u32 it_ = 0; \
    while (__builtin_amdgcn_readfirstlane(__hip_atomic_load((u32*)(lds + L_CNT) + (i), __ATOMIC_RELAXED, __HIP_MEMORY_SCOPE_WORKGROUP)) < (u32)(n) && ++it_ < (1u << 22)) { SL; } \
    PIN(); } while (0)
#define LWAIT(i, n) LWAITS(i, n, __builtin_amdgcn_s_sleep(1))
#define LWAITF(i, n) LWAITS(i, n, )
#define GETTZ() GETT(L_TZ1)
#define GETT(i) do { PIN(); tz = __builtin_amdgcn_readfirstlane(__hip_atomic_load((u32*)(lds + L_CNT) + (i), __ATOMIC_RELAXED, __HIP_MEMORY_SCOPE_WORKGROUP)); PIN(); } while (0)
#define PRIO(n) do { __builtin_amdgcn_s_setprio(n); PIN(); } while (0)
#define MKTAGS() Tags tg; const u32 e_ = __builtin_amdgcn_readfirstlane(__hip_atomic_load((u32*)(lds + L_CNT) + L_EP, __ATOMIC_RELAXED, __HIP_MEMORY_SCOPE_WORKGROUP)) + 1u; { _Pragma("unroll") for (int i_ = 0; i_ < 4; ++i_) tg.t[i_] = (e_ + 0x4d5u * i_) & TM; }
#define RETRY(m, n, addr) do { u32 bad_ = 0; \
    _Pragma("unroll") for (int k = 0; k < (n); ++k) { strip(m[k], tg); bad_ |= or4(m[k]); } \
    if (__builtin_expect(__builtin_amdgcn_ballot_w64((bad_ & TM) != 0) != 0 && !(ABL & 127), 0)) { u32 spin_ = 0; do { asm volatile("; retry" ::: "memory"); \
    bool st_[n]; \
    _Pragma("unroll") for (int k = 0; k < (n); ++k) st_[k] = (or4(m[k]) & TM) != 0; \
    _Pragma("unroll") for (int k = 0; k < (n); ++k) if (st_[k]) m[k] = ldsc(rws, (addr), 0); \
    PIN(); bad_ = 0; \
    _Pragma("unroll") for (int k = 0; k < (n); ++k) { if (st_[k]) strip(m[k], tg); bad_ |= or4(m[k]); } \
    } while (__builtin_amdgcn_ballot_w64((bad_ & TM) != 0) != 0 && ++spin_ < SPIN_CAP); TS_SET(21, tsv[21] + spin_ + 1); } } while (0)

__global__ void __launch_bounds__(NT, 4) fused_kernel(Params p) {
  __shared__ __attribute__((aligned(16))) char lds[L_END];
  const int tid = threadIdx.x, lane = tid & 63, wave = __builtin_amdgcn_readfirstlane(tid >> 6);
  const int g = lane >> 4, c = lane & 15, hi = lane >> 5;
  const int wg = blockIdx.x, grp = (wg & 7) * 4 + (wg >> 6), sub = (wg >> 3) & 7;
  const bool ta = wg < 198, th = ta && wg >= 154;
  int cgv[3];
#pragma unroll
  for (int i = 0; i < 3; ++i) {
    const int f = ta ? wg : wg - 198 + 58 * i, fq = f / 7, fr = f - 7 * fq;
    const int n = th ? ((wg - 154) >> 1) * 8 + 3 : fq * 8 + fr + (fr >= 3);
    const int sh = (int)((uintptr_t)(n < 64 ? p.wq : n < 120 ? p.wg : p.wu) >> 7);
    cgv[i] = (th ? i < 2 : f < 154) ? (n & ~7) | ((n - sh) & 7) : -1;
  }
  const int rbase = th ? (wg & 1) * 512 : ta ? 0 : 768;
  const int gw = grp, sO = (sub + 1) & 7, sD = (sub + 3) & 7;
  const int w = wave & 7, wv = wave & 3;
  const rsrc_t rws = mkr(p.ws, WS_END);
  const u32 lcnt = (u32)(uintptr_t)(__attribute__((address_space(3))) char*)lds + L_CNT;
  TS_DECL;
  TS(0);
  u32 tz = (u32)__builtin_amdgcn_s_memrealtime();
  const size_t kvoff = ((size_t)grp * SK + sub * 512 + w * 64) * DH;
  const int rot = (MODE & 2) ? (w + sub) & 3 : 0;
  int kb[4];
#pragma unroll
  for (int i = 0; i < 4; ++i) kb[i] = ((i + rot) & 3) * 4096;
  if (wave < 8) {
    PRIO(3);
    const u32 ep = __builtin_amdgcn_raw_buffer_load_b32(rws, (w == 0 && !(ABL & 2048)) ? 0 : OOB, 0, AUX_SC1);
    u32 xp = 0;
    if (!(ABL & 4096)) xp = *(const u32*)(p.x + w * DM + lane * 16);
    PIN();
    u32x4 wqr[4], gr[8], vr[16];
    const int r0 = rbase + w * 32, r1 = ta ? (rbase + 256 + w * 64) & 1023 : 768 + wv * 64;
    {
      const int cg = cgv[0], st = cg < 64 ? HQD * 2 : FF * 2;
      const rsrc_t r = mkr((const char*)(cg < 64 ? p.wq : cg < 120 ? p.wg : p.wu) + r0 * st + (cg - (cg < 64 ? 0 : cg < 120 ? 64 : 120)) * 128, 31 * st + 128);
      const int vo = (16 * (c >> 3) + 4 * g) * st + (c & 7) * 16;
#pragma unroll
      for (int j = 0; j < 4; ++j) wqr[j] = ldnt(r, vo, j * st);
    }
    if (tid < 32) ((u32*)(lds + L_CNT))[tid] = tid >= L_TZ0 ? tz : 0;
    BAR();
    GETTZ();
    {
      const int cg = w < 4 ? cgv[1] : cgv[2], st = cg < 64 ? HQD * 2 : FF * 2;
      const rsrc_t r = mkr((const char*)(cg < 64 ? p.wq : cg < 120 ? p.wg : p.wu) + r1 * st + (cg - (cg < 64 ? 0 : cg < 120 ? 64 : 120)) * 128, cg < 0 ? 0 : 63 * st + 128);
      const int vo = (32 * (c >> 3) + 8 * g) * st + (c & 7) * 16;
#pragma unroll
      for (int j = 0; j < 8; ++j) { PACET(TW0 + (((j * 7 + w) * SW16) >> 4)); gr[j] = ldnt(r, vo, j * st); }
    }
    PIN();
    TS(1);
    USE4(xp);
    if (w == 0) ((u32*)(lds + L_CNT))[L_EP] = ep;
    u32x2 fq[8];
#pragma unroll
    for (int i = 0; i < 8; ++i)
#pragma unroll
      for (int jj = 0; jj < 2; ++jj) {
        const u32 lo = wqr[2 * jj][i >> 1], hh = wqr[2 * jj + 1][i >> 1];
        fq[i][jj] = (i & 1) ? perm_hi(lo, hh) : perm_lo(lo, hh);
      }
    PINV(fq, 8);
    LWAIT(C_XN, 8);
    TS(2);
    {
      const u32x2 a0 = *(const u32x2*)(lds + L_XN + ((c & 7) * XN_STR + r0 + 16 * (c >> 3) + 4 * g) * 2);
      const u32x4 a1 = *(const u32x4*)(lds + L_XN + ((c & 7) * XN_STR + r1 + 32 * (c >> 3) + 8 * g) * 2);
      f32x4 d[8];
#pragma unroll
      for (int i = 0; i < 8; ++i) d[i] = mfma16(a0, fq[i], f32x4{0, 0, 0, 0});
      if (!ta) {
        store64<RG_STR>(d, (float*)(lds + L_RG) + (8 + w) * 8 * RG_STR, g, c);
#pragma unroll
        for (int i = 0; i < 8; ++i) d[i] = f32x4{0, 0, 0, 0};
      }
      u32x4 f[8];
#pragma unroll
      for (int jj = 0; jj < 4; ++jj) tr_pair(gr, f, jj);
#pragma unroll
      for (int i = 0; i < 8; ++i) d[i] = mfma32(a1, f[i], d[i]);
      store64<RG_STR>(d, (float*)(lds + L_RG) + w * 8 * RG_STR, g, c);
    }
    ARRIVE(C_RG);
    TS(3);
    LWAIT(C_RG, 8);
    MKTAGS();
    if (w == 7) {
      const u32 t20 = tz << 12;
      __builtin_amdgcn_raw_buffer_store_b128(u32x4{t20 | tg.t[0], t20 | tg.t[1], t20 | tg.t[2], t20 | tg.t[3]}, rws,
                                             (lane == 0 && !(wg & 24) && !(MODE & 16)) ? WS_T + ((wg >> 5) * 8 + (wg & 7)) * 16 : OOB, 0, AUX_SC1);
    }
    if (w < (ta ? 2 : 6)) {
      const int t = w >> 1, cg = t == 0 ? cgv[0] : t == 1 ? cgv[1] : cgv[2], l = (w & 1) * 64 + lane;
      const bool isq = cg < 64;
      const int b = isq ? l >> 4 : l & 7, cq = isq ? l & 15 : (l >> 5) * 4 + ((l >> 3) & 1) * 2 + ((l >> 4) & 1);
      const float* rg = (const float*)(lds + L_RG) + ((t == 0 ? (ta ? 0 : 8) : 4 * t - 4) * 8 + b) * RG_STR + cq * 4;
      f32x4 s = *(const f32x4*)rg;
#pragma unroll
      for (int k = 1; k < 4; ++k) s += *(const f32x4*)(rg + k * 8 * RG_STR);
      if (t == 0) {
#pragma unroll
        for (int k = 4; k < 8; ++k) s += *(const f32x4*)(rg + k * 8 * RG_STR);
      }
      const int slot = rbase != 0, gu = cg >= 120;
      const int aq = WS_Q + (b * 32 + (cg >> 1)) * 2048 + win(slot * 512 + (cg & 1) * 256 + cq * 16);
      const int ag = WS_GU + ((cg - (gu ? 120 : 64)) * 4 + (l >> 5)) * 4096 + slot * 2048 + win(((l >> 3) & 3) * 256 + gu * 128 + b * 16);
      publish(rws, (cg < 0 || (ABL & 1)) ? OOB : isq ? aq : ag, s, isq ? qtags(e_) : tg, isq ? 15u : TM);
    }
    TS(4);
    GETTZ();
    PRIO(0);
    const rsrc_t rv = mkr(p.vc + kvoff, 64 * DH * 2);
#pragma unroll
    for (int n = 0; n < 16; ++n) { if (n >= 3 && n <= 9) GETTZ(); PACE(PV + n * 8 + w + (n >= 8 ? (n - 7) * X20 : 0)); vr[n] = ldnt(rv, lane * 16 + (n & 3) * 1024, kb[n >> 2]); }
    ARRIVE(C_VI);
    TS(6);
    PRIO(3);
    u32x4 vt[8];
#pragma unroll
    for (int jj = 0; jj < 4; ++jj) tr_pair(vr, vt, jj);
    PINV(vt, 8);
    LWAIT(C_P + w, 1);
    TS(7);
    f32x4 acc[8];
    {
      const u32x4 pf = *(const u32x4*)(lds + L_P + w * 1024 + lane * 16);
#pragma unroll
      for (int i = 0; i < 8; ++i) acc[i] = mfma32(pf, vt[i], f32x4{0, 0, 0, 0});
    }
    PIN();
    TS(8);
    const u32x4 pf1 = *(const u32x4*)(lds + L_P + w * 1024 + (lane ^ 8) * 16);
#pragma unroll
    for (int jj = 0; jj < 4; ++jj) { tr_pair(vr + 8, vt, jj); PIN(); }
#pragma unroll
    for (int i = 0; i < 8; ++i) acc[i] = mfma32(pf1, vt[i], acc[i]);
    store_acc(acc, lds + L_RO + w * R_WS, g, c);
    ARRIVE(C_RO);
    TS(9);
    LWAIT(C_F, 1);
    LWAIT(C_RO, 8);
    TS(10);
    {
      const int g2 = w >> 2, i = lane & 7, cl = (lane >> 3) & 3;
      const char* ro = lds + L_RO + hi * 4 * R_WS + g2 * R_GS + i * R_IS + (wv * 4 + cl) * 16;
      const char* fp = lds + L_F + hi * 128 + g2 * 16;
      f32x4 o = *(const f32x4*)ro * *(const f32x4*)fp;
#pragma unroll
      for (int k = 1; k < 4; ++k) o += *(const f32x4*)(ro + k * R_WS) * *(const f32x4*)(fp + k * 32);
#pragma unroll
      for (int r = 0; r < 4; ++r) o[r] = add32(o[r]);
      publish(rws, (hi || (ABL & 16)) ? OOB : WS_O + (((grp & 3) * 2 + g2) * 4 + wv) * 65536 + (grp >> 2) * 8192 + (sub >> 1) * 2048 + win((sub & 1) * 512 + lane * 16), o, tg);
    }
    ARRIVE(C_M4);
    TS(11);
    u32x4 sm[1];
    const bool sv = ((lane >> 2) & 1) == ((gw >> 2) & 1);
    const int stoff = sv ? WS_ST + (w * 4 + (gw >> 3)) * 2048 + win(lane * 16) : OOB;
    sm[0] = ldsc(rws, stoff, 0);
    LWAIT(C_M4, 8);
    GETT((MODE & 32) ? L_TZ1 : L_TZ0);
    PACE(P4);
    if (MODE & 128) LWAITF(C_DI, 4);
    u32x4 om[4], bw[4];
    const int ooff = (ABL & 32) ? OOB - 16384 : WS_O + gw * 65536 + w * 8192 + win(lane * 16);
#pragma unroll
    for (int k = 0; k < 4; ++k) om[k] = ldsc(rws, ooff, k * 2048);
    TS(12);
    if (!sv) sm[0] = u32x4{tg.t[0], tg.t[1], tg.t[2], tg.t[3]};
    RETRY(sm, 1, stoff);
    f32x4 fs[4];
    {
      float M, L;
      merge_stats(asf(sm[0][0]), asf(sm[0][1]) + asf(sm[0][2]), M, L);
      *(float*)(lds + L_F2 + w * 256 + lane * 4) = __builtin_amdgcn_exp2f((asf(sm[0][0]) - M) * L2E) * rcp(L);
      PIN();
#pragma unroll
      for (int k = 0; k < 4; ++k) fs[k] = *(const f32x4*)(lds + L_F2 + w * 256 + (2 * k + hi) * 32 + ((gw >> 2) & 1) * 16);
    }
    LWAIT(C_OW, 4);
#pragma unroll
    for (int k = 0; k < 4; ++k) bw[k] = *(const u32x4*)(lds + L_OW + k * 8192 + w * 1024 + lane * 16);
    PINV(bw, 4);
    PINV(fs, 4);
    TS(13);
    RETRY(om, 4, ooff + k * 2048);
    TS(14);
    {
      float v[4];
#pragma unroll
      for (int i = 0; i < 4; ++i) v[i] = (asf(om[0][i]) * fs[0][i] + asf(om[1][i]) * fs[1][i]) + (asf(om[2][i]) * fs[2][i] + asf(om[3][i]) * fs[3][i]);
      swap32(v[0], v[2]); swap32(v[1], v[3]);
      *(u32*)(lds + L_A + (w * A_STR + (lane & 31) * 4 + 2 * hi) * 2) = pack_bf(v[0] + v[2], v[1] + v[3]);
    }
    ARRIVE(C_A);
    LWAITF(C_A, 8);
    TS(15);
    {
      u32x4 a[4];
#pragma unroll
      for (int k = 0; k < 4; ++k) a[k] = *(const u32x4*)(lds + L_A + ((c & 7) * A_STR + 32 * k + 8 * g) * 2);
      f32x4 d0 = mfma32(a[0], bw[0], f32x4{0, 0, 0, 0}), d1 = mfma32(a[1], bw[1], f32x4{0, 0, 0, 0});
      d0 = mfma32(a[2], bw[2], d0); d1 = mfma32(a[3], bw[3], d1);
      publish(rws, hi ? OOB : WS_FS + (((2 * sO + (c >> 3)) * 2 + g) * 8 + w) * 4096 + gw * 128 + (c & 7) * 16, d0 + d1, tg);
    }
    ARRIVE(C_S5);
    TS(16);
  } else {
    PRIO(3);
    const u32x4 xa = *(const u32x4*)(p.x + w * DM + lane * 8);
    const u32x4 xb = *(const u32x4*)(p.x + w * DM + 512 + lane * 8);
    const int oidx = (4 * ((wg >> 3) & 1) + g) * DM + (wg >> 4) * 64 + (c & 7) * 8 + (wg & 7);
    const u32 xfin = p.x[oidx];
    u32 tp = 0;
    if ((ABL & 128) && w == ((ABL >> 8) & 7)) {
      const char* a = (const char*)p.kc + kvoff * 2;
      if (lane == 1) a = (const char*)p.vc + kvoff * 2;
      if (lane == 2) a = (const char*)(p.wo + (size_t)((gw >> 2) * 512 + (gw & 3) * 32) * DM + sO * 128);
      if (lane == 3) a = (const char*)(p.wo + (size_t)((gw >> 2) * 512 + (gw & 3) * 32 + 384) * DM + sO * 128);
      if (lane == 4) a = (const char*)(p.wd + (size_t)(gw * 112) * DM + sD * 128);
      if (lane == 5) a = (const char*)(p.wd + (size_t)(gw * 112 + 111) * DM + sD * 128);
      if (lane == 6) a = (const char*)p.out;
      if (lane >= 8 && lane < 8 + 6) a = p.ws + (lane - 8) * 1048576 + 4096 + wg * 2048 + 1536;
      if ((PQ >> lane) & 1) tp = __builtin_nontemporal_load((const u32*)a);
    }
    asm volatile("s_barrier" ::: "memory");
    GETTZ();
    {
      float v[16];
#pragma unroll
      for (int i = 0; i < 4; ++i) { v[2 * i] = bf_lo(xa[i]); v[2 * i + 1] = bf_hi(xa[i]); v[8 + 2 * i] = bf_lo(xb[i]); v[9 + 2 * i] = bf_hi(xb[i]); }
      float s = 0.f;
#pragma unroll
      for (int i = 0; i < 16; ++i) s += v[i];
      const float mean = wave_sum(s) * (1.f / DM);
      float q = 0.f;
#pragma unroll
      for (int i = 0; i < 16; ++i) { const float d = v[i] - mean; q += d * d; }
      const float var = rbf(wave_sum(q) * (1.f / DM));
      const float mb = rbf(mean);
      const float rs = rcp(rbf(__builtin_amdgcn_sqrtf(rbf(var + 1e-5f))));
      u32x4 o[2];
#pragma unroll
      for (int i = 0; i < 8; ++i) {
        const u32 d = pack_bf(v[2 * i] - mb, v[2 * i + 1] - mb);
        o[i >> 2][i & 3] = pack_bf(bf_lo(d) * rs, bf_hi(d) * rs);
      }
      *(u32x4*)(lds + L_XN + (w * XN_STR + lane * 8) * 2) = o[0];
      *(u32x4*)(lds + L_XN + (w * XN_STR + 512 + lane * 8) * 2) = o[1];
    }
    ARRIVE(C_XN);
    TS(17);
    PRIO(2);
    const rsrc_t rk = mkr(p.kc + kvoff, 64 * DH * 2);
    const int kvo = (4 * (c & 3) + ((c >> 2) & 1)) * 256 + (4 * (c >> 3) + g) * 16;
    u32x4 kr[16];
#pragma unroll
    for (int n = 0; n < 16; ++n) { if (!(n & 1)) PACET(TK0 + (((n * 8 + w * 2) * SK16) >> 4) + (n * 8 + w * 2 >= X19 ? X18 : 0)); kr[n] = ldnt(rk, kvo + ((n >> 1) & 1) * 512 + (n & 1) * 128, kb[n >> 2]); }
    PIN();
    TS(1);
    USE4(tp);
    PRIO(3);
    u32x4 qm[1];
    const int qoff = (ABL & 2) ? OOB : WS_Q + ((grp >> 2) * 32 + (grp & 3) * 8 + w) * 2048 + win(lane * 16);
    qm[0] = ldsc(rws, qoff, 0);
    u32x4 tm = ldsc(rws, (w == 7 && !(MODE & 16)) ? WS_T + lane * 16 : OOB, 0);
    PIN();
    LWAIT(C_RG, 8);
    MKTAGS();
    {
      const Tags tq = qtags(e_);
      strip(qm[0], tq);
      for (u32 spin = 0; __builtin_expect(__builtin_amdgcn_ballot_w64((or4(qm[0]) & 15u) != 0) != 0 && !(ABL & 127), 0) && spin < SPIN_CAP; ++spin) {
        qm[0] = ldsc(rws, qoff, 0);
        strip(qm[0], tq);
      }
    }
    TS(2);
    {
      float q0 = asf(qm[0][0]), q1 = asf(qm[0][1]), q2 = asf(qm[0][2]), q3 = asf(qm[0][3]);
      swap32(q0, q2); swap32(q1, q3);
      *(u32*)(lds + L_QS + (w * A_STR + (lane & 31) * 4 + 2 * hi) * 2) = pack_bf(q0 + q2, q1 + q3);
    }
    if (w == 7) {
      strip(tm, tg);
      int d = (int)(tm[0] - (tz << 12)) >> 12;
      const bool ok = (or4(tm) & 0xfffu) == 0 && d >= -150 && d <= 150 && (MODE & 4);
      if (!ok) d = 0;
      if (MODE & 64) {
        const int n = __builtin_popcountll(__builtin_amdgcn_ballot_w64(ok));
        const u32 tc = tz + (int)(wave_sum((float)d) * rcp((float)(n + !n)));
        ((u32*)(lds + L_CNT))[L_TZ0 + (lane & 1)] = tc;
      } else {
        ((u32*)(lds + L_CNT))[L_TZ0] = tz + (int)wave_max((float)d);
      }
    }
    ARRIVE(C_QS);
    LWAIT(C_QS, 8);
    TS(3);
    {
      const u32x4 q0 = *(const u32x4*)(lds + L_QS + ((c & 7) * A_STR + 32 * (c >> 3) + 8 * g) * 2);
      const u32x4 q1 = *(const u32x4*)(lds + L_QS + ((c & 7) * A_STR + 64 + 32 * (c >> 3) + 8 * g) * 2);
      float sc[8];
#pragma unroll
      for (int e = 0; e < 2; ++e) {
        f32x4 t[4];
#pragma unroll
        for (int ua = 0; ua < 4; ++ua) t[ua] = mfma32(kr[(ua >> 1) * 8 + e * 4 + (ua & 1) * 2], q0, f32x4{0, 0, 0, 0});
#pragma unroll
        for (int ua = 0; ua < 4; ++ua) t[ua] = mfma32(kr[(ua >> 1) * 8 + e * 4 + (ua & 1) * 2 + 1], q1, t[ua]);
#pragma unroll
        for (int r = 0; r < 4; ++r) {
          float a = t[0][r], b = t[1][r], a2 = t[2][r], b2 = t[3][r];
          swap32(a, b); swap32(a2, b2);
          sc[e * 4 + r] = ror8_merge<0x3>(b2, b) + ror8_merge<0xc>(a, a2);
        }
      }
      float mx = -INFINITY;
#pragma unroll
      for (int i = 0; i < 4; ++i) {
        const u32 s1 = pack_bf(sc[2 * i], sc[2 * i + 1]);
        const u32 s2 = pack_bf(bf_lo(s1) * 0.08838834764831845f, bf_hi(s1) * 0.08838834764831845f);
        sc[2 * i] = bf_lo(s2); sc[2 * i + 1] = bf_hi(s2);
        mx = fmaxf(mx, fmaxf(sc[2 * i], sc[2 * i + 1]));
      }
      mx = fmaxf(mx, dpp<DPP_ROR8>(mx));
      const float mw = max32(max16(mx));
      float l = 0.f;
      u32x4 pf;
#pragma unroll
      for (int i = 0; i < 4; ++i) {
        const float e0 = __builtin_amdgcn_exp2f((sc[2 * i] - mw) * L2E), e1 = __builtin_amdgcn_exp2f((sc[2 * i + 1] - mw) * L2E);
        l += e0 + e1;
        pf[i] = pack_bf(e0, e1);
      }
      *(u32x4*)(lds + L_P + w * 1024 + lane * 16) = pf;
      ARRIVE(C_P + w);
      l += dpp<DPP_ROR8>(l);
      l = add32(add16(l));
      if (lane < 8) *(float2*)(lds + L_ST + (w * 8 + lane) * 8) = make_float2(mw, l);
    }
    ARRIVE(C_ST);
    TS(4);
    u32x4 wr[8], wt[8];
    if (w >= 4) {
      if (MODE & 8) LWAIT(C_VI, 8);
      PACE(PG3 + wv);
      TS(7);
      u32x4 gm[4];
      const int gbase = (ABL & 8) ? OOB - 16384 : WS_GU + (gw * 7 + 2 * wv + (wv == 3 ? 0 : hi)) * 4096;
      const int goff0 = gbase + win((g & 1) * 256 + c * 16), goff1 = gbase + 640 + (g & 1) * 256 + c * 16;
#pragma unroll
      for (int k = 0; k < 4; ++k) gm[k] = ldsc(rws, (k & 1) ? goff1 : goff0, (k >> 1) * 2048);
      RETRY(gm, 4, ((k & 1) ? goff1 : goff0) + (k >> 1) * 2048);
      TS(8);
      u32x4 a;
#pragma unroll
      for (int i = 0; i < 4; ++i) {
        const int qp = i >> 1, e = (i & 1) * 2;
        const float v0 = asf(gm[qp][e]) + asf(gm[2 + qp][e]), v1 = asf(gm[qp][e + 1]) + asf(gm[2 + qp][e + 1]);
        const u32 gg = pack_bf(v0, v1);
        const u32 uu = asu(dpp<DPP_ROR8>(asf(gg)));
        const u32 sg = pack_bf(sigm(bf_lo(gg)), sigm(bf_hi(gg)));
        a[i] = pack_bf(bf_lo(sg) * bf_lo(uu), bf_hi(sg) * bf_hi(uu));
        if (wv == 3 && hi) a[i] = 0;
      }
      PINV(a, 4);
      TS(9);
      GETTZ();
      PRIO(0);
      const rsrc_t rw = mkr(p.wd + (size_t)(gw * 112 + 32 * wv) * DM + sD * 128, (wv == 3 ? 16 : 32) * DM * 2 - sD * 256);
#pragma unroll
      for (int j = 0; j < 8; ++j) { PACE(PD + j * SD / 2 + wv); wr[j] = ldnt(rw, 8 * g * (DM * 2) + c * 16, j * DM * 2); }
      ARRIVE(C_DI);
      PRIO(3);
      TS(10);
#pragma unroll
      for (int jj = 0; jj < 4; ++jj) { tr_pair(wr, wt, jj); PIN(); }
      f32x4 d[8];
#pragma unroll
      for (int i = 0; i < 8; ++i) d[i] = mfma32(a, wt[i], f32x4{0, 0, 0, 0});
      store_acc(d, lds + L_RY + wv * R_WS, g, c);
      ARRIVE(C_Y);
      TS(11);
      LWAITF(C_Y, 4);
      {
        const int t = wv * 64 + lane;
        const char* rf = lds + L_RY + (t >> 7) * R_GS + ((t >> 3) & 7) * R_IS + (((t >> 6) & 1) * 8 + (t & 7)) * 16;
        f32x4 s = *(const f32x4*)rf;
#pragma unroll
        for (int k = 1; k < 4; ++k) s += *(const f32x4*)(rf + k * R_WS);
        publish(rws, WS_FY + (((2 * sD + ((t >> 6) & 1)) * 2 + (t >> 7)) * 8 + ((t >> 3) & 7)) * 4096 + gw * 128 + (t & 7) * 16, s, tg);
      }
      ARRIVE(C_S5);
      TS(12);
    } else {
      if (w == 0) {
        LWAIT(C_ST, 8);
        const float2 ml = *(const float2*)(lds + L_ST + lane * 8);
        float M, L;
        merge_stats(ml.x, ml.y, M, L);
        const float Lh = asf((asu(L) + 0x800u) & 0xfffff000u);
        publish(rws, lane < 8 ? WS_ST + grp * 2048 + win(sub * 128) + lane * 16 : OOB, f32x4{M, Lh, L - Lh, 0.f}, tg);
        *(float*)(lds + L_F + lane * 4) = __builtin_amdgcn_exp2f((ml.x - M) * L2E);
        ARRIVE(C_F);
        TS(6);
      }
      GETTZ();
      TS(7);
      PRIO(0);
      const rsrc_t rw = mkr(p.wo + (size_t)((gw >> 2) * 512 + (gw & 3) * 32 + 8 * wv) * DM + sO * 128, 400 * DM * 2);
#pragma unroll
      for (int j = 0; j < 8; ++j) { if (j == (MODE >> 8)) LWAIT(C_VI, 8); PACE(j < (MODE >> 8) ? PS + j * PT + wv : PO + (j - (MODE >> 8)) * SO + wv); wr[j] = ldnt(rw, 2 * g * (DM * 2) + c * 16, ((j & 3) * 128 + (j >> 2)) * DM * 2); }
      PIN();
      PRIO(3);
      TS(8);
#pragma unroll
      for (int jj = 0; jj < 4; ++jj) { tr_pair(wr, wt, jj); PIN(); }
#pragma unroll
      for (int i = 0; i < 8; ++i) *(u32x4*)(lds + L_OW + wv * 8192 + i * 1024 + lane * 16) = wt[i];
      ARRIVE(C_OW);
      TS(10);
      if (w == 0) {
      GETT(L_TZ0);
      LWAIT(C_S5, 12);
      PACE(PF);
      TS(12);
      u32x4 fm[8];
      const int foff = WS_FS + wg * 4096 + lane * 16;
#pragma unroll
      for (int k = 0; k < 8; ++k) fm[k] = ldsc(rws, foff, (k >> 2) * (WS_FY - WS_FS) + (k & 3) * 1024);
      RETRY(fm, 8, foff + (k >> 2) * (WS_FY - WS_FS) + (k & 3) * 1024);
      TS(14);
      float z[2];
#pragma unroll
      for (int h = 0; h < 2; ++h) {
        float v[4];
#pragma unroll
        for (int i = 0; i < 4; ++i) v[i] = (asf(fm[4 * h][i]) + asf(fm[4 * h + 1][i])) + (asf(fm[4 * h + 2][i]) + asf(fm[4 * h + 3][i]));
        swap32(v[0], v[2]); swap32(v[1], v[3]);
        v[0] += v[2]; v[1] += v[3];
        swap16(v[0], v[1]);
        const float t = v[0] + v[1];
        z[h] = rbf(t + dpp<DPP_ROR8>(t));
      }
      z[1] = rbf(z[1] * sigm(z[1]));
      const float r = bf_lo(xfin) + rbf(z[1] * z[0]);
      if (c < 8) p.out[oidx] = (u16)pack_bf(r, r);
      if (wg == 0 && lane == 0) __builtin_amdgcn_raw_buffer_store_b32(e_, rws, 0, 0, AUX_SC1);
      TS(15);
    }
    }
  }
  TS_FLUSH();
}

extern "C" void kernel_launch(void* const* d_in, const int* in_sizes, int n_in, void* d_out, int out_size, void* d_ws, size_t ws_size,
                              hipStream_t stream) {
  Params p;
  p.x = (const u16*)d_in[0]; p.kc = (const u16*)d_in[1]; p.vc = (const u16*)d_in[2]; p.wq = (const u16*)d_in[3]; p.wo = (const u16*)d_in[4];
  p.wg = (const u16*)d_in[6]; p.wu = (const u16*)d_in[7]; p.wd = (const u16*)d_in[8];
  p.out = (u16*)d_out; p.ws = (char*)d_ws;
  fused_kernel<<<NWG, NT, 0, stream>>>(p);
}
```

```cpp
#include <hip/hip_runtime.h>
#include <hip/hip_bf16.h>
#include <stdint.h>

typedef unsigned int u32;
typedef unsigned short u16;
typedef u32 u32x4 __attribute__((ext_vector_type(4)));
typedef u32 u32x2 __attribute__((ext_vector_type(2)));
typedef float f32x4 __attribute__((ext_vector_type(4)));
typedef __bf16 bf16x8 __attribute__((ext_vector_type(8)));
typedef short s16x4 __attribute__((ext_vector_type(4)));
typedef __amdgpu_buffer_rsrc_t rsrc_t;

constexpr int DM = 1024, HQD = 4096, SK = 4096, DH = 128, FF = 3584;
constexpr int NWG = 256, NT = 1024;

constexpr int WS_T = 1024;
constexpr int WS_Q = 4096;
constexpr int WS_GU = WS_Q + 256 * 2048;
constexpr int WS_O = WS_GU + 224 * 4096;
constexpr int WS_ST = WS_O + 32 * 65536;
constexpr int WS_FS = WS_ST + 32 * 2048;
constexpr int WS_FY = WS_FS + 256 * 4096;
constexpr int WS_END = WS_FY + 256 * 4096;
constexpr int AUX_NT = 2, AUX_SC1 = 16;
constexpr u32 TM = 0xfffu;
constexpr u32 SPIN_CAP = 20000;

constexpr int XN_STR = 1032;
constexpr int A_STR = 136;
constexpr int RG_STR = 68;
constexpr int R_IS = 288, R_GS = 8 * R_IS, R_WS = 2 * R_GS;
constexpr int L_CNT = 0;
constexpr int L_XN = 128;
constexpr int L_QS = L_XN + 8 * XN_STR * 2;
constexpr int L_ST = L_QS + 8 * A_STR * 2;
constexpr int L_F = L_ST + 512;
constexpr int L_F2 = L_F + 256;
constexpr int L_P = L_F2 + 2048;
constexpr int L_RG = L_P + 8192;
constexpr int L_RO = L_RG + 16 * 8 * RG_STR * 4;
constexpr int L_RY = L_RO + 8 * R_WS;
constexpr int L_A = L_RY + 4 * R_WS;
constexpr int L_OW = L_A + 8 * A_STR * 2;
constexpr int L_END = L_OW + 32768;
enum { C_XN = 0, C_RQ, C_QS, C_ST, C_RG, C_VI, C_DI, C_F, C_RO, C_M4, C_Y, C_OW, C_A, C_S5, C_TZ, C_P = 16, L_EP = 29, L_TZ0 = 30, L_TZ1 = 31 };

#define PIN() do { asm volatile("" ::: "memory"); __builtin_amdgcn_sched_barrier(0); } while (0)
#define USE4(x) asm volatile("" ::"v"(x))
#define BAR() do { asm volatile("s_waitcnt lgkmcnt(0)\n\ts_barrier" ::: "memory"); __builtin_amdgcn_sched_barrier(0); } while (0)

__device__ __forceinline__ f32x4 mfma32(u32x4 a, u32x4 b, f32x4 c) {
  return __builtin_amdgcn_mfma_f32_16x16x32_bf16(__builtin_bit_cast(bf16x8, a), __builtin_bit_cast(bf16x8, b), c, 0, 0, 0);
}
__device__ __forceinline__ f32x4 mfma16(u32x2 a, u32x2 b, f32x4 c) {
  return __builtin_amdgcn_mfma_f32_16x16x16bf16_1k(__builtin_bit_cast(s16x4, a), __builtin_bit_cast(s16x4, b), c, 0, 0, 0);
}
__device__ __forceinline__ float asf(u32 x) { return __builtin_bit_cast(float, x); }
__device__ __forceinline__ u32 asu(float x) { return __builtin_bit_cast(u32, x); }
__device__ __forceinline__ float bf_lo(u32 x) { return asf(x << 16); }
__device__ __forceinline__ float bf_hi(u32 x) { return asf(x & 0xffff0000u); }
__device__ __forceinline__ u32 pack_bf(float lo, float hi) {
  typedef __bf16 bf16x2 __attribute__((ext_vector_type(2)));
  typedef float f32x2 __attribute__((ext_vector_type(2)));
  f32x2 v = {lo, hi};
  return __builtin_bit_cast(u32, __builtin_convertvector(v, bf16x2));
}
__device__ __forceinline__ float rcp(float x) { return __builtin_amdgcn_rcpf(x); }
constexpr float L2E = 1.4426950408889634f;
constexpr int OOB = 0x7ffffff0;
__device__ __forceinline__ float sigm(float x) { return rcp(1.f + __builtin_amdgcn_exp2f(-L2E * x)); }
__device__ __forceinline__ float rbf(float x) { return bf_lo(pack_bf(x, x)); }
__device__ __forceinline__ u32 perm_lo(u32 a, u32 b) { return __builtin_amdgcn_perm(b, a, 0x05040100u); }
__device__ __forceinline__ u32 perm_hi(u32 a, u32 b) { return __builtin_amdgcn_perm(b, a, 0x07060302u); }
template <int CTRL> __device__ __forceinline__ float dpp(float x) {
  return asf(__builtin_amdgcn_mov_dpp(asu(x), CTRL, 0xf, 0xf, true));
}
template <int BANK> __device__ __forceinline__ float ror8_merge(float old, float src) {
  return asf(__builtin_amdgcn_update_dpp(asu(old), asu(src), 0x128, 0xf, BANK, false));
}
constexpr int DPP_X1 = 0xB1, DPP_X2 = 0x4E, DPP_HMIR = 0x141, DPP_MIR = 0x140, DPP_ROR8 = 0x128, DPP_ROR4 = 0x124;
__device__ __forceinline__ void swap32(float& a, float& b) {
  auto r = __builtin_amdgcn_permlane32_swap(asu(a), asu(b), false, false);
  a = asf(r[0]); b = asf(r[1]);
}
__device__ __forceinline__ void swap16(float& a, float& b) {
  auto r = __builtin_amdgcn_permlane16_swap(asu(a), asu(b), false, false);
  a = asf(r[0]); b = asf(r[1]);
}
__device__ __forceinline__ float add32(float x) { float a = x, b = x; swap32(a, b); return a + b; }
__device__ __forceinline__ float add16(float x) { float a = x, b = x; swap16(a, b); return a + b; }
__device__ __forceinline__ float max32(float x) { float a = x, b = x; swap32(a, b); return fmaxf(a, b); }
__device__ __forceinline__ float max16(float x) { float a = x, b = x; swap16(a, b); return fmaxf(a, b); }
__device__ __forceinline__ float wave_sum(float x) {
  x += dpp<DPP_X1>(x); x += dpp<DPP_X2>(x); x += dpp<DPP_HMIR>(x); x += dpp<DPP_MIR>(x);
  return add32(add16(x));
}
__device__ __forceinline__ float wave_max(float f) {
  f = fmaxf(f, dpp<DPP_X1>(f)); f = fmaxf(f, dpp<DPP_X2>(f)); f = fmaxf(f, dpp<DPP_HMIR>(f)); f = fmaxf(f, dpp<DPP_MIR>(f));
  return max32(max16(f));
}
__device__ __forceinline__ u32x4 ldnt(rsrc_t r, int voff, int soff) { return __builtin_amdgcn_raw_buffer_load_b128(r, voff, soff, AUX_NT); }
__device__ __forceinline__ u32x4 ldsc(rsrc_t r, int voff, int soff) { return __builtin_amdgcn_raw_buffer_load_b128(r, voff, soff, AUX_SC1); }
__device__ __forceinline__ rsrc_t mkr(const void* p, int bytes) { return __builtin_amdgcn_make_buffer_rsrc((void*)p, 0, bytes, 0x00020000); }

struct Tags { u32 t[4]; };
__device__ __forceinline__ int win(int o) { return o + (o >= 384 ? 128 : 0); }
__device__ __forceinline__ void publish(rsrc_t r, int voff, f32x4 v, const Tags& tg, u32 tm = TM) {
  u32x4 o;
#pragma unroll
  for (int i = 0; i < 4; ++i) o[i] = ((asu(v[i]) + (tm + 1) / 2) & ~tm) | tg.t[i];
  __builtin_amdgcn_raw_buffer_store_b128(o, r, voff, 0, AUX_SC1);
}
__device__ __forceinline__ Tags qtags(u32 e) {
  Tags t;
  t.t[0] = e & 15; t.t[1] = (e + 1 + ((e >> 4) & 7)) & 15; t.t[2] = (e + (e >> 7)) & 15; t.t[3] = (e + (e >> 11)) & 15;
  return t;
}
__device__ __forceinline__ void strip(u32x4& m, const Tags& tg) {
#pragma unroll
  for (int i = 0; i < 4; ++i) m[i] ^= tg.t[i];
}
__device__ __forceinline__ u32 or4(const u32x4& m) { return (m[0] | m[1]) | (m[2] | m[3]); }

struct Params {
  const u16* x; const u16* kc; const u16* vc; const u16* wq; const u16* wo;
  const u16* wg; const u16* wu; const u16* wd;
  u16* out; char* ws;
};
constexpr int P_STEP16 = 59, P_LEAD = 4, PO = 360, SO = 4, PG3 = 352, PD = 396, SD = 7, P4 = 408, PF = 478, MODE = 135, PT = 0, TK0 = 315, SK16 = 59, PQ = 0, PS = 320, TW0 = 100, SW16 = 59, PV = 228, X18 = 22, X19 = 48, X20 = 0, ABL = 0;
#define PACE(pos) PACET((((pos) - P_LEAD) * P_STEP16) >> 4)
#define PACET(t) do { PIN(); const int due_ = (t); while ((int)((u32)__builtin_amdgcn_s_memrealtime() - tz) < due_) __builtin_amdgcn_s_sleep(1); PIN(); } while (0)

template <int STR> __device__ __forceinline__ void store64(f32x4 (&d)[8], float* red, int g, int c) {
  f32x4 s[4];
#pragma unroll
  for (int i = 0; i < 4; ++i)
#pragma unroll
    for (int r = 0; r < 4; ++r) {
      float a = d[i][r], b = d[i + 4][r];
      swap32(a, b);
      s[r][i] = a + dpp<DPP_ROR8>(b);
    }
  if (c < 8) {
#pragma unroll
    for (int r = 0; r < 4; ++r) *(f32x4*)(red + (4 * (g & 1) + r) * STR + 8 * c + 4 * (g >> 1)) = s[r];
  }
}
__device__ __forceinline__ void store_acc(f32x4 (&d)[8], char* red, int g, int c) {
  if (g < 2) {
#pragma unroll
    for (int i = 0; i < 8; ++i) *(f32x4*)(red + g * R_GS + i * R_IS + c * 16) = d[i];
  }
}
__device__ __forceinline__ void tr_pair(const u32x4* w, u32x4 (&f)[8], int jj) {
#pragma unroll
  for (int i = 0; i < 8; ++i) {
    const u32 lo = w[2 * jj][i >> 1], hi = w[2 * jj + 1][i >> 1];
    f[i][jj] = (i & 1) ? perm_hi(lo, hi) : perm_lo(lo, hi);
  }
}
__device__ __forceinline__ void merge_stats(float m, float l, float& M, float& L) {
  M = fmaxf(m, dpp<DPP_ROR8>(m));
  M = max32(max16(M));
  L = l * __builtin_amdgcn_exp2f((m - M) * L2E);
  L += dpp<DPP_ROR8>(L);
  L = add32(add16(L));
}

#define ARRIVE(i) do { PIN(); asm volatile("s_mov_b64 exec, 1\n\tds_add_u32 %0, %1\n\ts_mov_b64 exec, -1" :: "v"(lcnt + 4 * (i)), "v"(1u) : "memory"); PIN(); } while (0)
#define PINV(a, n) do { _Pragma("unroll") for (int i_ = 0; i_ < (n); ++i_) asm volatile("" : "+v"((a)[i_])); } while (0)
#define LWAITS(i, n, SL) do { PIN(); u32 it_ = 0; \
    while (__builtin_amdgcn_readfirstlane(__hip_atomic_load((u32*)(lds + L_CNT) + (i), __ATOMIC_RELAXED, __HIP_MEMORY_SCOPE_WORKGROUP)) < (u32)(n) && ++it_ < (1u << 22)) { SL; } \
    PIN(); } while (0)
#define LWAIT(i, n) LWAITS(i, n, __builtin_amdgcn_s_sleep(1))
#define LWAITF(i, n) LWAITS(i, n, )
#define GETTZ() GETT(L_TZ1)
#define GETT(i) do { PIN(); tz = __builtin_amdgcn_readfirstlane(__hip_atomic_load((u32*)(lds + L_CNT) + (i), __ATOMIC_RELAXED, __HIP_MEMORY_SCOPE_WORKGROUP)); PIN(); } while (0)
#define PRIO(n) do { __builtin_amdgcn_s_setprio(n); PIN(); } while (0)
#define MKTAGS() Tags tg; const u32 e_ = __builtin_amdgcn_readfirstlane(__hip_atomic_load((u32*)(lds + L_CNT) + L_EP, __ATOMIC_RELAXED, __HIP_MEMORY_SCOPE_WORKGROUP)) + 1u; { _Pragma("unroll") for (int i_ = 0; i_ < 4; ++i_) tg.t[i_] = (e_ + 0x4d5u * i_) & TM; }
#define RETRY(m, n, addr) do { u32 bad_ = 0; \
    _Pragma("unroll") for (int k = 0; k < (n); ++k) { strip(m[k], tg); bad_ |= or4(m[k]); } \
    if (__builtin_expect(__builtin_amdgcn_ballot_w64((bad_ & TM) != 0) != 0, 0)) { u32 spin_ = 0; do { asm volatile("; retry" ::: "memory"); \
    bool st_[n]; \
    _Pragma("unroll") for (int k = 0; k < (n); ++k) st_[k] = (or4(m[k]) & TM) != 0; \
    _Pragma("unroll") for (int k = 0; k < (n); ++k) if (st_[k]) m[k] = ldsc(rws, (addr), 0); \
    PIN(); bad_ = 0; \
    _Pragma("unroll") for (int k = 0; k < (n); ++k) { if (st_[k]) strip(m[k], tg); bad_ |= or4(m[k]); } \
    } while (__builtin_amdgcn_ballot_w64((bad_ & TM) != 0) != 0 && ++spin_ < SPIN_CAP); } } while (0)

__global__ void __launch_bounds__(NT, 4) fused_kernel(Params p) {
  __shared__ __attribute__((aligned(16))) char lds[L_END];
  const int tid = threadIdx.x, lane = tid & 63, wave = __builtin_amdgcn_readfirstlane(tid >> 6);
  const int g = lane >> 4, c = lane & 15, hi = lane >> 5;
  const int wg = blockIdx.x, grp = (wg & 7) * 4 + (wg >> 6), sub = (wg >> 3) & 7;
  const bool ta = wg < 198, th = ta && wg >= 154;
  int cgv[3];
#pragma unroll
  for (int i = 0; i < 3; ++i) {
    const int f = ta ? wg : wg - 198 + 58 * i, fq = f / 7, fr = f - 7 * fq;
    const int n = th ? ((wg - 154) >> 1) * 8 + 3 : fq * 8 + fr + (fr >= 3);
    const int sh = (int)((uintptr_t)(n < 64 ? p.wq : n < 120 ? p.wg : p.wu) >> 7);
    cgv[i] = (th ? i < 2 : f < 154) ? (n & ~7) | ((n - sh) & 7) : -1;
  }
  const int rbase = th ? (wg & 1) * 512 : ta ? 0 : 768;
  const int gw = grp, sO = (sub + 1) & 7, sD = (sub + 3) & 7;
  const int w = wave & 7, wv = wave & 3;
  const rsrc_t rws = mkr(p.ws, WS_END);
  const u32 lcnt = (u32)(uintptr_t)(__attribute__((address_space(3))) char*)lds + L_CNT;
  u32 tz = (u32)__builtin_amdgcn_s_memrealtime();
  const size_t kvoff = ((size_t)grp * SK + sub * 512 + w * 64) * DH;
  const int rot = (MODE & 2) ? (w + sub) & 3 : 0;
  int kb[4];
#pragma unroll
  for (int i = 0; i < 4; ++i) kb[i] = ((i + rot) & 3) * 4096;
  if (wave < 8) {
    PRIO(3);
    const u32 ep = __builtin_amdgcn_raw_buffer_load_b32(rws, w == 0 ? 0 : OOB, 0, AUX_SC1);
    const u32 xp = *(const u32*)(p.x + w * DM + lane * 16);
    PIN();
    u32x4 wqr[4], gr[8], vr[16];
    const int r0 = rbase + w * 32, r1 = ta ? (rbase + 256 + w * 64) & 1023 : 768 + wv * 64;
    {
      const int cg = cgv[0], st = cg < 64 ? HQD * 2 : FF * 2;
      const rsrc_t r = mkr((const char*)(cg < 64 ? p.wq : cg < 120 ? p.wg : p.wu) + r0 * st + (cg - (cg < 64 ? 0 : cg < 120 ? 64 : 120)) * 128, 31 * st + 128);
      const int vo = (16 * (c >> 3) + 4 * g) * st + (c & 7) * 16;
#pragma unroll
      for (int j = 0; j < 4; ++j) wqr[j] = ldnt(r, vo, j * st);
    }
    if (tid < 32) ((u32*)(lds + L_CNT))[tid] = tid >= L_TZ0 ? tz : 0;
    BAR();
    GETTZ();
    {
      const int cg = w < 4 ? cgv[1] : cgv[2], st = cg < 64 ? HQD * 2 : FF * 2;
      const rsrc_t r = mkr((const char*)(cg < 64 ? p.wq : cg < 120 ? p.wg : p.wu) + r1 * st + (cg - (cg < 64 ? 0 : cg < 120 ? 64 : 120)) * 128, cg < 0 ? 0 : 63 * st + 128);
      const int vo = (32 * (c >> 3) + 8 * g) * st + (c & 7) * 16;
#pragma unroll
      for (int j = 0; j < 8; ++j) { PACET(TW0 + (((j * 7 + w) * SW16) >> 4)); gr[j] = ldnt(r, vo, j * st); }
    }
    PIN();
    USE4(xp);
    if (w == 0) ((u32*)(lds + L_CNT))[L_EP] = ep;
    u32x2 fq[8];
#pragma unroll
    for (int i = 0; i < 8; ++i)
#pragma unroll
      for (int jj = 0; jj < 2; ++jj) {
        const u32 lo = wqr[2 * jj][i >> 1], hh = wqr[2 * jj + 1][i >> 1];
        fq[i][jj] = (i & 1) ? perm_hi(lo, hh) : perm_lo(lo, hh);
      }
    PINV(fq, 8);
    LWAIT(C_XN, 8);
    {
      const u32x2 a0 = *(const u32x2*)(lds + L_XN + ((c & 7) * XN_STR + r0 + 16 * (c >> 3) + 4 * g) * 2);
      const u32x4 a1 = *(const u32x4*)(lds + L_XN + ((c & 7) * XN_STR + r1 + 32 * (c >> 3) + 8 * g) * 2);
      f32x4 d[8];
#pragma unroll
      for (int i = 0; i < 8; ++i) d[i] = mfma16(a0, fq[i], f32x4{0, 0, 0, 0});
      if (!ta) {
        store64<RG_STR>(d, (float*)(lds + L_RG) + (8 + w) * 8 * RG_STR, g, c);
#pragma unroll
        for (int i = 0; i < 8; ++i) d[i] = f32x4{0, 0, 0, 0};
      }
      u32x4 f[8];
#pragma unroll
      for (int jj = 0; jj < 4; ++jj) tr_pair(gr, f, jj);
#pragma unroll
      for (int i = 0; i < 8; ++i) d[i] = mfma32(a1, f[i], d[i]);
      store64<RG_STR>(d, (float*)(lds + L_RG) + w * 8 * RG_STR, g, c);
    }
    ARRIVE(C_RG);
    LWAIT(C_RG, 8);
    MKTAGS();
    if (w == 7) {
      const u32 t20 = tz << 12;
      __builtin_amdgcn_raw_buffer_store_b128(u32x4{t20 | tg.t[0], t20 | tg.t[1], t20 | tg.t[2], t20 | tg.t[3]}, rws,
                                             (lane == 0 && !(wg & 24) && !(MODE & 16)) ? WS_T + ((wg >> 5) * 8 + (wg & 7)) * 16 : OOB, 0, AUX_SC1);
    }
    if (w < (ta ? 2 : 6)) {
      const int t = w >> 1, cg = t == 0 ? cgv[0] : t == 1 ? cgv[1] : cgv[2], l = (w & 1) * 64 + lane;
      const bool isq = cg < 64;
      const int b = isq ? l >> 4 : l & 7, cq = isq ? l & 15 : (l >> 5) * 4 + ((l >> 3) & 1) * 2 + ((l >> 4) & 1);
      const float* rg = (const float*)(lds + L_RG) + ((t == 0 ? (ta ? 0 : 8) : 4 * t - 4) * 8 + b) * RG_STR + cq * 4;
      f32x4 s = *(const f32x4*)rg;
#pragma unroll
      for (int k = 1; k < 4; ++k) s += *(const f32x4*)(rg + k * 8 * RG_STR);
      if (t == 0) {
#pragma unroll
        for (int k = 4; k < 8; ++k) s += *(const f32x4*)(rg + k * 8 * RG_STR);
      }
      const int slot = rbase != 0, gu = cg >= 120;
      const int aq = WS_Q + (b * 32 + (cg >> 1)) * 2048 + win(slot * 512 + (cg & 1) * 256 + cq * 16);
      const int ag = WS_GU + ((cg - (gu ? 120 : 64)) * 4 + (l >> 5)) * 4096 + slot * 2048 + win(((l >> 3) & 3) * 256 + gu * 128 + b * 16);
      publish(rws, cg < 0 ? OOB : isq ? aq : ag, s, isq ? qtags(e_) : tg, isq ? 15u : TM);
    }
    GETTZ();
    PRIO(0);
    const rsrc_t rv = mkr(p.vc + kvoff, 64 * DH * 2);
#pragma unroll
    for (int n = 0; n < 16; ++n) { if (n >= 3 && n <= 9) GETTZ(); PACE(PV + n * 8 + w); vr[n] = ldnt(rv, lane * 16 + (n & 3) * 1024, kb[n >> 2]); }
    ARRIVE(C_VI);
    PRIO(3);
    u32x4 vt[8];
#pragma unroll
    for (int jj = 0; jj < 4; ++jj) tr_pair(vr, vt, jj);
    PINV(vt, 8);
    LWAIT(C_P + w, 1);
    f32x4 acc[8];
    {
      const u32x4 pf = *(const u32x4*)(lds + L_P + w * 1024 + lane * 16);
#pragma unroll
      for (int i = 0; i < 8; ++i) acc[i] = mfma32(pf, vt[i], f32x4{0, 0, 0, 0});
    }
    PIN();
    const u32x4 pf1 = *(const u32x4*)(lds + L_P + w * 1024 + (lane ^ 8) * 16);
#pragma unroll
    for (int jj = 0; jj < 4; ++jj) { tr_pair(vr + 8, vt, jj); PIN(); }
#pragma unroll
    for (int i = 0; i < 8; ++i) acc[i] = mfma32(pf1, vt[i], acc[i]);
    store_acc(acc, lds + L_RO + w * R_WS, g, c);
    ARRIVE(C_RO);
    LWAIT(C_F, 1);
    LWAIT(C_RO, 8);
    {
      const int g2 = w >> 2, i = lane & 7, cl = (lane >> 3) & 3;
      const char* ro = lds + L_RO + hi * 4 * R_WS + g2 * R_GS + i * R_IS + (wv * 4 + cl) * 16;
      const char* fp = lds + L_F + hi * 128 + g2 * 16;
      f32x4 o = *(const f32x4*)ro * *(const f32x4*)fp;
#pragma unroll
      for (int k = 1; k < 4; ++k) o += *(const f32x4*)(ro + k * R_WS) * *(const f32x4*)(fp + k * 32);
#pragma unroll
      for (int r = 0; r < 4; ++r) o[r] = add32(o[r]);
      publish(rws, hi ? OOB : WS_O + (((grp & 3) * 2 + g2) * 4 + wv) * 65536 + (grp >> 2) * 8192 + (sub >> 1) * 2048 + win((sub & 1) * 512 + lane * 16), o, tg);
    }
    ARRIVE(C_M4);
    u32x4 sm[1];
    const bool sv = ((lane >> 2) & 1) == ((gw >> 2) & 1);
    const int stoff = sv ? WS_ST + (w * 4 + (gw >> 3)) * 2048 + win(lane * 16) : OOB;
    sm[0] = ldsc(rws, stoff, 0);
    LWAIT(C_M4, 8);
    GETT((MODE & 32) ? L_TZ1 : L_TZ0);
    PACE(P4);
    if (MODE & 128) LWAITF(C_DI, 4);
    u32x4 om[4], bw[4];
    const int ooff = WS_O + gw * 65536 + w * 8192 + win(lane * 16);
#pragma unroll
    for (int k = 0; k < 4; ++k) om[k] = ldsc(rws, ooff, k * 2048);
    if (!sv) sm[0] = u32x4{tg.t[0], tg.t[1], tg.t[2], tg.t[3]};
    RETRY(sm, 1, stoff);
    f32x4 fs[4];
    {
      float M, L;
      merge_stats(asf(sm[0][0]), asf(sm[0][1]) + asf(sm[0][2]), M, L);
      *(float*)(lds + L_F2 + w * 256 + lane * 4) = __builtin_amdgcn_exp2f((asf(sm[0][0]) - M) * L2E) * rcp(L);
      PIN();
#pragma unroll
      for (int k = 0; k < 4; ++k) fs[k] = *(const f32x4*)(lds + L_F2 + w * 256 + (2 * k + hi) * 32 + ((gw >> 2) & 1) * 16);
    }
    LWAIT(C_OW, 4);
#pragma unroll
    for (int k = 0; k < 4; ++k) bw[k] = *(const u32x4*)(lds + L_OW + k * 8192 + w * 1024 + lane * 16);
    PINV(bw, 4);
    PINV(fs, 4);
    RETRY(om, 4, ooff + k * 2048);
    {
      float v[4];
#pragma unroll
      for (int i = 0; i < 4; ++i) v[i] = (asf(om[0][i]) * fs[0][i] + asf(om[1][i]) * fs[1][i]) + (asf(om[2][i]) * fs[2][i] + asf(om[3][i]) * fs[3][i]);
      swap32(v[0], v[2]); swap32(v[1], v[3]);
      *(u32*)(lds + L_A + (w * A_STR + (lane & 31) * 4 + 2 * hi) * 2) = pack_bf(v[0] + v[2], v[1] + v[3]);
    }
    ARRIVE(C_A);
    LWAITF(C_A, 8);
    {
      u32x4 a[4];
#pragma unroll
      for (int k = 0; k < 4; ++k) a[k] = *(const u32x4*)(lds + L_A + ((c & 7) * A_STR + 32 * k + 8 * g) * 2);
      f32x4 d0 = mfma32(a[0], bw[0], f32x4{0, 0, 0, 0}), d1 = mfma32(a[1], bw[1], f32x4{0, 0, 0, 0});
      d0 = mfma32(a[2], bw[2], d0); d1 = mfma32(a[3], bw[3], d1);
      publish(rws, hi ? OOB : WS_FS + (((2 * sO + (c >> 3)) * 2 + g) * 8 + w) * 4096 + gw * 128 + (c & 7) * 16, d0 + d1, tg);
    }
    ARRIVE(C_S5);
  } else {
    PRIO(3);
    const u32x4 xa = *(const u32x4*)(p.x + w * DM + lane * 8);
    const u32x4 xb = *(const u32x4*)(p.x + w * DM + 512 + lane * 8);
    const int oidx = (4 * ((wg >> 3) & 1) + g) * DM + (wg >> 4) * 64 + (c & 7) * 8 + (wg & 7);
    const u32 xfin = p.x[oidx];
    asm volatile("s_barrier" ::: "memory");
    GETTZ();
    {
      float v[16];
#pragma unroll
      for (int i = 0; i < 4; ++i) { v[2 * i] = bf_lo(xa[i]); v[2 * i + 1] = bf_hi(xa[i]); v[8 + 2 * i] = bf_lo(xb[i]); v[9 + 2 * i] = bf_hi(xb[i]); }
      float s = 0.f;
#pragma unroll
      for (int i = 0; i < 16; ++i) s += v[i];
      const float mean = wave_sum(s) * (1.f / DM);
      float q = 0.f;
#pragma unroll
      for (int i = 0; i < 16; ++i) { const float d = v[i] - mean; q += d * d; }
      const float var = rbf(wave_sum(q) * (1.f / DM));
      const float mb = rbf(mean);
      const float rs = rcp(rbf(__builtin_amdgcn_sqrtf(rbf(var + 1e-5f))));
      u32x4 o[2];
#pragma unroll
      for (int i = 0; i < 8; ++i) {
        const u32 d = pack_bf(v[2 * i] - mb, v[2 * i + 1] - mb);
        o[i >> 2][i & 3] = pack_bf(bf_lo(d) * rs, bf_hi(d) * rs);
      }
      *(u32x4*)(lds + L_XN + (w * XN_STR + lane * 8) * 2) = o[0];
      *(u32x4*)(lds + L_XN + (w * XN_STR + 512 + lane * 8) * 2) = o[1];
    }
    ARRIVE(C_XN);
    PRIO(2);
    const rsrc_t rk = mkr(p.kc + kvoff, 64 * DH * 2);
    const int kvo = (4 * (c & 3) + ((c >> 2) & 1)) * 256 + (4 * (c >> 3) + g) * 16;
    u32x4 kr[16];
#pragma unroll
    for (int n = 0; n < 16; ++n) { if (!(n & 1)) PACET(TK0 + (((n * 8 + w * 2) * SK16) >> 4) + (n * 8 + w * 2 >= X19 ? X18 : 0)); kr[n] = ldnt(rk, kvo + ((n >> 1) & 1) * 512 + (n & 1) * 128, kb[n >> 2]); }
    PIN();
    PRIO(3);
    u32x4 qm[1];
    const int qoff = WS_Q + ((grp >> 2) * 32 + (grp & 3) * 8 + w) * 2048 + win(lane * 16);
    qm[0] = ldsc(rws, qoff, 0);
    u32x4 tm = ldsc(rws, (w == 7 && !(MODE & 16)) ? WS_T + lane * 16 : OOB, 0);
    PIN();
    LWAIT(C_RG, 8);
    MKTAGS();
    {
      const Tags tq = qtags(e_);
      strip(qm[0], tq);
      for (u32 spin = 0; __builtin_expect(__builtin_amdgcn_ballot_w64((or4(qm[0]) & 15u) != 0) != 0, 0) && spin < SPIN_CAP; ++spin) {
        qm[0] = ldsc(rws, qoff, 0);
        strip(qm[0], tq);
      }
    }
    {
      float q0 = asf(qm[0][0]), q1 = asf(qm[0][1]), q2 = asf(qm[0][2]), q3 = asf(qm[0][3]);
      swap32(q0, q2); swap32(q1, q3);
      *(u32*)(lds + L_QS + (w * A_STR + (lane & 31) * 4 + 2 * hi) * 2) = pack_bf(q0 + q2, q1 + q3);
    }
    if (w == 7) {
      strip(tm, tg);
      int d = (int)(tm[0] - (tz << 12)) >> 12;
      const bool ok = (or4(tm) & 0xfffu) == 0 && d >= -150 && d <= 150 && (MODE & 4);
      if (!ok) d = 0;
      if (MODE & 64) {
        const int n = __builtin_popcountll(__builtin_amdgcn_ballot_w64(ok));
        const u32 tc = tz + (int)(wave_sum((float)d) * rcp((float)(n + !n)));
        ((u32*)(lds + L_CNT))[L_TZ0 + (lane & 1)] = tc;
      } else {
        ((u32*)(lds + L_CNT))[L_TZ0] = tz + (int)wave_max((float)d);
      }
    }
    ARRIVE(C_QS);
    LWAIT(C_QS, 8);
    {
      const u32x4 q0 = *(const u32x4*)(lds + L_QS + ((c & 7) * A_STR + 32 * (c >> 3) + 8 * g) * 2);
      const u32x4 q1 = *(const u32x4*)(lds + L_QS + ((c & 7) * A_STR + 64 + 32 * (c >> 3) + 8 * g) * 2);
      float sc[8];
#pragma unroll
      for (int e = 0; e < 2; ++e) {
        f32x4 t[4];
#pragma unroll
        for (int ua = 0; ua < 4; ++ua) t[ua] = mfma32(kr[(ua >> 1) * 8 + e * 4 + (ua & 1) * 2], q0, f32x4{0, 0, 0, 0});
#pragma unroll
        for (int ua = 0; ua < 4; ++ua) t[ua] = mfma32(kr[(ua >> 1) * 8 + e * 4 + (ua & 1) * 2 + 1], q1, t[ua]);
#pragma unroll
        for (int r = 0; r < 4; ++r) {
          float a = t[0][r], b = t[1][r], a2 = t[2][r], b2 = t[3][r];
          swap32(a, b); swap32(a2, b2);
          sc[e * 4 + r] = ror8_merge<0x3>(b2, b) + ror8_merge<0xc>(a, a2);
        }
      }
      float mx = -INFINITY;
#pragma unroll
      for (int i = 0; i < 4; ++i) {
        const u32 s1 = pack_bf(sc[2 * i], sc[2 * i + 1]);
        const u32 s2 = pack_bf(bf_lo(s1) * 0.08838834764831845f, bf_hi(s1) * 0.08838834764831845f);
        sc[2 * i] = bf_lo(s2); sc[2 * i + 1] = bf_hi(s2);
        mx = fmaxf(mx, fmaxf(sc[2 * i], sc[2 * i + 1]));
      }
      mx = fmaxf(mx, dpp<DPP_ROR8>(mx));
      const float mw = max32(max16(mx));
      float l = 0.f;
      u32x4 pf;
#pragma unroll
      for (int i = 0; i < 4; ++i) {
        const float e0 = __builtin_amdgcn_exp2f((sc[2 * i] - mw) * L2E), e1 = __builtin_amdgcn_exp2f((sc[2 * i + 1] - mw) * L2E);
        l += e0 + e1;
        pf[i] = pack_bf(e0, e1);
      }
      *(u32x4*)(lds + L_P + w * 1024 + lane * 16) = pf;
      ARRIVE(C_P + w);
      l += dpp<DPP_ROR8>(l);
      l = add32(add16(l));
      if (lane < 8) *(float2*)(lds + L_ST + (w * 8 + lane) * 8) = make_float2(mw, l);
    }
    ARRIVE(C_ST);
    u32x4 wr[8], wt[8];
    if (w >= 4) {
      if (MODE & 8) LWAIT(C_VI, 8);
      PACE(PG3 + wv);
      u32x4 gm[4];
      const int gbase = WS_GU + (gw * 7 + 2 * wv + (wv == 3 ? 0 : hi)) * 4096;
      const int goff0 = gbase + win((g & 1) * 256 + c * 16), goff1 = gbase + 640 + (g & 1) * 256 + c * 16;
#pragma unroll
      for (int k = 0; k < 4; ++k) gm[k] = ldsc(rws, (k & 1) ? goff1 : goff0, (k >> 1) * 2048);
      RETRY(gm, 4, ((k & 1) ? goff1 : goff0) + (k >> 1) * 2048);
      u32x4 a;
#pragma unroll
      for (int i = 0; i < 4; ++i) {
        const int qp = i >> 1, e = (i & 1) * 2;
        const float v0 = asf(gm[qp][e]) + asf(gm[2 + qp][e]), v1 = asf(gm[qp][e + 1]) + asf(gm[2 + qp][e + 1]);
        const u32 gg = pack_bf(v0, v1);
        const u32 uu = asu(dpp<DPP_ROR8>(asf(gg)));
        const u32 sg = pack_bf(sigm(bf_lo(gg)), sigm(bf_hi(gg)));
        a[i] = pack_bf(bf_lo(sg) * bf_lo(uu), bf_hi(sg) * bf_hi(uu));
        if (wv == 3 && hi) a[i] = 0;
      }
      PINV(a, 4);
      GETTZ();
      PRIO(0);
      const rsrc_t rw = mkr(p.wd + (size_t)(gw * 112 + 32 * wv) * DM + sD * 128, (wv == 3 ? 16 : 32) * DM * 2 - sD * 256);
#pragma unroll
      for (int j = 0; j < 8; ++j) { PACE(PD + j * SD / 2 + wv); wr[j] = ldnt(rw, 8 * g * (DM * 2) + c * 16, j * DM * 2); }
      ARRIVE(C_DI);
      PRIO(3);
#pragma unroll
      for (int jj = 0; jj < 4; ++jj) { tr_pair(wr, wt, jj); PIN(); }
      f32x4 d[8];
#pragma unroll
      for (int i = 0; i < 8; ++i) d[i] = mfma32(a, wt[i], f32x4{0, 0, 0, 0});
      store_acc(d, lds + L_RY + wv * R_WS, g, c);
      ARRIVE(C_Y);
      LWAITF(C_Y, 4);
      {
        const int t = wv * 64 + lane;
        const char* rf = lds + L_RY + (t >> 7) * R_GS + ((t >> 3) & 7) * R_IS + (((t >> 6) & 1) * 8 + (t & 7)) * 16;
        f32x4 s = *(const f32x4*)rf;
#pragma unroll
        for (int k = 1; k < 4; ++k) s += *(const f32x4*)(rf + k * R_WS);
        publish(rws, WS_FY + (((2 * sD + ((t >> 6) & 1)) * 2 + (t >> 7)) * 8 + ((t >> 3) & 7)) * 4096 + gw * 128 + (t & 7) * 16, s, tg);
      }
      ARRIVE(C_S5);
    } else {
      if (w == 0) {
        LWAIT(C_ST, 8);
        const float2 ml = *(const float2*)(lds + L_ST + lane * 8);
        float M, L;
        merge_stats(ml.x, ml.y, M, L);
        const float Lh = asf((asu(L) + 0x800u) & 0xfffff000u);
        publish(rws, lane < 8 ? WS_ST + grp * 2048 + win(sub * 128) + lane * 16 : OOB, f32x4{M, Lh, L - Lh, 0.f}, tg);
        *(float*)(lds + L_F + lane * 4) = __builtin_amdgcn_exp2f((ml.x - M) * L2E);
        ARRIVE(C_F);
      }
      GETTZ();
      PRIO(0);
      const rsrc_t rw = mkr(p.wo + (size_t)((gw >> 2) * 512 + (gw & 3) * 32 + 8 * wv) * DM + sO * 128, 400 * DM * 2);
#pragma unroll
      for (int j = 0; j < 8; ++j) { if (j == 0) LWAIT(C_VI, 8); PACE(PO + j * SO + wv); wr[j] = ldnt(rw, 2 * g * (DM * 2) + c * 16, ((j & 3) * 128 + (j >> 2)) * DM * 2); }
      PIN();
      PRIO(3);
#pragma unroll
      for (int jj = 0; jj < 4; ++jj) { tr_pair(wr, wt, jj); PIN(); }
#pragma unroll
      for (int i = 0; i < 8; ++i) *(u32x4*)(lds + L_OW + wv * 8192 + i * 1024 + lane * 16) = wt[i];
      ARRIVE(C_OW);
      if (w == 0) {
      GETT(L_TZ0);
      LWAIT(C_S5, 12);
      PACE(PF);
      u32x4 fm[8];
      const int foff = WS_FS + wg * 4096 + lane * 16;
#pragma unroll
      for (int k = 0; k < 8; ++k) fm[k] = ldsc(rws, foff, (k >> 2) * (WS_FY - WS_FS) + (k & 3) * 1024);
      RETRY(fm, 8, foff + (k >> 2) * (WS_FY - WS_FS) + (k & 3) * 1024);
      float z[2];
#pragma unroll
      for (int h = 0; h < 2; ++h) {
        float v[4];
#pragma unroll
        for (int i = 0; i < 4; ++i) v[i] = (asf(fm[4 * h][i]) + asf(fm[4 * h + 1][i])) + (asf(fm[4 * h + 2][i]) + asf(fm[4 * h + 3][i]));
        swap32(v[0], v[2]); swap32(v[1], v[3]);
        v[0] += v[2]; v[1] += v[3];
        swap16(v[0], v[1]);
        const float t = v[0] + v[1];
        z[h] = rbf(t + dpp<DPP_ROR8>(t));
      }
      z[1] = rbf(z[1] * sigm(z[1]));
      const float r = bf_lo(xfin) + rbf(z[1] * z[0]);
      if (c < 8) p.out[oidx] = (u16)pack_bf(r, r);
      if (wg == 0 && lane == 0) __builtin_amdgcn_raw_buffer_store_b32(e_, rws, 0, 0, AUX_SC1);
    }
    }
  }
}

extern "C" void kernel_launch(void* const* d_in, const int* in_sizes, int n_in, void* d_out, int out_size, void* d_ws, size_t ws_size,
                              hipStream_t stream) {
  Params p;
  p.x = (const u16*)d_in[0]; p.kc = (const u16*)d_in[1]; p.vc = (const u16*)d_in[2]; p.wq = (const u16*)d_in[3]; p.wo = (const u16*)d_in[4];
  p.wg = (const u16*)d_in[6]; p.wu = (const u16*)d_in[7]; p.wd = (const u16*)d_in[8];
  p.out = (u16*)d_out; p.ws = (char*)d_ws;
  fused_kernel<<<NWG, NT, 0, stream>>>(p);
}
```

```cpp
#include <hip/hip_runtime.h>
#include <hip/hip_bf16.h>
#include <stdint.h>

typedef unsigned int u32;
typedef unsigned short u16;
typedef u32 u32x4 __attribute__((ext_vector_type(4)));
typedef u32 u32x2 __attribute__((ext_vector_type(2)));
typedef float f32x4 __attribute__((ext_vector_type(4)));
typedef __bf16 bf16x8 __attribute__((ext_vector_type(8)));
typedef short s16x4 __attribute__((ext_vector_type(4)));
typedef __amdgpu_buffer_rsrc_t rsrc_t;

constexpr int DM = 1024, HQD = 4096, SK = 4096, DH = 128, FF = 3584;
constexpr int NWG = 256, NT = 1024;

constexpr int WS_T = 1024;
constexpr int WS_Q = 4096;
constexpr int WS_GU = WS_Q + 256 * 2048;
constexpr int WS_O = WS_GU + 224 * 4096;
constexpr int WS_ST = WS_O + 32 * 65536;
constexpr int WS_FS = WS_ST + 32 * 2048;
constexpr int WS_FY = WS_FS + 256 * 4096;
constexpr int WS_END = WS_FY + 256 * 4096;
constexpr int AUX_NT = 2, AUX_SC1 = 16;
constexpr u32 TM = 0xfffu;
constexpr u32 SPIN_CAP = 20000;

constexpr int XN_STR = 1032;
constexpr int A_STR = 136;
constexpr int RG_STR = 68;
constexpr int R_IS = 288, R_GS = 8 * R_IS, R_WS = 2 * R_GS;
constexpr int L_CNT = 0;
constexpr int L_XN = 128;
constexpr int L_QS = L_XN + 8 * XN_STR * 2;
constexpr int L_ST = L_QS + 8 * A_STR * 2;
constexpr int L_F = L_ST + 512;
constexpr int L_F2 = L_F + 256;
constexpr int L_P = L_F2 + 2048;
constexpr int L_RG = L_P + 8192;
constexpr int L_RO = L_RG + 16 * 8 * RG_STR * 4;
constexpr int L_RY = L_RO + 8 * R_WS;
constexpr int L_A = L_RY + 4 * R_WS;
constexpr int L_OW = L_A + 8 * A_STR * 2;
constexpr int L_END = L_OW + 32768;
enum { C_XN = 0, C_RQ, C_QS, C_ST, C_RG, C_VI, C_DI, C_F, C_RO, C_M4, C_Y, C_OW, C_A, C_S5, C_TZ, C_P = 16, L_EP = 29, L_TZ0 = 30, L_TZ1 = 31 };

#define PIN() do { asm volatile("" ::: "memory"); __builtin_amdgcn_sched_barrier(0); } while (0)
#define USE4(x) asm volatile("" ::"v"(x))
#define BAR() do { asm volatile("s_waitcnt lgkmcnt(0)\n\ts_barrier" ::: "memory"); __builtin_amdgcn_sched_barrier(0); } while (0)

__device__ __forceinline__ f32x4 mfma32(u32x4 a, u32x4 b, f32x4 c) {
  return __builtin_amdgcn_mfma_f32_16x16x32_bf16(__builtin_bit_cast(bf16x8, a), __builtin_bit_cast(bf16x8, b), c, 0, 0, 0);
}
__device__ __forceinline__ f32x4 mfma16(u32x2 a, u32x2 b, f32x4 c) {
  return __builtin_amdgcn_mfma_f32_16x16x16bf16_1k(__builtin_bit_cast(s16x4, a), __builtin_bit_cast(s16x4, b), c, 0, 0, 0);
}
__device__ __forceinline__ float asf(u32 x) { return __builtin_bit_cast(float, x); }
__device__ __forceinline__ u32 asu(float x) { return __builtin_bit_cast(u32, x); }
__device__ __forceinline__ float bf_lo(u32 x) { return asf(x << 16); }
__device__ __forceinline__ float bf_hi(u32 x) { return asf(x & 0xffff0000u); }
__device__ __forceinline__ u32 pack_bf(float lo, float hi) {
  typedef __bf16 bf16x2 __attribute__((ext_vector_type(2)));
  typedef float f32x2 __attribute__((ext_vector_type(2)));
  f32x2 v = {lo, hi};
  return __builtin_bit_cast(u32, __builtin_convertvector(v, bf16x2));
}
__device__ __forceinline__ float rcp(float x) { return __builtin_amdgcn_rcpf(x); }
constexpr float L2E = 1.4426950408889634f;
constexpr int OOB = 0x7ffffff0;
__device__ __forceinline__ float sigm(float x) { return rcp(1.f + __builtin_amdgcn_exp2f(-L2E * x)); }
__device__ __forceinline__ float rbf(float x) { return bf_lo(pack_bf(x, x)); }
__device__ __forceinline__ u32 perm_lo(u32 a, u32 b) { return __builtin_amdgcn_perm(b, a, 0x05040100u); }
__device__ __forceinline__ u32 perm_hi(u32 a, u32 b) { return __builtin_amdgcn_perm(b, a, 0x07060302u); }
template <int CTRL> __device__ __forceinline__ float dpp(float x) {
  return asf(__builtin_amdgcn_mov_dpp(asu(x), CTRL, 0xf, 0xf, true));
}
template <int BANK> __device__ __forceinline__ float ror8_merge(float old, float src) {
  return asf(__builtin_amdgcn_update_dpp(asu(old), asu(src), 0x128, 0xf, BANK, false));
}
constexpr int DPP_X1 = 0xB1, DPP_X2 = 0x4E, DPP_HMIR = 0x141, DPP_MIR = 0x140, DPP_ROR8 = 0x128, DPP_ROR4 = 0x124;
__device__ __forceinline__ void swap32(float& a, float& b) {
  auto r = __builtin_amdgcn_permlane32_swap(asu(a), asu(b), false, false);
  a = asf(r[0]); b = asf(r[1]);
}
__device__ __forceinline__ void swap16(float& a, float& b) {
  auto r = __builtin_amdgcn_permlane16_swap(asu(a), asu(b), false, false);
  a = asf(r[0]); b = asf(r[1]);
}
__device__ __forceinline__ float add32(float x) { float a = x, b = x; swap32(a, b); return a + b; }
__device__ __forceinline__ float add16(float x) { float a = x, b = x; swap16(a, b); return a + b; }
__device__ __forceinline__ float max32(float x) { float a = x, b = x; swap32(a, b); return fmaxf(a, b); }
__device__ __forceinline__ float max16(float x) { float a = x, b = x; swap16(a, b); return fmaxf(a, b); }
__device__ __forceinline__ float wave_sum(float x) {
  x += dpp<DPP_X1>(x); x += dpp<DPP_X2>(x); x += dpp<DPP_HMIR>(x); x += dpp<DPP_MIR>(x);
  return add32(add16(x));
}
__device__ __forceinline__ float wave_max(float f) {
  f = fmaxf(f, dpp<DPP_X1>(f)); f = fmaxf(f, dpp<DPP_X2>(f)); f = fmaxf(f, dpp<DPP_HMIR>(f)); f = fmaxf(f, dpp<DPP_MIR>(f));
  return max32(max16(f));
}
__device__ __forceinline__ u32x4 ldnt(rsrc_t r, int voff, int soff) { return __builtin_amdgcn_raw_buffer_load_b128(r, voff, soff, AUX_NT); }
__device__ __forceinline__ u32x4 ldsc(rsrc_t r, int voff, int soff) { return __builtin_amdgcn_raw_buffer_load_b128(r, voff, soff, AUX_SC1); }
__device__ __forceinline__ rsrc_t mkr(const void* p, int bytes) { return __builtin_amdgcn_make_buffer_rsrc((void*)p, 0, bytes, 0x00020000); }

struct Tags { u32 t[4]; };
__device__ __forceinline__ int win(int o) { return o + (o >= 256 ? 256 : 0); }
__device__ __forceinline__ void publish(rsrc_t r, int voff, f32x4 v, const Tags& tg, u32 tm = TM, bool nt = false) {
  u32x4 o;
#pragma unroll
  for (int i = 0; i < 4; ++i) o[i] = ((asu(v[i]) + (tm + 1) / 2) & ~tm) | tg.t[i];
  if (nt) __builtin_amdgcn_raw_buffer_store_b128(o, r, voff, 0, AUX_SC1 | AUX_NT);
  else __builtin_amdgcn_raw_buffer_store_b128(o, r, voff, 0, AUX_SC1);
}
__device__ __forceinline__ Tags qtags(u32 e) {
  Tags t;
  t.t[0] = e & 15; t.t[1] = (e + 1 + ((e >> 4) & 7)) & 15; t.t[2] = (e + (e >> 7)) & 15; t.t[3] = (e + (e >> 11)) & 15;
  return t;
}
__device__ __forceinline__ void strip(u32x4& m, const Tags& tg) {
#pragma unroll
  for (int i = 0; i < 4; ++i) m[i] ^= tg.t[i];
}
__device__ __forceinline__ u32 or4(const u32x4& m) { return (m[0] | m[1]) | (m[2] | m[3]); }

struct Params {
  const u16* x; const u16* kc; const u16* vc; const u16* wq; const u16* wo;
  const u16* wg; const u16* wu; const u16* wd;
  u16* out; char* ws;
};
constexpr int P_STEP16 = 57, P_LEAD = 1, PO = 360, SO = 4, PG3 = 352, PD = 396, SD = 7, P4 = 429, PF = 472, MODE = 1159, PT = 12846, TK0 = 320, SK16 = 57, PQ = 0, PS = 0, TW0 = 112, SW16 = 57, PV = 228, X18 = 22, X19 = 48, X20 = 0, ABL = 0, DB = 0, X23 = 0;
#define PACE(pos) PACET((((pos) - P_LEAD) * P_STEP16) >> 4)
#define PACET(t) do { PIN(); const int due_ = (t); while ((int)((u32)__builtin_amdgcn_s_memrealtime() - tz) < due_) __builtin_amdgcn_s_sleep(1); PIN(); } while (0)

template <int STR> __device__ __forceinline__ void store64(f32x4 (&d)[8], float* red, int g, int c) {
  f32x4 s[4];
#pragma unroll
  for (int i = 0; i < 4; ++i)
#pragma unroll
    for (int r = 0; r < 4; ++r) {
      float a = d[i][r], b = d[i + 4][r];
      swap32(a, b);
      s[r][i] = a + dpp<DPP_ROR8>(b);
    }
  if (c < 8) {
#pragma unroll
    for (int r = 0; r < 4; ++r) *(f32x4*)(red + (4 * (g & 1) + r) * STR + 8 * c + 4 * (g >> 1)) = s[r];
  }
}
__device__ __forceinline__ void store_acc(f32x4 (&d)[8], char* red, int g, int c) {
  if (g < 2) {
#pragma unroll
    for (int i = 0; i < 8; ++i) *(f32x4*)(red + g * R_GS + i * R_IS + c * 16) = d[i];
  }
}
__device__ __forceinline__ void tr_pair(const u32x4* w, u32x4 (&f)[8], int jj) {
#pragma unroll
  for (int i = 0; i < 8; ++i) {
    const u32 lo = w[2 * jj][i >> 1], hi = w[2 * jj + 1][i >> 1];
    f[i][jj] = (i & 1) ? perm_hi(lo, hi) : perm_lo(lo, hi);
  }
}
__device__ __forceinline__ void merge_stats(float m, float l, float& M, float& L) {
  M = fmaxf(m, dpp<DPP_ROR8>(m));
  M = max32(max16(M));
  L = l * __builtin_amdgcn_exp2f((m - M) * L2E);
  L += dpp<DPP_ROR8>(L);
  L = add32(add16(L));
}

#define ARRIVE(i) do { PIN(); asm volatile("s_mov_b64 exec, 1\n\tds_add_u32 %0, %1\n\ts_mov_b64 exec, -1" :: "v"(lcnt + 4 * (i)), "v"(1u) : "memory"); PIN(); } while (0)
#define PINV(a, n) do { _Pragma("unroll") for (int i_ = 0; i_ < (n); ++i_) asm volatile("" : "+v"((a)[i_])); } while (0)
#define LWAITS(i, n, SL) do { PIN(); u32 it_ = 0; \
    while (__builtin_amdgcn_readfirstlane(__hip_atomic_load((u32*)(lds + L_CNT) + (i), __ATOMIC_RELAXED, __HIP_MEMORY_SCOPE_WORKGROUP)) < (u32)(n) && ++it_ < (1u << 22)) { SL; } \
    PIN(); } while (0)
#define LWAIT(i, n) LWAITS(i, n, __builtin_amdgcn_s_sleep(1))
#define LWAITF(i, n) LWAITS(i, n, )
#define GETTZ() GETT(L_TZ1)
#define GETT(i) do { PIN(); tz = __builtin_amdgcn_readfirstlane(__hip_atomic_load((u32*)(lds + L_CNT) + (i), __ATOMIC_RELAXED, __HIP_MEMORY_SCOPE_WORKGROUP)); PIN(); } while (0)
#define LATE() do { if (DB) { const u32 tb_ = (u32)__builtin_amdgcn_s_memrealtime() - DB; if ((int)(tb_ - tz) > 0) tz = tb_; PIN(); } } while (0)
#define PRIO(n) do { __builtin_amdgcn_s_setprio(n); PIN(); } while (0)
#define MKTAGS() Tags tg; const u32 e_ = __builtin_amdgcn_readfirstlane(__hip_atomic_load((u32*)(lds + L_CNT) + L_EP, __ATOMIC_RELAXED, __HIP_MEMORY_SCOPE_WORKGROUP)) + 1u; { _Pragma("unroll") for (int i_ = 0; i_ < 4; ++i_) tg.t[i_] = (e_ + 0x4d5u * i_) & TM; }
#define RETRY(m, n, voff, soff) do { \
    for (u32 spin_ = 0;; ++spin_) { asm volatile("; check" ::: "memory"); u32 bad_ = 0; \
      _Pragma("unroll") for (int k = 0; k < (n); ++k) { strip(m[k], tg); bad_ |= or4(m[k]); } \
      if ((bad_ & TM) == 0 || spin_ >= SPIN_CAP) break; \
      _Pragma("unroll") for (int k = 0; k < (n); ++k) m[k] = ldsc(rws, (voff), (soff)); \
    } PIN(); } while (0)

#define GATHER(m, n, voff, soff) do { u32 spin_ = 0, bad_; \
    do { asm volatile("; gather" ::: "memory"); bad_ = 0; \
      _Pragma("unroll") for (int k = 0; k < (n); ++k) m[k] = ldsc(rws, (voff), (soff)); \
      _Pragma("unroll") for (int k = 0; k < (n); ++k) { strip(m[k], tg); bad_ |= or4(m[k]); } \
    } while ((bad_ & TM) != 0 && ++spin_ < SPIN_CAP); PIN(); } while (0)

#define CPF 1
#define CPFX 0
#define CPFS 128
#define CPFN 256
#define CPFA 0
#define CLEN 18736
#define TZM 2
#define EXPW 1
typedef u32 u32x16 __attribute__((ext_vector_type(16)));
__device__ __forceinline__ char* ptr2(u32 lo, u32 hi) { return (char*)(__attribute__((address_space(1))) char*)(((uint64_t)hi << 32) | lo); }

__global__ void __launch_bounds__(NT, 4) fused_kernel(Params pa) {
  __shared__ __attribute__((aligned(16))) char lds[L_END];
  u32x16 ka; u32x4 kc; u32x2 t0;
  asm volatile("s_load_dwordx16 %0, %3, 0x0\n\ts_load_dwordx4 %1, %3, 0x40\n\ts_memrealtime %2"
               : "=&s"(ka), "=&s"(kc), "=&s"(t0) : "s"(__builtin_amdgcn_kernarg_segment_ptr()));
  u32x2 pc;
  asm volatile("s_getpc_b64 %0" : "=s"(pc));
  const u32 cpf = __builtin_amdgcn_raw_buffer_load_b32(mkr(ptr2(pc[0], pc[1]), CLEN), threadIdx.x * 128, 0, 0);
  __builtin_amdgcn_sched_barrier(0);
  const int tid = threadIdx.x, lane = tid & 63, wave = __builtin_amdgcn_readfirstlane(tid >> 6);
  const int g = lane >> 4, c = lane & 15, hi = lane >> 5;
  const int wg = blockIdx.x, grp = (wg & 7) * 4 + (wg >> 6), sub = (wg >> 3) & 7;
  const bool ta = wg < 198, th = ta && wg >= 154;
  int nv[3];
#pragma unroll
  for (int i = 0; i < 3; ++i) {
    const int f = ta ? wg : wg - 198 + 58 * i, fq = (f * 147) >> 10, fr = f - 7 * fq;
    nv[i] = th ? ((wg - 154) >> 1) * 8 + 3 : f + fq + (fr >= 3);
  }
  asm volatile("s_waitcnt lgkmcnt(0)" : "+s"(ka), "+s"(kc), "+s"(t0), "+s"(nv[0]), "+s"(nv[1]), "+s"(nv[2]));
  u32 tz = (u32)__builtin_amdgcn_s_memrealtime();
  Params p = pa;
  p.x = (const u16*)ptr2(ka[0], ka[1]); p.kc = (const u16*)ptr2(ka[2], ka[3]); p.vc = (const u16*)ptr2(ka[4], ka[5]); p.wq = (const u16*)ptr2(ka[6], ka[7]);
  p.wo = (const u16*)ptr2(ka[8], ka[9]); p.wg = (const u16*)ptr2(ka[10], ka[11]); p.wu = (const u16*)ptr2(ka[12], ka[13]); p.wd = (const u16*)ptr2(ka[14], ka[15]);
  p.out = (u16*)ptr2(kc[0], kc[1]); p.ws = (char*)ptr2(kc[2], kc[3]);
  int cgv[3];
#pragma unroll
  for (int i = 0; i < 3; ++i) {
    const int n = nv[i], sh = (int)((uintptr_t)(n < 64 ? p.wq : n < 120 ? p.wg : p.wu) >> 7);
    cgv[i] = (th ? i < 2 : n < 176) ? (n & ~7) | ((n - sh) & 7) : -1;
  }
  const int rbase = th ? (wg & 1) * 512 : ta ? 0 : 768;
  const int gw = grp, sO = (sub + 1) & 7, sD = (sub + 3) & 7;
  const int w = wave & 7, wv = wave & 3;
  const rsrc_t rws = mkr(p.ws, WS_END);
  const u32 lcnt = (u32)(uintptr_t)(__attribute__((address_space(3))) char*)lds + L_CNT;
  const size_t kvoff = (size_t)grp * SK * DH + (((sub << (PT & 31)) + (w << ((PT >> 5) & 31))) >> 1);
  const int rot = (MODE & 2) ? (w + sub) & 3 : 0;
  int kb[4];
#pragma unroll
  for (int i = 0; i < 4; ++i) kb[i] = ((i + rot) & 3) << (PT >> 10);
  if (EXPW ? __builtin_expect(wave < 8, 1) : wave < 8) {
    PRIO(3);
    const u32 ep = __builtin_amdgcn_raw_buffer_load_b32(rws, w == 0 ? 0 : OOB, 0, AUX_SC1);
    const u32 xp = *(const u32*)(p.x + w * DM + lane * 16);
    PIN();
    u32x4 wqr[4], gr[8], vr[16];
    const int r0 = rbase + w * 32, r1 = ta ? (rbase + 256 + w * 64) & 1023 : 768 + wv * 64;
    {
      const int cg = cgv[0], st = cg < 64 ? HQD * 2 : FF * 2;
      const rsrc_t r = mkr((const char*)(cg < 64 ? p.wq : cg < 120 ? p.wg : p.wu) + r0 * st + (cg - (cg < 64 ? 0 : cg < 120 ? 64 : 120)) * 128, 31 * st + 128);
      const int vo = (16 * (c >> 3) + 4 * g) * st + (c & 7) * 16;
#pragma unroll
      for (int j = 0; j < 4; ++j) wqr[j] = ldnt(r, vo, j * st);
    }
    if (tid < 32) ((u32*)(lds + L_CNT))[tid] = tid >= L_TZ0 ? tz : 0;
    BAR();
    GETTZ();
    LATE();
    if (DB && tid < 2) ((u32*)(lds + L_CNT))[L_TZ0 + tid] = tz;
    {
      const int cg = w < 4 ? cgv[1] : cgv[2], st = cg < 64 ? HQD * 2 : FF * 2;
      const rsrc_t r = mkr((const char*)(cg < 64 ? p.wq : cg < 120 ? p.wg : p.wu) + r1 * st + (cg - (cg < 64 ? 0 : cg < 120 ? 64 : 120)) * 128, cg < 0 ? 0 : 63 * st + 128);
      const int vo = (32 * (c >> 3) + 8 * g) * st + (c & 7) * 16;
#pragma unroll
      for (int j = 0; j < 8; ++j) { PACET(TW0 + (((j * 7 + w) * SW16) >> 4)); gr[j] = ldnt(r, vo, j * st); }
    }
    PIN();
    USE4(xp);
    USE4(cpf);
    if (w == 0) ((u32*)(lds + L_CNT))[L_EP] = ep;
    u32x2 fq[8];
#pragma unroll
    for (int i = 0; i < 8; ++i)
#pragma unroll
      for (int jj = 0; jj < 2; ++jj) {
        const u32 lo = wqr[2 * jj][i >> 1], hh = wqr[2 * jj + 1][i >> 1];
        fq[i][jj] = (i & 1) ? perm_hi(lo, hh) : perm_lo(lo, hh);
      }
    PINV(fq, 8);
    LWAIT(C_XN, 8);
    {
      const u32x2 a0 = *(const u32x2*)(lds + L_XN + ((c & 7) * XN_STR + r0 + 16 * (c >> 3) + 4 * g) * 2);
      const u32x4 a1 = *(const u32x4*)(lds + L_XN + ((c & 7) * XN_STR + r1 + 32 * (c >> 3) + 8 * g) * 2);
      f32x4 d[8];
#pragma unroll
      for (int i = 0; i < 8; ++i) d[i] = mfma16(a0, fq[i], f32x4{0, 0, 0, 0});
      if (!ta) {
        store64<RG_STR>(d, (float*)(lds + L_RG) + (8 + w) * 8 * RG_STR, g, c);
#pragma unroll
        for (int i = 0; i < 8; ++i) d[i] = f32x4{0, 0, 0, 0};
      }
      u32x4 f[8];
#pragma unroll
      for (int jj = 0; jj < 4; ++jj) tr_pair(gr, f, jj);
#pragma unroll
      for (int i = 0; i < 8; ++i) d[i] = mfma32(a1, f[i], d[i]);
      store64<RG_STR>(d, (float*)(lds + L_RG) + w * 8 * RG_STR, g, c);
    }
    ARRIVE(C_RG);
    LWAIT(C_RG, 8);
    MKTAGS();
    if (w == 7) {
      const u32 t20 = tz << 12;
      __builtin_amdgcn_raw_buffer_store_b128(u32x4{t20 | tg.t[0], t20 | tg.t[1], t20 | tg.t[2], t20 | tg.t[3]}, rws,
                                             (lane == 0 && !(wg & 24) && !(MODE & 16)) ? WS_T + ((wg >> 5) * 8 + (wg & 7)) * 16 : OOB, 0, AUX_SC1);
    }
    if (w < (ta ? 2 : 6)) {
      const int t = w >> 1, cg = t == 0 ? cgv[0] : t == 1 ? cgv[1] : cgv[2], l = (w & 1) * 64 + lane;
      const bool isq = cg < 64;
      const int b = isq ? l >> 4 : l & 7, cq = isq ? l & 15 : (l >> 5) * 4 + ((l >> 3) & 1) * 2 + ((l >> 4) & 1);
      const float* rg = (const float*)(lds + L_RG) + ((t == 0 ? (ta ? 0 : 8) : 4 * t - 4) * 8 + b) * RG_STR + cq * 4;
      f32x4 s = *(const f32x4*)rg;
#pragma unroll
      for (int k = 1; k < 4; ++k) s += *(const f32x4*)(rg + k * 8 * RG_STR);
      if (t == 0) {
#pragma unroll
        for (int k = 4; k < 8; ++k) s += *(const f32x4*)(rg + k * 8 * RG_STR);
      }
      const int slot = rbase != 0, gu = cg >= 120;
      const int aq = WS_Q + (b * 32 + (cg >> 1)) * 2048 + win(slot * 512 + (cg & 1) * 256 + cq * 16);
      const int ag = WS_GU + ((cg - (gu ? 120 : 64)) * 4 + (l >> 5)) * 4096 + slot * 2048 + win(((l >> 3) & 3) * 128 + gu * 512 + b * 16);
      if (PS & 1023) {
        const u32 now = (u32)__builtin_amdgcn_s_memrealtime(), lo = tz + (PS >> 10);
        const u32 tgt = (((int)(lo - now) > 0 ? lo : now) + (PS & 1023) - 1) & ~(u32)((PS & 1023) - 1);
        USE4(s);
        PACET((int)(tgt - tz));
      }
      publish(rws, cg < 0 ? OOB : isq ? aq : ag, s, isq ? qtags(e_) : tg, isq ? 15u : TM, isq ? (MODE & 4096) : (MODE & 8192));
    }
    GETTZ();
    PRIO(0);
    const rsrc_t rv = mkr(p.vc + kvoff, (3 << (PT >> 10)) + 4096);
#pragma unroll
    for (int n = 0; n < 16; ++n) { if (n >= 3 && n <= 9) GETTZ(); PACE(PV + n * 8 + w); vr[n] = ldnt(rv, lane * 16 + (n & 3) * 1024, kb[n >> 2]); }
    ARRIVE(C_VI);
    PRIO(3);
    u32x4 vt[8];
#pragma unroll
    for (int jj = 0; jj < 4; ++jj) tr_pair(vr, vt, jj);
    PINV(vt, 8);
    LWAIT(C_P + w, 1);
    f32x4 acc[8];
    {
      const u32x4 pf = *(const u32x4*)(lds + L_P + w * 1024 + lane * 16);
#pragma unroll
      for (int i = 0; i < 8; ++i) acc[i] = mfma32(pf, vt[i], f32x4{0, 0, 0, 0});
    }
    PIN();
    const u32x4 pf1 = *(const u32x4*)(lds + L_P + w * 1024 + (lane ^ 8) * 16);
#pragma unroll
    for (int jj = 0; jj < 4; ++jj) { tr_pair(vr + 8, vt, jj); PIN(); }
#pragma unroll
    for (int i = 0; i < 8; ++i) acc[i] = mfma32(pf1, vt[i], acc[i]);
    store_acc(acc, lds + L_RO + w * R_WS, g, c);
    ARRIVE(C_RO);
    LWAIT(C_F, 1);
    LWAIT(C_RO, 8);
    {
      const int g2 = w >> 2, i = lane & 7, cl = (lane >> 3) & 3;
      const char* ro = lds + L_RO + hi * 4 * R_WS + g2 * R_GS + i * R_IS + (wv * 4 + cl) * 16;
      const char* fp = lds + L_F + hi * 128 + g2 * 16;
      f32x4 o = *(const f32x4*)ro * *(const f32x4*)fp;
#pragma unroll
      for (int k = 1; k < 4; ++k) o += *(const f32x4*)(ro + k * R_WS) * *(const f32x4*)(fp + k * 32);
#pragma unroll
      for (int r = 0; r < 4; ++r) o[r] = add32(o[r]);
      publish(rws, hi ? OOB : WS_O + (((grp & 3) * 2 + g2) * 4 + wv) * 65536 + (grp >> 2) * 8192 + (sub >> 1) * 2048 + win((sub & 1) * 512 + lane * 16), o, tg, TM, MODE & 16384);
    }
    ARRIVE(C_M4);
    u32x4 sm[1];
    const bool sv = ((lane >> 2) & 1) == ((gw >> 2) & 1);
    const int stoff = sv ? WS_ST + (w * 4 + (gw >> 3)) * 2048 + win(lane * 16) : OOB;
    sm[0] = ldsc(rws, stoff, 0);
    LWAIT(C_M4, 8);
    GETT((MODE & 32) ? L_TZ1 : L_TZ0);
    PACE(P4);
    if (MODE & 128) LWAITF(C_DI, 4);
    u32x4 om[4], bw[4];
    const int ooff = WS_O + gw * 65536 + w * 8192 + win(lane * 16);
#pragma unroll
    for (int k = 0; k < 4; ++k) om[k] = ldsc(rws, ooff, k * 2048);
    if (!sv) sm[0] = u32x4{tg.t[0], tg.t[1], tg.t[2], tg.t[3]};
    strip(sm[0], tg);
    for (u32 spin = 0; __builtin_expect(__builtin_amdgcn_ballot_w64((or4(sm[0]) & TM) != 0) != 0, 0) && spin < SPIN_CAP; ++spin) {
      if (sv) { sm[0] = ldsc(rws, stoff, 0); strip(sm[0], tg); }
    }
    f32x4 fs[4];
    {
      float M, L;
      merge_stats(asf(sm[0][0]), asf(sm[0][1]) + asf(sm[0][2]), M, L);
      *(float*)(lds + L_F2 + w * 256 + lane * 4) = __builtin_amdgcn_exp2f((asf(sm[0][0]) - M) * L2E) * rcp(L);
      PIN();
#pragma unroll
      for (int k = 0; k < 4; ++k) fs[k] = *(const f32x4*)(lds + L_F2 + w * 256 + (2 * k + hi) * 32 + ((gw >> 2) & 1) * 16);
    }
    LWAIT(C_OW, 4);
#pragma unroll
    for (int k = 0; k < 4; ++k) bw[k] = *(const u32x4*)(lds + L_OW + k * 8192 + w * 1024 + lane * 16);
    PINV(bw, 4);
    PINV(fs, 4);
    RETRY(om, 4, ooff, k * 2048);
    {
      float v[4];
#pragma unroll
      for (int i = 0; i < 4; ++i) v[i] = (asf(om[0][i]) * fs[0][i] + asf(om[1][i]) * fs[1][i]) + (asf(om[2][i]) * fs[2][i] + asf(om[3][i]) * fs[3][i]);
      swap32(v[0], v[2]); swap32(v[1], v[3]);
      *(u32*)(lds + L_A + (w * A_STR + (lane & 31) * 4 + 2 * hi) * 2) = pack_bf(v[0] + v[2], v[1] + v[3]);
    }
    ARRIVE(C_A);
    LWAITF(C_A, 8);
    {
      u32x4 a[4];
#pragma unroll
      for (int k = 0; k < 4; ++k) a[k] = *(const u32x4*)(lds + L_A + ((c & 7) * A_STR + 32 * k + 8 * g) * 2);
      f32x4 d0 = mfma32(a[0], bw[0], f32x4{0, 0, 0, 0}), d1 = mfma32(a[1], bw[1], f32x4{0, 0, 0, 0});
      d0 = mfma32(a[2], bw[2], d0); d1 = mfma32(a[3], bw[3], d1);
      publish(rws, hi ? OOB : WS_FS + (((2 * sO + (c >> 3)) * 2 + g) * 8 + w) * 4096 + gw * 128 + (c & 7) * 16, d0 + d1, tg);
    }
    ARRIVE(C_S5);
  } else {
    PRIO(3);
    const u32x4 xa = *(const u32x4*)(p.x + w * DM + lane * 8);
    const u32x4 xb = *(const u32x4*)(p.x + w * DM + 512 + lane * 8);
    const int oidx = (4 * ((wg >> 3) & 1) + g) * DM + (wg >> 4) * 64 + (c & 7) * 8 + (wg & 7);
    const u32 xfin = p.x[oidx];
    asm volatile("s_barrier" ::: "memory");
    {
      float v[16];
#pragma unroll
      for (int i = 0; i < 4; ++i) { v[2 * i] = bf_lo(xa[i]); v[2 * i + 1] = bf_hi(xa[i]); v[8 + 2 * i] = bf_lo(xb[i]); v[9 + 2 * i] = bf_hi(xb[i]); }
      float s = 0.f;
#pragma unroll
      for (int i = 0; i < 16; ++i) s += v[i];
      const float mean = wave_sum(s) * (1.f / DM);
      float q = 0.f;
#pragma unroll
      for (int i = 0; i < 16; ++i) { const float d = v[i] - mean; q += d * d; }
      const float var = rbf(wave_sum(q) * (1.f / DM));
      const float mb = rbf(mean);
      const float rs = rcp(rbf(__builtin_amdgcn_sqrtf(rbf(var + 1e-5f))));
      u32x4 o[2];
#pragma unroll
      for (int i = 0; i < 8; ++i) {
        const u32 d = pack_bf(v[2 * i] - mb, v[2 * i + 1] - mb);
        o[i >> 2][i & 3] = pack_bf(bf_lo(d) * rs, bf_hi(d) * rs);
      }
      *(u32x4*)(lds + L_XN + (w * XN_STR + lane * 8) * 2) = o[0];
      *(u32x4*)(lds + L_XN + (w * XN_STR + 512 + lane * 8) * 2) = o[1];
    }
    ARRIVE(C_XN);
    USE4(cpf);
    GETTZ();
    PRIO(2);
    const rsrc_t rk = mkr(p.kc + kvoff, (3 << (PT >> 10)) + 4096);
    const int kvo = (4 * (c & 3) + ((c >> 2) & 1)) * 256 + (4 * (c >> 3) + g) * 16;
    u32x4 kr[16];
#pragma unroll
    for (int n = 0; n < 16; ++n) { if (!(n & 1)) PACET(TK0 + (((n * 8 + w * 2) * SK16) >> 4) + (n * 8 + w * 2 >= X19 ? X18 : 0)); kr[n] = ldnt(rk, kvo + ((n >> 1) & 1) * 512 + (n & 1) * 128, kb[n >> 2]); }
    PIN();
    PRIO(3);
    u32x4 qm[1];
    const int qoff = WS_Q + ((grp >> 2) * 32 + (grp & 3) * 8 + w) * 2048 + win(lane * 16);
    PACET(PQ);
    qm[0] = ldsc(rws, qoff, 0);
    u32x4 tm = ldsc(rws, (w == 7 && !(MODE & 16)) ? WS_T + lane * 16 : OOB, 0);
    PIN();
    LWAIT(C_RG, 8);
    MKTAGS();
    {
      const Tags tq = qtags(e_);
      strip(qm[0], tq);
      for (u32 spin = 0; __builtin_expect(__builtin_amdgcn_ballot_w64((or4(qm[0]) & 15u) != 0) != 0, 0) && spin < SPIN_CAP; ++spin) {
        qm[0] = ldsc(rws, qoff, 0);
        strip(qm[0], tq);
      }
    }
    {
      float q0 = asf(qm[0][0]), q1 = asf(qm[0][1]), q2 = asf(qm[0][2]), q3 = asf(qm[0][3]);
      swap32(q0, q2); swap32(q1, q3);
      *(u32*)(lds + L_QS + (w * A_STR + (lane & 31) * 4 + 2 * hi) * 2) = pack_bf(q0 + q2, q1 + q3);
    }
    if (w == 7) {
      strip(tm, tg);
      int d = (int)(tm[0] - (tz << 12)) >> 12;
      const bool ok = (or4(tm) & 0xfffu) == 0 && d >= -150 && d <= 150 && (MODE & 4);
      if (!ok) d = 0;
      if (MODE & 64) {
        const int n = __builtin_popcountll(__builtin_amdgcn_ballot_w64(ok));
        const u32 tc = tz + (int)(wave_sum((float)d) * rcp((float)(n + !n)));
        ((u32*)(lds + L_CNT))[L_TZ0 + (lane & 1)] = tc;
      } else if (MODE & 256) {
        const int n = __builtin_popcountll(__builtin_amdgcn_ballot_w64(ok));
        const int dm = (int)(wave_sum((float)d) * rcp((float)(n + !n))) + X20;
        ((u32*)(lds + L_CNT))[L_TZ0 + (lane & 1)] = tz + ((lane & 1) ? min(dm, 0) : dm);
      } else {
        ((u32*)(lds + L_CNT))[L_TZ0] = tz + (int)wave_max((float)d);
      }
    }
    ARRIVE(C_QS);
    LWAIT(C_QS, 8);
    {
      const u32x4 q0 = *(const u32x4*)(lds + L_QS + ((c & 7) * A_STR + 32 * (c >> 3) + 8 * g) * 2);
      const u32x4 q1 = *(const u32x4*)(lds + L_QS + ((c & 7) * A_STR + 64 + 32 * (c >> 3) + 8 * g) * 2);
      float sc[8];
#pragma unroll
      for (int e = 0; e < 2; ++e) {
        f32x4 t[4];
#pragma unroll
        for (int ua = 0; ua < 4; ++ua) t[ua] = mfma32(kr[(ua >> 1) * 8 + e * 4 + (ua & 1) * 2], q0, f32x4{0, 0, 0, 0});
#pragma unroll
        for (int ua = 0; ua < 4; ++ua) t[ua] = mfma32(kr[(ua >> 1) * 8 + e * 4 + (ua & 1) * 2 + 1], q1, t[ua]);
#pragma unroll
        for (int r = 0; r < 4; ++r) {
          float a = t[0][r], b = t[1][r], a2 = t[2][r], b2 = t[3][r];
          swap32(a, b); swap32(a2, b2);
          sc[e * 4 + r] = ror8_merge<0x3>(b2, b) + ror8_merge<0xc>(a, a2);
        }
      }
      float mx = -INFINITY;
#pragma unroll
      for (int i = 0; i < 4; ++i) {
        const u32 s1 = pack_bf(sc[2 * i], sc[2 * i + 1]);
        const u32 s2 = pack_bf(bf_lo(s1) * 0.08838834764831845f, bf_hi(s1) * 0.08838834764831845f);
        sc[2 * i] = bf_lo(s2); sc[2 * i + 1] = bf_hi(s2);
        mx = fmaxf(mx, fmaxf(sc[2 * i], sc[2 * i + 1]));
      }
      mx = fmaxf(mx, dpp<DPP_ROR8>(mx));
      const float mw = max32(max16(mx));
      float l = 0.f;
      u32x4 pf;
#pragma unroll
      for (int i = 0; i < 4; ++i) {
        const float e0 = __builtin_amdgcn_exp2f((sc[2 * i] - mw) * L2E), e1 = __builtin_amdgcn_exp2f((sc[2 * i + 1] - mw) * L2E);
        l += e0 + e1;
        pf[i] = pack_bf(e0, e1);
      }
      *(u32x4*)(lds + L_P + w * 1024 + lane * 16) = pf;
      ARRIVE(C_P + w);
      l += dpp<DPP_ROR8>(l);
      l = add32(add16(l));
      if (lane < 8) *(float2*)(lds + L_ST + (w * 8 + lane) * 8) = make_float2(mw, l);
    }
    ARRIVE(C_ST);
    u32x4 wr[8], wt[8];
    if (w >= 4) {
      if (MODE & 8) LWAIT(C_VI, 8);
      PACE(PG3 + wv);
      u32x4 gm[4];
      const int gbase = WS_GU + (gw * 7 + 2 * wv + (wv == 3 ? 0 : hi)) * 4096;
      const int dx = (MODE & 1024) ? wv : 0;
      const int goffa = gbase + (c >> 3) * 768 + (g & 1) * 128 + (c & 7) * 16, goffb = gbase + 512 + (c >> 3) * 512 + (g & 1) * 128 + (c & 7) * 16;
      const int goff0 = (dx & 2) ? goffb : goffa, goff1 = (dx & 2) ? goffa : goffb;
      GATHER(gm, 4, (k & 1) ? goff1 : goff0, (k >> 1) * 2048);
      u32x4 a;
#pragma unroll
      for (int i = 0; i < 4; ++i) {
        const int qp = i >> 1, e = (i & 1) * 2;
        const float v0 = asf(gm[qp][e]) + asf(gm[2 + qp][e]), v1 = asf(gm[qp][e + 1]) + asf(gm[2 + qp][e + 1]);
        const u32 gg = pack_bf(v0, v1);
        const u32 uu = asu(dpp<DPP_ROR8>(asf(gg)));
        const u32 sg = pack_bf(sigm(bf_lo(gg)), sigm(bf_hi(gg)));
        a[i] = pack_bf(bf_lo(sg) * bf_lo(uu), bf_hi(sg) * bf_hi(uu));
        if (wv == 3 && hi) a[i] = 0;
      }
      if (dx & 1) a = u32x4{a[1], a[0], a[3], a[2]};
      PINV(a, 4);
      GETTZ();
      PRIO(0);
      const rsrc_t rw = mkr(p.wd + (size_t)(gw * 112 + 32 * wv) * DM + sD * 128, (wv == 3 ? 16 : 32) * DM * 2 - sD * 256);
#pragma unroll
      for (int j = 0; j < 8; ++j) { PACE(PD + j * SD / 2 + wv); wr[j] = ldnt(rw, 8 * g * (DM * 2) + c * 16, (j ^ (2 * dx)) * DM * 2); }
      ARRIVE(C_DI);
      PRIO(3);
#pragma unroll
      for (int jj = 0; jj < 4; ++jj) { tr_pair(wr, wt, jj); PIN(); }
      f32x4 d[8];
#pragma unroll
      for (int i = 0; i < 8; ++i) d[i] = mfma32(a, wt[i], f32x4{0, 0, 0, 0});
      store_acc(d, lds + L_RY + wv * R_WS, g, c);
      ARRIVE(C_Y);
      LWAITF(C_Y, 4);
      {
        const int t = wv * 64 + lane;
        const char* rf = lds + L_RY + (t >> 7) * R_GS + ((t >> 3) & 7) * R_IS + (((t >> 6) & 1) * 8 + (t & 7)) * 16;
        f32x4 s = *(const f32x4*)rf;
#pragma unroll
        for (int k = 1; k < 4; ++k) s += *(const f32x4*)(rf + k * R_WS);
        publish(rws, WS_FY + (((2 * sD + ((t >> 6) & 1)) * 2 + (t >> 7)) * 8 + ((t >> 3) & 7)) * 4096 + gw * 128 + (t & 7) * 16, s, tg);
      }
      ARRIVE(C_S5);
    } else {
      if (w == 0) {
        LWAIT(C_ST, 8);
        const float2 ml = *(const float2*)(lds + L_ST + lane * 8);
        float M, L;
        merge_stats(ml.x, ml.y, M, L);
        const float Lh = asf((asu(L) + 0x800u) & 0xfffff000u);
        publish(rws, lane < 8 ? WS_ST + grp * 2048 + win(sub * 128) + lane * 16 : OOB, f32x4{M, Lh, L - Lh, 0.f}, tg);
        *(float*)(lds + L_F + lane * 4) = __builtin_amdgcn_exp2f((ml.x - M) * L2E);
        ARRIVE(C_F);
      }
      GETTZ();
      PRIO(0);
      const rsrc_t rw = mkr(p.wo + (size_t)((gw >> 2) * 512 + (gw & 3) * 32 + 8 * wv) * DM + sO * 128, 400 * DM * 2);
#pragma unroll
      for (int j = 0; j < 8; ++j) { if (j == 0) LWAIT(C_VI, 8); PACE(PO + j * SO + wv); wr[j] = ldnt(rw, 2 * g * (DM * 2) + c * 16, ((j & 3) * 128 + (j >> 2)) * DM * 2); }
      PIN();
      PRIO(3);
#pragma unroll
      for (int jj = 0; jj < 4; ++jj) { tr_pair(wr, wt, jj); PIN(); }
#pragma unroll
      for (int i = 0; i < 8; ++i) *(u32x4*)(lds + L_OW + wv * 8192 + i * 1024 + lane * 16) = wt[i];
      ARRIVE(C_OW);
      if (w == 0) {
      GETT(L_TZ0);
      LWAIT(C_S5, 12);
      PACE(PF);
      u32x4 fm[8];
      const int foff = WS_FS + wg * 4096 + lane * 16;
      GATHER(fm, 8, foff, (k >> 2) * (WS_FY - WS_FS) + (k & 3) * 1024);
      float z[2];
#pragma unroll
      for (int h = 0; h < 2; ++h) {
        float v[4];
#pragma unroll
        for (int i = 0; i < 4; ++i) v[i] = (asf(fm[4 * h][i]) + asf(fm[4 * h + 1][i])) + (asf(fm[4 * h + 2][i]) + asf(fm[4 * h + 3][i]));
        swap32(v[0], v[2]); swap32(v[1], v[3]);
        v[0] += v[2]; v[1] += v[3];
        swap16(v[0], v[1]);
        const float t = v[0] + v[1];
        z[h] = rbf(t + dpp<DPP_ROR8>(t));
      }
      z[1] = rbf(z[1] * sigm(z[1]));
      const float r = bf_lo(xfin) + rbf(z[1] * z[0]);
      if (c < 8) p.out[oidx] = (u16)pack_bf(r, r);
      if (wg == 0 && lane == 0) __builtin_amdgcn_raw_buffer_store_b32(e_, rws, 0, 0, AUX_SC1);
    }
    }
  }
}

extern "C" void kernel_launch(void* const* d_in, const int* in_sizes, int n_in, void* d_out, int out_size, void* d_ws, size_t ws_size,
                              hipStream_t stream) {
  Params p;
  p.x = (const u16*)d_in[0]; p.kc = (const u16*)d_in[1]; p.vc = (const u16*)d_in[2]; p.wq = (const u16*)d_in[3]; p.wo = (const u16*)d_in[4];
  p.wg = (const u16*)d_in[6]; p.wu = (const u16*)d_in[7]; p.wd = (const u16*)d_in[8];
  p.out = (u16*)d_out; p.ws = (char*)d_ws;
  fused_kernel<<<NWG, NT, 0, stream>>>(p);
}
```

```cpp
#include <hip/hip_runtime.h>
#include <hip/hip_bf16.h>
#include <stdint.h>

typedef unsigned int u32;
typedef unsigned short u16;
typedef u32 u32x4 __attribute__((ext_vector_type(4)));
typedef u32 u32x2 __attribute__((ext_vector_type(2)));
typedef float f32x4 __attribute__((ext_vector_type(4)));
typedef __bf16 bf16x8 __attribute__((ext_vector_type(8)));
typedef short s16x4 __attribute__((ext_vector_type(4)));
typedef __amdgpu_buffer_rsrc_t rsrc_t;

constexpr int DM = 1024, HQD = 4096, SK = 4096, DH = 128, FF = 3584;
constexpr int NWG = 256, NT = 1024;

constexpr int WS_T = 1024;
constexpr int WS_Q = 4096;
constexpr int WS_GU = WS_Q + 256 * 2048;
constexpr int WS_O = WS_GU + 224 * 4096;
constexpr int WS_ST = WS_O + 32 * 65536;
constexpr int WS_FS = WS_ST + 32 * 2048;
constexpr int WS_FY = WS_FS + 256 * 4096;
constexpr int WS_END = WS_FY + 256 * 4096;
constexpr int AUX_NT = 2, AUX_SC1 = 16;
constexpr u32 TM = 0xfffu;
constexpr u32 SPIN_CAP = 20000;

constexpr int XN_STR = 1032;
constexpr int A_STR = 136;
constexpr int RG_STR = 68;
constexpr int R_IS = 288, R_GS = 8 * R_IS, R_WS = 2 * R_GS;
constexpr int L_CNT = 0;
constexpr int L_XN = 128;
constexpr int L_QS = L_XN + 8 * XN_STR * 2;
constexpr int L_ST = L_QS + 8 * A_STR * 2;
constexpr int L_F = L_ST + 512;
constexpr int L_F2 = L_F + 256;
constexpr int L_P = L_F2 + 2048;
constexpr int L_RG = L_P + 8192;
constexpr int L_RO = L_RG + 16 * 8 * RG_STR * 4;
constexpr int L_RY = L_RO + 8 * R_WS;
constexpr int L_A = L_RY + 4 * R_WS;
constexpr int L_OW = L_A + 8 * A_STR * 2;
constexpr int L_END = L_OW + 32768;
enum { C_XN = 0, C_RQ, C_QS, C_ST, C_RG, C_VI, C_DI, C_F, C_RO, C_M4, C_Y, C_OW, C_A, C_S5, C_TZ, C_P = 16, L_EP = 29, L_TZ0 = 30, L_TZ1 = 31 };

#define PIN() do { asm volatile("" ::: "memory"); __builtin_amdgcn_sched_barrier(0); } while (0)
#define USE4(x) asm volatile("" ::"v"(x))
#define BAR() do { asm volatile("s_waitcnt lgkmcnt(0)\n\ts_barrier" ::: "memory"); __builtin_amdgcn_sched_barrier(0); } while (0)

__device__ __forceinline__ f32x4 mfma32(u32x4 a, u32x4 b, f32x4 c) {
  return __builtin_amdgcn_mfma_f32_16x16x32_bf16(__builtin_bit_cast(bf16x8, a), __builtin_bit_cast(bf16x8, b), c, 0, 0, 0);
}
__device__ __forceinline__ f32x4 mfma16(u32x2 a, u32x2 b, f32x4 c) {
  return __builtin_amdgcn_mfma_f32_16x16x16bf16_1k(__builtin_bit_cast(s16x4, a), __builtin_bit_cast(s16x4, b), c, 0, 0, 0);
}
__device__ __forceinline__ float asf(u32 x) { return __builtin_bit_cast(float, x); }
__device__ __forceinline__ u32 asu(float x) { return __builtin_bit_cast(u32, x); }
__device__ __forceinline__ float bf_lo(u32 x) { return asf(x << 16); }
__device__ __forceinline__ float bf_hi(u32 x) { return asf(x & 0xffff0000u); }
__device__ __forceinline__ u32 pack_bf(float lo, float hi) {
  typedef __bf16 bf16x2 __attribute__((ext_vector_type(2)));
  typedef float f32x2 __attribute__((ext_vector_type(2)));
  f32x2 v = {lo, hi};
  return __builtin_bit_cast(u32, __builtin_convertvector(v, bf16x2));
}
__device__ __forceinline__ float rcp(float x) { return __builtin_amdgcn_rcpf(x); }
constexpr float L2E = 1.4426950408889634f;
constexpr int OOB = 0x7ffffff0;
__device__ __forceinline__ float sigm(float x) { return rcp(1.f + __builtin_amdgcn_exp2f(-L2E * x)); }
__device__ __forceinline__ float rbf(float x) { return bf_lo(pack_bf(x, x)); }
__device__ __forceinline__ u32 perm_lo(u32 a, u32 b) { return __builtin_amdgcn_perm(b, a, 0x05040100u); }
__device__ __forceinline__ u32 perm_hi(u32 a, u32 b) { return __builtin_amdgcn_perm(b, a, 0x07060302u); }
template <int CTRL> __device__ __forceinline__ float dpp(float x) {
  return asf(__builtin_amdgcn_mov_dpp(asu(x), CTRL, 0xf, 0xf, true));
}
template <int BANK> __device__ __forceinline__ float ror8_merge(float old, float src) {
  return asf(__builtin_amdgcn_update_dpp(asu(old), asu(src), 0x128, 0xf, BANK, false));
}
constexpr int DPP_X1 = 0xB1, DPP_X2 = 0x4E, DPP_HMIR = 0x141, DPP_MIR = 0x140, DPP_ROR8 = 0x128, DPP_ROR4 = 0x124;
__device__ __forceinline__ void swap32(float& a, float& b) {
  auto r = __builtin_amdgcn_permlane32_swap(asu(a), asu(b), false, false);
  a = asf(r[0]); b = asf(r[1]);
}
__device__ __forceinline__ void swap16(float& a, float& b) {
  auto r = __builtin_amdgcn_permlane16_swap(asu(a), asu(b), false, false);
  a = asf(r[0]); b = asf(r[1]);
}
__device__ __forceinline__ float add32(float x) { float a = x, b = x; swap32(a, b); return a + b; }
__device__ __forceinline__ float add16(float x) { float a = x, b = x; swap16(a, b); return a + b; }
__device__ __forceinline__ float max32(float x) { float a = x, b = x; swap32(a, b); return fmaxf(a, b); }
__device__ __forceinline__ float max16(float x) { float a = x, b = x; swap16(a, b); return fmaxf(a, b); }
__device__ __forceinline__ float wave_sum(float x) {
  x += dpp<DPP_X1>(x); x += dpp<DPP_X2>(x); x += dpp<DPP_HMIR>(x); x += dpp<DPP_MIR>(x);
  return add32(add16(x));
}
__device__ __forceinline__ float wave_max(float f) {
  f = fmaxf(f, dpp<DPP_X1>(f)); f = fmaxf(f, dpp<DPP_X2>(f)); f = fmaxf(f, dpp<DPP_HMIR>(f)); f = fmaxf(f, dpp<DPP_MIR>(f));
  return max32(max16(f));
}
__device__ __forceinline__ u32x4 ldnt(rsrc_t r, int voff, int soff) { return __builtin_amdgcn_raw_buffer_load_b128(r, voff, soff, AUX_NT); }
__device__ __forceinline__ u32x4 ldsc(rsrc_t r, int voff, int soff) { return __builtin_amdgcn_raw_buffer_load_b128(r, voff, soff, AUX_SC1); }
__device__ __forceinline__ rsrc_t mkr(const void* p, int bytes) { return __builtin_amdgcn_make_buffer_rsrc((void*)p, 0, bytes, 0x00020000); }

struct Tags { u32 t[4]; };
__device__ __forceinline__ int win(int o) { return o + (o >= 256 ? 256 : 0); }
__device__ __forceinline__ void publish(rsrc_t r, int voff, f32x4 v, const Tags& tg, u32 tm = TM, bool nt = false) {
  u32x4 o;
#pragma unroll
  for (int i = 0; i < 4; ++i) o[i] = ((asu(v[i]) + (tm + 1) / 2) & ~tm) | tg.t[i];
  if (nt) __builtin_amdgcn_raw_buffer_store_b128(o, r, voff, 0, AUX_SC1 | AUX_NT);
  else __builtin_amdgcn_raw_buffer_store_b128(o, r, voff, 0, AUX_SC1);
}
__device__ __forceinline__ Tags qtags(u32 e) {
  Tags t;
  t.t[0] = e & 15; t.t[1] = (e + 1 + ((e >> 4) & 7)) & 15; t.t[2] = (e + (e >> 7)) & 15; t.t[3] = (e + (e >> 11)) & 15;
  return t;
}
__device__ __forceinline__ void strip(u32x4& m, const Tags& tg) {
#pragma unroll
  for (int i = 0; i < 4; ++i) m[i] ^= tg.t[i];
}
__device__ __forceinline__ u32 or4(const u32x4& m) { return (m[0] | m[1]) | (m[2] | m[3]); }

struct Params {
  const u16* x; const u16* kc; const u16* vc; const u16* wq; const u16* wo;
  const u16* wg; const u16* wu; const u16* wd;
  u16* out; char* ws;
};
constexpr int P_STEP16 = 57, P_LEAD = 5, PO = 360, SO = 4, PG3 = 352, PD = 396, SD = 7, P4 = 432, PF = 475, MODE = 1159, PT = 12846, TK0 = 305, SK16 = 57, PQ = 0, PS = 0, TW0 = 97, SW16 = 57, PV = 228, X18 = 22, X19 = 48, X20 = 0, ABL = 0, DB = 0, X23 = 0;
#define PACE(pos) PACET((((pos) - P_LEAD) * P_STEP16) >> 4)
#define PACET(t) do { PIN(); const int due_ = (t); while ((int)((u32)__builtin_amdgcn_s_memrealtime() - tz) < due_) __builtin_amdgcn_s_sleep(1); PIN(); } while (0)

template <int STR> __device__ __forceinline__ void store64(f32x4 (&d)[8], float* red, int g, int c) {
  f32x4 s[4];
#pragma unroll
  for (int i = 0; i < 4; ++i)
#pragma unroll
    for (int r = 0; r < 4; ++r) {
      float a = d[i][r], b = d[i + 4][r];
      swap32(a, b);
      s[r][i] = a + dpp<DPP_ROR8>(b);
    }
  if (c < 8) {
#pragma unroll
    for (int r = 0; r < 4; ++r) *(f32x4*)(red + (4 * (g & 1) + r) * STR + 8 * c + 4 * (g >> 1)) = s[r];
  }
}
__device__ __forceinline__ void store_acc(f32x4 (&d)[8], char* red, int g, int c) {
  if (g < 2) {
#pragma unroll
    for (int i = 0; i < 8; ++i) *(f32x4*)(red + g * R_GS + i * R_IS + c * 16) = d[i];
  }
}
__device__ __forceinline__ void tr_pair(const u32x4* w, u32x4 (&f)[8], int jj) {
#pragma unroll
  for (int i = 0; i < 8; ++i) {
    const u32 lo = w[2 * jj][i >> 1], hi = w[2 * jj + 1][i >> 1];
    f[i][jj] = (i & 1) ? perm_hi(lo, hi) : perm_lo(lo, hi);
  }
}
__device__ __forceinline__ void merge_stats(float m, float l, float& M, float& L) {
  M = fmaxf(m, dpp<DPP_ROR8>(m));
  M = max32(max16(M));
  L = l * __builtin_amdgcn_exp2f((m - M) * L2E);
  L += dpp<DPP_ROR8>(L);
  L = add32(add16(L));
}

#define ARRIVE(i) do { PIN(); asm volatile("s_mov_b64 exec, 1\n\tds_add_u32 %0, %1\n\ts_mov_b64 exec, -1" :: "v"(lcnt + 4 * (i)), "v"(1u) : "memory"); PIN(); } while (0)
#define PINV(a, n) do { _Pragma("unroll") for (int i_ = 0; i_ < (n); ++i_) asm volatile("" : "+v"((a)[i_])); } while (0)
#define LWAITS(i, n, SL) do { PIN(); u32 it_ = 0; \
    while (__builtin_amdgcn_readfirstlane(__hip_atomic_load((u32*)(lds + L_CNT) + (i), __ATOMIC_RELAXED, __HIP_MEMORY_SCOPE_WORKGROUP)) < (u32)(n) && ++it_ < (1u << 22)) { SL; } \
    PIN(); } while (0)
#define LWAIT(i, n) LWAITS(i, n, __builtin_amdgcn_s_sleep(1))
#define LWAITF(i, n) LWAITS(i, n, )
#define GETTZ() GETT(L_TZ1)
#define GETT(i) do { PIN(); tz = __builtin_amdgcn_readfirstlane(__hip_atomic_load((u32*)(lds + L_CNT) + (i), __ATOMIC_RELAXED, __HIP_MEMORY_SCOPE_WORKGROUP)); PIN(); } while (0)
#define LATE() do { if (DB) { const u32 tb_ = (u32)__builtin_amdgcn_s_memrealtime() - DB; if ((int)(tb_ - tz) > 0) tz = tb_; PIN(); } } while (0)
#define PRIO(n) do { __builtin_amdgcn_s_setprio(n); PIN(); } while (0)
#define MKTAGS() Tags tg; const u32 e_ = __builtin_amdgcn_readfirstlane(__hip_atomic_load((u32*)(lds + L_CNT) + L_EP, __ATOMIC_RELAXED, __HIP_MEMORY_SCOPE_WORKGROUP)) + 1u; { _Pragma("unroll") for (int i_ = 0; i_ < 4; ++i_) tg.t[i_] = (e_ + 0x4d5u * i_) & TM; }
#define RETRY(m, n, voff, soff) do { \
    for (u32 spin_ = 0;; ++spin_) { asm volatile("; check" ::: "memory"); u32 bad_ = 0; \
      _Pragma("unroll") for (int k = 0; k < (n); ++k) { strip(m[k], tg); bad_ |= or4(m[k]); } \
      if ((bad_ & TM) == 0 || spin_ >= SPIN_CAP) break; \
      _Pragma("unroll") for (int k = 0; k < (n); ++k) m[k] = ldsc(rws, (voff), (soff)); \
    } PIN(); } while (0)

#define GATHER(m, n, voff, soff) do { u32 spin_ = 0, bad_; \
    do { asm volatile("; gather" ::: "memory"); bad_ = 0; \
      _Pragma("unroll") for (int k = 0; k < (n); ++k) m[k] = ldsc(rws, (voff), (soff)); \
      _Pragma("unroll") for (int k = 0; k < (n); ++k) { strip(m[k], tg); bad_ |= or4(m[k]); } \
    } while ((bad_ & TM) != 0 && ++spin_ < SPIN_CAP); PIN(); } while (0)

#define CPF 1
#define CPFX 0
#define CPFS 128
#define CPFN 256
#define CPFA 0
#define CLEN 18816
#define TZM 2
#define OM 1
#define OA 0
#define EE 0
#define NB 1
#define XP 0
#define LNP 3
#define EXPW 1

typedef uint64_t u64;
typedef u64 u64x8 __attribute__((ext_vector_type(8)));
typedef u64 u64x2 __attribute__((ext_vector_type(2)));
__device__ __forceinline__ char* ptr1(u64 a) { return (char*)(__attribute__((address_space(1))) char*)a; }
__device__ __forceinline__ int colgrp(int wg, int i) {
  const u32 f = wg < 198 ? wg : wg - 198 + 58 * i, a = wg - 154;
  u32 n0 = f + (((f + 4) * 147) >> 10), n1 = ((a << 2) & ~7u) | 3;
  asm("" : "+s"(n0), "+s"(n1));
  return a < 44u ? n1 : n0;
}
#define COMMON() \
  int wg = blockIdx.x, tid = threadIdx.x; \
  asm volatile("" : "+s"(wg)); \
  asm volatile("" : "+v"(tid)); \
  __builtin_assume(wg >= 0 && wg < NWG && tid >= 0 && tid < NT); \
  const int lane = tid & 63, g = lane >> 4, c = lane & 15, hi = lane >> 5; \
  const int grp = (wg & 7) * 4 + (wg >> 6), sub = (wg >> 3) & 7; \
  Params p = pa; \
  p.x = (const u16*)ptr1(ka[0]); p.kc = (const u16*)ptr1(ka[1]); p.vc = (const u16*)ptr1(ka[2]); p.wq = (const u16*)ptr1(ka[3]); \
  p.wo = (const u16*)ptr1(ka[4]); p.wg = (const u16*)ptr1(ka[5]); p.wu = (const u16*)ptr1(ka[6]); p.wd = (const u16*)ptr1(ka[7]); \
  p.out = (u16*)ptr1(kc[0]); p.ws = ptr1(kc[1]); \
  const int gw = grp, sO = (sub + 1) & 7, sD = (sub + 3) & 7; \
  const int w = wave & 7, wv = wave & 3; \
  const rsrc_t rws = mkr(p.ws, WS_END); \
  const u32 lcnt = (u32)(uintptr_t)(__attribute__((address_space(3))) char*)lds + L_CNT; \
  const size_t kvoff = (size_t)grp * SK * DH + (((sub << (PT & 31)) + (w << ((PT >> 5) & 31))) >> 1); \
    \
  const int rot = (MODE & 2) ? (w + sub) & 3 : 0; \
  int kb[4]; \
  _Pragma("unroll") for (int i = 0; i < 4; ++i) kb[i] = ((i + rot) & 3) << (PT >> 10)

__global__ void __launch_bounds__(NT, 4) fused_kernel(Params pa) {
  __shared__ __attribute__((aligned(16))) char lds[L_END];
  u64 pc;
  asm volatile("s_load_dword s100, %1, 0x0\n\ts_load_dword s101, %1, 0x40\n\ts_getpc_b64 %0"
               : "=s"(pc) : "s"(__builtin_amdgcn_kernarg_segment_ptr()) : "s100", "s101");
  const u32 cpf = __builtin_amdgcn_raw_buffer_load_b32(mkr(ptr1(pc), CLEN), threadIdx.x * 128, 0, 0);
  __builtin_amdgcn_sched_barrier(0);
  const int wave = (int)((u32)__builtin_amdgcn_readfirstlane(threadIdx.x) >> 6);
  u32 tz;
  if (EXPW ? __builtin_expect(wave < 8, 1) : wave < 8) {
    u64x8 ka; u64x2 kc;
    asm volatile("s_load_dwordx16 %0, %2, 0x0\n\ts_load_dwordx4 %1, %2, 0x40" : "=&s"(ka), "=&s"(kc) : "s"(__builtin_amdgcn_kernarg_segment_ptr()));
    PRIO(3);
    u32x4 wqr[4], gr[8], vr[16];
    int r0;
    {
      const int wg = blockIdx.x, tid = threadIdx.x;
      int n = colgrp(wg, 0);
      r0 = ((u32)(wg - 154) < 44u ? (wg & 1) * 512 : wg < 198 ? 0 : 768) + wave * 32;
      const int sk = n < 64 ? HQD / 512 : FF / 512;
      u32 off = (r0 * sk + ((u32)(n - (n < 64 ? 0 : n < 120 ? 64 : 120)) >> 3)) << 10;
      int st = sk << 10, st2 = sk << 11, st3 = 3 * st, nr = 1 << 18, fl = 0x00020000, mk = 0x380;
      int vo = ((tid & 8) * 2 + ((tid >> 2) & 12)) * st + (tid & 7) * 16, vk = n * 128 + 127;
      u32x2 t1;
      asm volatile("" : "+v"(vo), "+v"(vk));
      asm volatile("s_waitcnt lgkmcnt(0)\n\ts_memrealtime %2"
                   : "+s"(ka), "+s"(kc), "=&s"(t1), "+s"(n), "+s"(st), "+s"(off), "+s"(st2), "+s"(st3), "+s"(nr), "+s"(fl), "+s"(mk));
      const u64 a = n < 64 ? ka[3] : n < 120 ? ka[5] : ka[6];
      const rsrc_t r = __builtin_amdgcn_make_buffer_rsrc(ptr1(a) + off, 0, nr, fl);
      vo |= (vk - (u32)a) & mk;
      wqr[0] = ldnt(r, vo, 0); wqr[1] = ldnt(r, vo, st); wqr[2] = ldnt(r, vo, st2); wqr[3] = ldnt(r, vo, st3);
      PIN();
      asm volatile("s_waitcnt lgkmcnt(0)" : "+s"(t1));
      tz = t1[0];
    }
    COMMON();
    const u32 ep = __builtin_amdgcn_raw_buffer_load_b32(rws, w == 0 ? 0 : OOB, 0, AUX_SC1);
    PIN();
    if (tid < 32) ((u32*)(lds + L_CNT))[tid] = tid >= L_TZ0 ? tz : 0;
    const bool ta = wg < 198, th = ta && wg >= 154;
    int cgv[3];
#pragma unroll
    for (int i = 0; i < 3; ++i) {
      const int n = colgrp(wg, i), sh = (int)((uintptr_t)(n < 64 ? p.wq : n < 120 ? p.wg : p.wu) >> 7);
      cgv[i] = (th ? i < 2 : n < 176) ? (n & ~7) | ((n - sh) & 7) : -1;
    }
    const int rbase = th ? (wg & 1) * 512 : ta ? 0 : 768;
    const int r1 = ta ? (rbase + 256 + w * 64) & 1023 : 768 + wv * 64;
    {
      const int cg = w < 4 ? cgv[1] : cgv[2], st = cg < 64 ? HQD * 2 : FF * 2;
      u64 a = (u64)(uintptr_t)((const char*)(cg < 64 ? p.wq : cg < 120 ? p.wg : p.wu) + r1 * st + (cg - (cg < 64 ? 0 : cg < 120 ? 64 : 120)) * 128);
      int nr = cg < 0 ? 0 : 63 * st + 128, vo = (32 * (c >> 3) + 8 * g) * st + (c & 7) * 16;
      asm volatile("" : "+s"(a), "+s"(nr));
      asm volatile("" : "+v"(vo));
      const rsrc_t r = mkr(ptr1(a), nr);
#pragma unroll
      for (int j = 0; j < 8; ++j) { PACET(TW0 + (((j * 7 + w) * SW16) >> 4)); gr[j] = ldnt(r, vo, j * st); }
    }
    BAR();
    GETTZ();
    PIN();
    USE4(cpf);
    if (w == 0) ((u32*)(lds + L_CNT))[L_EP] = ep;
    u32x2 fq[8];
#pragma unroll
    for (int i = 0; i < 8; ++i)
#pragma unroll
      for (int jj = 0; jj < 2; ++jj) {
        const u32 lo = wqr[2 * jj][i >> 1], hh = wqr[2 * jj + 1][i >> 1];
        fq[i][jj] = (i & 1) ? perm_hi(lo, hh) : perm_lo(lo, hh);
      }
    PINV(fq, 8);
    {
      const u32x2 a0 = *(const u32x2*)(lds + L_XN + ((c & 7) * XN_STR + r0 + 16 * (c >> 3) + 4 * g) * 2);
      const u32x4 a1 = *(const u32x4*)(lds + L_XN + ((c & 7) * XN_STR + r1 + 32 * (c >> 3) + 8 * g) * 2);
      f32x4 d[8];
#pragma unroll
      for (int i = 0; i < 8; ++i) d[i] = mfma16(a0, fq[i], f32x4{0, 0, 0, 0});
      if (!ta) {
        store64<RG_STR>(d, (float*)(lds + L_RG) + (8 + w) * 8 * RG_STR, g, c);
#pragma unroll
        for (int i = 0; i < 8; ++i) d[i] = f32x4{0, 0, 0, 0};
      }
      u32x4 f[8];
#pragma unroll
      for (int jj = 0; jj < 4; ++jj) tr_pair(gr, f, jj);
#pragma unroll
      for (int i = 0; i < 8; ++i) d[i] = mfma32(a1, f[i], d[i]);
      store64<RG_STR>(d, (float*)(lds + L_RG) + w * 8 * RG_STR, g, c);
    }
    ARRIVE(C_RG);
    LWAIT(C_RG, 8);
    MKTAGS();
    if (w == 7) {
      const u32 t20 = tz << 12;
      __builtin_amdgcn_raw_buffer_store_b128(u32x4{t20 | tg.t[0], t20 | tg.t[1], t20 | tg.t[2], t20 | tg.t[3]}, rws,
                                             (lane == 0 && !(wg & 24) && !(MODE & 16)) ? WS_T + ((wg >> 5) * 8 + (wg & 7)) * 16 : OOB, 0, AUX_SC1);
    }
    if (w < (ta ? 2 : 6)) {
      const int t = w >> 1, cg = t == 0 ? cgv[0] : t == 1 ? cgv[1] : cgv[2], l = (w & 1) * 64 + lane;
      const bool isq = cg < 64;
      const int b = isq ? l >> 4 : l & 7, cq = isq ? l & 15 : (l >> 5) * 4 + ((l >> 3) & 1) * 2 + ((l >> 4) & 1);
      const float* rg = (const float*)(lds + L_RG) + ((t == 0 ? (ta ? 0 : 8) : 4 * t - 4) * 8 + b) * RG_STR + cq * 4;
      f32x4 s = *(const f32x4*)rg;
#pragma unroll
      for (int k = 1; k < 4; ++k) s += *(const f32x4*)(rg + k * 8 * RG_STR);
      if (t == 0) {
#pragma unroll
        for (int k = 4; k < 8; ++k) s += *(const f32x4*)(rg + k * 8 * RG_STR);
      }
      const int slot = rbase != 0, gu = cg >= 120;
      const int aq = WS_Q + (b * 32 + (cg >> 1)) * 2048 + win(slot * 512 + (cg & 1) * 256 + cq * 16);
      const int ag = WS_GU + ((cg - (gu ? 120 : 64)) * 4 + (l >> 5)) * 4096 + slot * 2048 + win(((l >> 3) & 3) * 128 + gu * 512 + b * 16);
      if (PS & 1023) {
        const u32 now = (u32)__builtin_amdgcn_s_memrealtime(), lo = tz + (PS >> 10);
        const u32 tgt = (((int)(lo - now) > 0 ? lo : now) + (PS & 1023) - 1) & ~(u32)((PS & 1023) - 1);
        USE4(s);
        PACET((int)(tgt - tz));
      }
      publish(rws, cg < 0 ? OOB : isq ? aq : ag, s, isq ? qtags(e_) : tg, isq ? 15u : TM, isq ? (MODE & 4096) : (MODE & 8192));
    }
    GETTZ();
    PRIO(0);
    const rsrc_t rv = mkr(p.vc + kvoff, (3 << (PT >> 10)) + 4096);
#pragma unroll
    for (int n = 0; n < 16; ++n) { if (n >= 3 && n <= 9) GETTZ(); PACE(PV + n * 8 + w); vr[n] = ldnt(rv, lane * 16 + (n & 3) * 1024, kb[n >> 2]); }
    ARRIVE(C_VI);
    PRIO(3);
    u32x4 vt[8];
#pragma unroll
    for (int jj = 0; jj < 4; ++jj) tr_pair(vr, vt, jj);
    PINV(vt, 8);
    LWAIT(C_P + w, 1);
    f32x4 acc[8];
    {
      const u32x4 pf = *(const u32x4*)(lds + L_P + w * 1024 + lane * 16);
#pragma unroll
      for (int i = 0; i < 8; ++i) acc[i] = mfma32(pf, vt[i], f32x4{0, 0, 0, 0});
    }
    PIN();
    const u32x4 pf1 = *(const u32x4*)(lds + L_P + w * 1024 + (lane ^ 8) * 16);
#pragma unroll
    for (int jj = 0; jj < 4; ++jj) { tr_pair(vr + 8, vt, jj); PIN(); }
#pragma unroll
    for (int i = 0; i < 8; ++i) acc[i] = mfma32(pf1, vt[i], acc[i]);
    store_acc(acc, lds + L_RO + w * R_WS, g, c);
    ARRIVE(C_RO);
    LWAIT(C_F, 1);
    LWAIT(C_RO, 8);
    {
      const int g2 = w >> 2, i = lane & 7, cl = (lane >> 3) & 3;
      const char* ro = lds + L_RO + hi * 4 * R_WS + g2 * R_GS + i * R_IS + (wv * 4 + cl) * 16;
      const char* fp = lds + L_F + hi * 128 + g2 * 16;
      f32x4 o = *(const f32x4*)ro * *(const f32x4*)fp;
#pragma unroll
      for (int k = 1; k < 4; ++k) o += *(const f32x4*)(ro + k * R_WS) * *(const f32x4*)(fp + k * 32);
#pragma unroll
      for (int r = 0; r < 4; ++r) o[r] = add32(o[r]);
      publish(rws, hi ? OOB : WS_O + (((grp & 3) * 2 + g2) * 4 + wv) * 65536 + (grp >> 2) * 8192 + (sub >> 1) * 2048 + win((sub & 1) * 512 + lane * 16), o, tg, TM, MODE & 16384);
    }
    ARRIVE(C_M4);
    u32x4 sm[1];
    const bool sv = ((lane >> 2) & 1) == ((gw >> 2) & 1);
    const int stoff = sv ? WS_ST + (w * 4 + (gw >> 3)) * 2048 + win(lane * 16) : OOB;
    sm[0] = ldsc(rws, stoff, 0);
    LWAIT(C_M4, 8);
    GETT((MODE & 32) ? L_TZ1 : L_TZ0);
    PACE(P4);
    if (MODE & 128) LWAITF(C_DI, 4);
    u32x4 om[4], bw[4];
    const int ooff = WS_O + gw * 65536 + w * 8192 + win(lane * 16);
#pragma unroll
    for (int k = 0; k < 4; ++k) om[k] = ldsc(rws, ooff, k * 2048);
    if (!sv) sm[0] = u32x4{tg.t[0], tg.t[1], tg.t[2], tg.t[3]};
    strip(sm[0], tg);
    for (u32 spin = 0; __builtin_expect(__builtin_amdgcn_ballot_w64((or4(sm[0]) & TM) != 0) != 0, 0) && spin < SPIN_CAP; ++spin) {
      if (sv) { sm[0] = ldsc(rws, stoff, 0); strip(sm[0], tg); }
    }
    f32x4 fs[4];
    {
      float M, L;
      merge_stats(asf(sm[0][0]), asf(sm[0][1]) + asf(sm[0][2]), M, L);
      *(float*)(lds + L_F2 + w * 256 + lane * 4) = __builtin_amdgcn_exp2f((asf(sm[0][0]) - M) * L2E) * rcp(L);
      PIN();
#pragma unroll
      for (int k = 0; k < 4; ++k) fs[k] = *(const f32x4*)(lds + L_F2 + w * 256 + (2 * k + hi) * 32 + ((gw >> 2) & 1) * 16);
    }
    LWAIT(C_OW, 4);
#pragma unroll
    for (int k = 0; k < 4; ++k) bw[k] = *(const u32x4*)(lds + L_OW + k * 8192 + w * 1024 + lane * 16);
    PINV(bw, 4);
    PINV(fs, 4);
    RETRY(om, 4, ooff, k * 2048);
    {
      float v[4];
#pragma unroll
      for (int i = 0; i < 4; ++i) v[i] = (asf(om[0][i]) * fs[0][i] + asf(om[1][i]) * fs[1][i]) + (asf(om[2][i]) * fs[2][i] + asf(om[3][i]) * fs[3][i]);
      swap32(v[0], v[2]); swap32(v[1], v[3]);
      *(u32*)(lds + L_A + (w * A_STR + (lane & 31) * 4 + 2 * hi) * 2) = pack_bf(v[0] + v[2], v[1] + v[3]);
    }
    ARRIVE(C_A);
    LWAITF(C_A, 8);
    {
      u32x4 a[4];
#pragma unroll
      for (int k = 0; k < 4; ++k) a[k] = *(const u32x4*)(lds + L_A + ((c & 7) * A_STR + 32 * k + 8 * g) * 2);
      f32x4 d0 = mfma32(a[0], bw[0], f32x4{0, 0, 0, 0}), d1 = mfma32(a[1], bw[1], f32x4{0, 0, 0, 0});
      d0 = mfma32(a[2], bw[2], d0); d1 = mfma32(a[3], bw[3], d1);
      publish(rws, hi ? OOB : WS_FS + (((2 * sO + (c >> 3)) * 2 + g) * 8 + w) * 4096 + gw * 128 + (c & 7) * 16, d0 + d1, tg);
    }
    ARRIVE(C_S5);
  } else {
    u64x8 ka; u64x2 kc;
    asm volatile("s_load_dwordx16 %0, %2, 0x0\n\ts_load_dwordx4 %1, %2, 0x40\n\ts_waitcnt lgkmcnt(0)"
                 : "=&s"(ka), "=&s"(kc) : "s"(__builtin_amdgcn_kernarg_segment_ptr()));
    PRIO(LNP);
    COMMON();
    const u32x4 xa = *(const u32x4*)(p.x + w * DM + lane * 8);
    const u32x4 xb = *(const u32x4*)(p.x + w * DM + 512 + lane * 8);
    const int fb = OM ? grp * 8 + sub : wg;
    const int oidx = (4 * ((fb >> 3) & 1) + g) * DM + (fb >> 4) * 64 + (c & 7) * 8 + (fb & 7);
    const u32 xfin = p.x[oidx];
    PIN();
    {
      float v[16];
#pragma unroll
      for (int i = 0; i < 4; ++i) { v[2 * i] = bf_lo(xa[i]); v[2 * i + 1] = bf_hi(xa[i]); v[8 + 2 * i] = bf_lo(xb[i]); v[9 + 2 * i] = bf_hi(xb[i]); }
      float s = 0.f;
#pragma unroll
      for (int i = 0; i < 16; ++i) s += v[i];
      const float mean = wave_sum(s) * (1.f / DM);
      float q = 0.f;
#pragma unroll
      for (int i = 0; i < 16; ++i) { const float d = v[i] - mean; q += d * d; }
      const float var = rbf(wave_sum(q) * (1.f / DM));
      const float mb = rbf(mean);
      const float rs = rcp(rbf(__builtin_amdgcn_sqrtf(rbf(var + 1e-5f))));
      u32x4 o[2];
#pragma unroll
      for (int i = 0; i < 8; ++i) {
        const u32 d = pack_bf(v[2 * i] - mb, v[2 * i + 1] - mb);
        o[i >> 2][i & 3] = pack_bf(bf_lo(d) * rs, bf_hi(d) * rs);
      }
      *(u32x4*)(lds + L_XN + (w * XN_STR + lane * 8) * 2) = o[0];
      *(u32x4*)(lds + L_XN + (w * XN_STR + 512 + lane * 8) * 2) = o[1];
    }
    USE4(cpf);
    BAR();
    GETTZ();
    PRIO(2);
    const rsrc_t rk = mkr(p.kc + kvoff, (3 << (PT >> 10)) + 4096);
    const int kvo = (4 * (c & 3) + ((c >> 2) & 1)) * 256 + (4 * (c >> 3) + g) * 16;
    u32x4 kr[16];
#pragma unroll
    for (int n = 0; n < 16; ++n) { if (!(n & 1)) PACET(TK0 + (((n * 8 + w * 2) * SK16) >> 4) + (n * 8 + w * 2 >= X19 ? X18 : 0)); kr[n] = ldnt(rk, kvo + ((n >> 1) & 1) * 512 + (n & 1) * 128, kb[n >> 2]); }
    PIN();
    PRIO(3);
    u32x4 qm[1];
    const int qoff = WS_Q + ((grp >> 2) * 32 + (grp & 3) * 8 + w) * 2048 + win(lane * 16);
    PACET(PQ);
    qm[0] = ldsc(rws, qoff, 0);
    u32x4 tm = ldsc(rws, (w == 7 && !(MODE & 16)) ? WS_T + lane * 16 : OOB, 0);
    PIN();
    LWAIT(C_RG, 8);
    MKTAGS();
    {
      const Tags tq = qtags(e_);
      strip(qm[0], tq);
      for (u32 spin = 0; __builtin_expect(__builtin_amdgcn_ballot_w64((or4(qm[0]) & 15u) != 0) != 0, 0) && spin < SPIN_CAP; ++spin) {
        qm[0] = ldsc(rws, qoff, 0);
        strip(qm[0], tq);
      }
    }
    {
      float q0 = asf(qm[0][0]), q1 = asf(qm[0][1]), q2 = asf(qm[0][2]), q3 = asf(qm[0][3]);
      swap32(q0, q2); swap32(q1, q3);
      *(u32*)(lds + L_QS + (w * A_STR + (lane & 31) * 4 + 2 * hi) * 2) = pack_bf(q0 + q2, q1 + q3);
    }
    if (w == 7) {
      strip(tm, tg);
      int d = (int)(tm[0] - (tz << 12)) >> 12;
      const bool ok = (or4(tm) & 0xfffu) == 0 && d >= -150 && d <= 150 && (MODE & 4);
      if (!ok) d = 0;
      if (MODE & 64) {
        const int n = __builtin_popcountll(__builtin_amdgcn_ballot_w64(ok));
        const u32 tc = tz + (int)(wave_sum((float)d) * rcp((float)(n + !n)));
        ((u32*)(lds + L_CNT))[L_TZ0 + (lane & 1)] = tc;
      } else if (MODE & 256) {
        const int n = __builtin_popcountll(__builtin_amdgcn_ballot_w64(ok));
        const int dm = (int)(wave_sum((float)d) * rcp((float)(n + !n))) + X20;
        ((u32*)(lds + L_CNT))[L_TZ0 + (lane & 1)] = tz + ((lane & 1) ? min(dm, 0) : dm);
      } else {
        ((u32*)(lds + L_CNT))[L_TZ0] = tz + (int)wave_max((float)d);
      }
    }
    ARRIVE(C_QS);
    LWAIT(C_QS, 8);
    {
      const u32x4 q0 = *(const u32x4*)(lds + L_QS + ((c & 7) * A_STR + 32 * (c >> 3) + 8 * g) * 2);
      const u32x4 q1 = *(const u32x4*)(lds + L_QS + ((c & 7) * A_STR + 64 + 32 * (c >> 3) + 8 * g) * 2);
      float sc[8];
#pragma unroll
      for (int e = 0; e < 2; ++e) {
        f32x4 t[4];
#pragma unroll
        for (int ua = 0; ua < 4; ++ua) t[ua] = mfma32(kr[(ua >> 1) * 8 + e * 4 + (ua & 1) * 2], q0, f32x4{0, 0, 0, 0});
#pragma unroll
        for (int ua = 0; ua < 4; ++ua) t[ua] = mfma32(kr[(ua >> 1) * 8 + e * 4 + (ua & 1) * 2 + 1], q1, t[ua]);
#pragma unroll
        for (int r = 0; r < 4; ++r) {
          float a = t[0][r], b = t[1][r], a2 = t[2][r], b2 = t[3][r];
          swap32(a, b); swap32(a2, b2);
          sc[e * 4 + r] = ror8_merge<0x3>(b2, b) + ror8_merge<0xc>(a, a2);
        }
      }
      float mx = -INFINITY;
#pragma unroll
      for (int i = 0; i < 4; ++i) {
        const u32 s1 = pack_bf(sc[2 * i], sc[2 * i + 1]);
        const u32 s2 = pack_bf(bf_lo(s1) * 0.08838834764831845f, bf_hi(s1) * 0.08838834764831845f);
        sc[2 * i] = bf_lo(s2); sc[2 * i + 1] = bf_hi(s2);
        mx = fmaxf(mx, fmaxf(sc[2 * i], sc[2 * i + 1]));
      }
      mx = fmaxf(mx, dpp<DPP_ROR8>(mx));
      const float mw = max32(max16(mx));
      float l = 0.f;
      u32x4 pf;
#pragma unroll
      for (int i = 0; i < 4; ++i) {
        const float e0 = __builtin_amdgcn_exp2f((sc[2 * i] - mw) * L2E), e1 = __builtin_amdgcn_exp2f((sc[2 * i + 1] - mw) * L2E);
        l += e0 + e1;
        pf[i] = pack_bf(e0, e1);
      }
      *(u32x4*)(lds + L_P + w * 1024 + lane * 16) = pf;
      ARRIVE(C_P + w);
      l += dpp<DPP_ROR8>(l);
      l = add32(add16(l));
      if (lane < 8) *(float2*)(lds + L_ST + (w * 8 + lane) * 8) = make_float2(mw, l);
    }
    ARRIVE(C_ST);
    u32x4 wr[8], wt[8];
    if (w >= 4) {
      if (MODE & 8) LWAIT(C_VI, 8);
      PACE(PG3 + wv);
      u32x4 gm[4];
      const int gbase = WS_GU + (gw * 7 + 2 * wv + (wv == 3 ? 0 : hi)) * 4096;
      const int dx = (MODE & 1024) ? wv : 0;
      const int goffa = gbase + (c >> 3) * 768 + (g & 1) * 128 + (c & 7) * 16, goffb = gbase + 512 + (c >> 3) * 512 + (g & 1) * 128 + (c & 7) * 16;
      const int goff0 = (dx & 2) ? goffb : goffa, goff1 = (dx & 2) ? goffa : goffb;
      GATHER(gm, 4, (k & 1) ? goff1 : goff0, (k >> 1) * 2048);
      u32x4 a;
#pragma unroll
      for (int i = 0; i < 4; ++i) {
        const int qp = i >> 1, e = (i & 1) * 2;
        const float v0 = asf(gm[qp][e]) + asf(gm[2 + qp][e]), v1 = asf(gm[qp][e + 1]) + asf(gm[2 + qp][e + 1]);
        const u32 gg = pack_bf(v0, v1);
        const u32 uu = asu(dpp<DPP_ROR8>(asf(gg)));
        const u32 sg = pack_bf(sigm(bf_lo(gg)), sigm(bf_hi(gg)));
        a[i] = pack_bf(bf_lo(sg) * bf_lo(uu), bf_hi(sg) * bf_hi(uu));
        if (wv == 3 && hi) a[i] = 0;
      }
      if (dx & 1) a = u32x4{a[1], a[0], a[3], a[2]};
      PINV(a, 4);
      GETTZ();
      PRIO(0);
      const rsrc_t rw = mkr(p.wd + (size_t)(gw * 112 + 32 * wv) * DM + sD * 128, (wv == 3 ? 16 : 32) * DM * 2 - sD * 256);
#pragma unroll
      for (int j = 0; j < 8; ++j) { PACE(PD + j * SD / 2 + wv); wr[j] = ldnt(rw, 8 * g * (DM * 2) + c * 16, (j ^ (2 * dx)) * DM * 2); }
      ARRIVE(C_DI);
      PRIO(3);
#pragma unroll
      for (int jj = 0; jj < 4; ++jj) { tr_pair(wr, wt, jj); PIN(); }
      f32x4 d[8];
#pragma unroll
      for (int i = 0; i < 8; ++i) d[i] = mfma32(a, wt[i], f32x4{0, 0, 0, 0});
      store_acc(d, lds + L_RY + wv * R_WS, g, c);
      ARRIVE(C_Y);
      LWAITF(C_Y, 4);
      {
        const int t = wv * 64 + lane;
        const char* rf = lds + L_RY + (t >> 7) * R_GS + ((t >> 3) & 7) * R_IS + (((t >> 6) & 1) * 8 + (t & 7)) * 16;
        f32x4 s = *(const f32x4*)rf;
#pragma unroll
        for (int k = 1; k < 4; ++k) s += *(const f32x4*)(rf + k * R_WS);
        publish(rws, WS_FY + (((2 * sD + ((t >> 6) & 1)) * 2 + (t >> 7)) * 8 + ((t >> 3) & 7)) * 4096 + gw * 128 + (t & 7) * 16, s, tg);
      }
      ARRIVE(C_S5);
    } else {
      if (w == 0) {
        LWAIT(C_ST, 8);
        const float2 ml = *(const float2*)(lds + L_ST + lane * 8);
        float M, L;
        merge_stats(ml.x, ml.y, M, L);
        const float Lh = asf((asu(L) + 0x800u) & 0xfffff000u);
        publish(rws, lane < 8 ? WS_ST + grp * 2048 + win(sub * 128) + lane * 16 : OOB, f32x4{M, Lh, L - Lh, 0.f}, tg);
        *(float*)(lds + L_F + lane * 4) = __builtin_amdgcn_exp2f((ml.x - M) * L2E);
        ARRIVE(C_F);
      }
      GETTZ();
      PRIO(0);
      const rsrc_t rw = mkr(p.wo + (size_t)((gw >> 2) * 512 + (gw & 3) * 32 + 8 * wv) * DM + sO * 128, 400 * DM * 2);
#pragma unroll
      for (int j = 0; j < 8; ++j) { if (j == 0) LWAIT(C_VI, 8); PACE(PO + j * SO + wv); wr[j] = ldnt(rw, 2 * g * (DM * 2) + c * 16, ((j & 3) * 128 + (j >> 2)) * DM * 2); }
      PIN();
      PRIO(3);
#pragma unroll
      for (int jj = 0; jj < 4; ++jj) { tr_pair(wr, wt, jj); PIN(); }
#pragma unroll
      for (int i = 0; i < 8; ++i) *(u32x4*)(lds + L_OW + wv * 8192 + i * 1024 + lane * 16) = wt[i];
      ARRIVE(C_OW);
      if (w == 0) {
      GETT(L_TZ0);
      LWAIT(C_S5, 12);
      PACE(PF);
      u32x4 fm[8];
      const int foff = WS_FS + fb * 4096 + lane * 16;
      GATHER(fm, 8, foff, (k >> 2) * (WS_FY - WS_FS) + (k & 3) * 1024);
      float z[2];
#pragma unroll
      for (int h = 0; h < 2; ++h) {
        float v[4];
#pragma unroll
        for (int i = 0; i < 4; ++i) v[i] = (asf(fm[4 * h][i]) + asf(fm[4 * h + 1][i])) + (asf(fm[4 * h + 2][i]) + asf(fm[4 * h + 3][i]));
        swap32(v[0], v[2]); swap32(v[1], v[3]);
        v[0] += v[2]; v[1] += v[3];
        swap16(v[0], v[1]);
        const float t = v[0] + v[1];
        z[h] = rbf(t + dpp<DPP_ROR8>(t));
      }
      z[1] = rbf(z[1] * sigm(z[1]));
      const float r = bf_lo(xfin) + rbf(z[1] * z[0]);
      if (c < 8) p.out[oidx] = (u16)pack_bf(r, r);
      if (wg == 0 && lane == 0) __builtin_amdgcn_raw_buffer_store_b32(e_, rws, 0, 0, AUX_SC1);
    }
    }
  }
}

extern "C" void kernel_launch(void* const* d_in, const int* in_sizes, int n_in, void* d_out, int out_size, void* d_ws, size_t ws_size,
                              hipStream_t stream) {
  Params p;
  p.x = (const u16*)d_in[0]; p.kc = (const u16*)d_in[1]; p.vc = (const u16*)d_in[2]; p.wq = (const u16*)d_in[3]; p.wo = (const u16*)d_in[4];
  p.wg = (const u16*)d_in[6]; p.wu = (const u16*)d_in[7]; p.wd = (const u16*)d_in[8];
  p.out = (u16*)d_out; p.ws = (char*)d_ws;
  fused_kernel<<<NWG, NT, 0, stream>>>(p);
}
```
